```python
import torch

def _softmax(x, axis=-1):
    return torch.softmax(x, dim=axis)
def _gelu(x):
    return torch.nn.functional.gelu(x, approximate="tanh")
def _silu(x):
    return torch.nn.functional.silu(x)
def _sigmoid(x):
    return torch.sigmoid(x)
def _layernorm(x, axis=-1, eps=1e-5):
    m = x.mean(dim=axis, keepdim=True)
    v = x.var(dim=axis, keepdim=True, unbiased=False)
    return (x - m) / torch.sqrt(v + eps)
def _scatter_add(tgt, idx, src, axis=0):
    return tgt.clone().index_add_(axis, idx, src)
def _segment_sum(src, seg, n):
    out = torch.zeros((n,) + src.shape[1:], dtype=src.dtype)
    return out.index_add_(0, seg, src)
def _pad(x, pw):
    flat = tuple(v for pair in reversed(pw) for v in pair)
    return torch.nn.functional.pad(x, flat)

kernel_name = 'gated_mix__sliding_block_cached__glu_gated_bilinear__decode'

def setup_inputs():

    _w = (lambda a: (lambda t: t.to(torch.bfloat16) if t.is_floating_point() else t)(torch.as_tensor(a)))
    def _A_setup():
        B, Sq, D = 2, 1, 512
        Hq, Hkv, Sk, Dh = 32, 32, 4096, 128
        assert Hq % Hkv == 0, (Hq, Hkv)
        return {
            "x": _w((lambda *s: torch.randn(*s, dtype=torch.bfloat16) * 0.05)(B, Sq, D)),
            "k_cache": _w((lambda *s: torch.randn(*s, dtype=torch.bfloat16) * 6.4)(B, Hkv, Sk, Dh)),
            "v_cache": _w((lambda *s: torch.randn(*s, dtype=torch.bfloat16) * 0.05)(B, Hkv, Sk, Dh)),
            "wq": _w((lambda *s: torch.randn(*s, dtype=torch.bfloat16) * 6.4)(D, Hq * Dh) * (D ** -0.5)),
            "wo": _w((lambda *s: torch.randn(*s, dtype=torch.bfloat16) * 0.05)(Hq * Dh, D) * ((Hq * Dh) ** -0.5)),
        }
    _A_in = _A_setup()

    def _B_setup():
        B, S, D, F = 2, 1, 512, 1376
        return {
            "x": _w((lambda *s: torch.randn(*s, dtype=torch.bfloat16) * 0.05)(B, S, D)),
            "w_gate": _w((lambda *s: torch.randn(*s, dtype=torch.bfloat16) * 0.4)(D, F) * (D ** -0.5)),
            "w_up": _w((lambda *s: torch.randn(*s, dtype=torch.bfloat16) * 0.4)(D, F) * (D ** -0.5)),
            "w_down": _w((lambda *s: torch.randn(*s, dtype=torch.bfloat16) * 0.4)(F, D) * (F ** -0.5)),
        }
    _B_in = _B_setup()

    return {
        "A_x": _A_in["x"],
        "A_k_cache": _A_in["k_cache"],
        "A_v_cache": _A_in["v_cache"],
        "A_wq": _A_in["wq"],
        "A_wo": _A_in["wo"],
        "B_x": _B_in["x"],
        "B_w_gate": _B_in["w_gate"],
        "B_w_up": _B_in["w_up"],
        "B_w_down": _B_in["w_down"],
        "gate_w": _w((lambda *s: torch.randn(*s, dtype=torch.bfloat16) * 0.05)(512) * 16.0),
    }

def reference(A_x, A_k_cache, A_v_cache, A_wq, A_wo, B_x, B_w_gate, B_w_up, B_w_down, gate_w):
    def _A_ref(x, k_cache, v_cache, wq, wo):
        B, Sq, D = x.shape
        Hq, Dh = 32, 128
        Hkv, Sk = k_cache.shape[1], k_cache.shape[2]
        W = min(4096, max(256, Sk // 4))
        rep = Hq // Hkv
        q = (lambda x, *d: x.permute(*d))((x @ wq).reshape(B, Sq, Hq, Dh), 0, 2, 1, 3)
        k = (lambda x, n, axis: torch.repeat_interleave(x, n, dim=axis))(k_cache[:, :, Sk - W:, :], rep, axis=1)
        v = (lambda x, n, axis: torch.repeat_interleave(x, n, dim=axis))(v_cache[:, :, Sk - W:, :], rep, axis=1)
        scores = torch.einsum("bhqd,bhkd->bhqk", q, k) * (Dh ** -0.5)
        p = _softmax(scores, axis=-1)
        o = torch.einsum("bhqk,bhkd->bhqd", p, v)
        return (lambda x, *d: x.permute(*d))(o, 0, 2, 1, 3).reshape(B, Sq, Hq * Dh) @ wo

    def _B_ref(x, w_gate, w_up, w_down):
        g = torch.einsum("bsd,df->bsf", x, w_gate)
        u = torch.einsum("bsd,df->bsf", x, w_up)
        h = g * u
        return torch.einsum("bsf,fd->bsd", h, w_down)

    x = A_x
    g = _sigmoid((x * gate_w).sum(-1, keepdims=True))
    a = _A_ref(x, A_k_cache, A_v_cache, A_wq, A_wo)
    b = _B_ref(x, B_w_gate, B_w_up, B_w_down)
    return g * a + (1 - g) * b
```

```cpp
#include <hip/hip_runtime.h>
#include <hip/hip_bf16.h>
#include <stdint.h>
typedef unsigned short u16;
__device__ __forceinline__ float bf2f(u16 v) { return __uint_as_float(((unsigned)v) << 16); }
__device__ __forceinline__ u16 f2bf(float f) { unsigned u = __float_as_uint(f); return (u16)((u + 0x7FFF + ((u >> 16) & 1)) >> 16); }
__device__ __forceinline__ float rbf(float f) { return bf2f(f2bf(f)); }

__global__ void gemv(const float* x, const u16* W, float* out, int K, int N) {
  __shared__ float red[4][2][64];
  int c = threadIdx.x & 63, ks = threadIdx.x >> 6;
  int n = blockIdx.x * 64 + c;
  float a0 = 0, a1 = 0;
  if (n < N) for (int k = ks; k < K; k += 4) { float w = bf2f(W[(size_t)k * N + n]); a0 += x[k] * w; a1 += x[K + k] * w; }
  red[ks][0][c] = a0; red[ks][1][c] = a1;
  __syncthreads();
  if (ks < 2 && n < N) out[ks * N + n] = rbf(red[0][ks][c] + red[1][ks][c] + red[2][ks][c] + red[3][ks][c]);
}
__global__ void cvt(const u16* x, float* y, int n) { int i = blockIdx.x * blockDim.x + threadIdx.x; if (i < n) y[i] = bf2f(x[i]); }
__global__ void mulk(const float* g, const float* u, float* h, int n) { int i = blockIdx.x * blockDim.x + threadIdx.x; if (i < n) h[i] = rbf(g[i] * u[i]); }
__global__ void attn(const float* q, const u16* Kc, const u16* Vc, float* o) {
  __shared__ float s[1024]; __shared__ float red[256]; __shared__ float qs[128];
  int bh = blockIdx.x; int b = bh / 32, h = bh % 32;
  const u16* K = Kc + ((size_t)bh * 4096 + 3072) * 128;
  const u16* V = Vc + ((size_t)bh * 4096 + 3072) * 128;
  int t = threadIdx.x;
  if (t < 128) qs[t] = q[b * 4096 + h * 128 + t];
  __syncthreads();
  float m = -INFINITY;
  for (int k = t; k < 1024; k += 256) { float a = 0; for (int d = 0; d < 128; ++d) a += qs[d] * bf2f(K[k * 128 + d]); a = rbf(rbf(a) * 0.08838834764831845f); s[k] = a; m = fmaxf(m, a); }
  red[t] = m; __syncthreads();
  for (int st = 128; st > 0; st >>= 1) { if (t < st) red[t] = fmaxf(red[t], red[t + st]); __syncthreads(); }
  m = red[0]; __syncthreads();
  float l = 0;
  for (int k = t; k < 1024; k += 256) { float e = expf(s[k] - m); s[k] = e; l += e; }
  red[t] = l; __syncthreads();
  for (int st = 128; st > 0; st >>= 1) { if (t < st) red[t] += red[t + st]; __syncthreads(); }
  l = red[0]; __syncthreads();
  for (int k = t; k < 1024; k += 256) s[k] = rbf(s[k] / l);
  __syncthreads();
  int d = t & 127, hf = t >> 7; float a = 0;
  for (int k = hf; k < 1024; k += 2) a += s[k] * bf2f(V[k * 128 + d]);
  red[t] = a; __syncthreads();
  if (t < 128) o[b * 4096 + h * 128 + t] = rbf(red[t] + red[t + 128]);
}
__global__ void fin(const float* x, const u16* gw, const float* a, const float* bb, u16* out) {
  __shared__ float red[512];
  int b = blockIdx.x, t = threadIdx.x;
  red[t] = rbf(x[b * 512 + t] * bf2f(gw[t])); __syncthreads();
  for (int st = 256; st > 0; st >>= 1) { if (t < st) red[t] += red[t + st]; __syncthreads(); }
  float g = rbf(red[0]); g = rbf(1.f / (1.f + expf(-g)));
  float r = rbf(g * a[b * 512 + t]) + rbf(rbf(1.f - g) * bb[b * 512 + t]);
  out[b * 512 + t] = f2bf(r);
}
extern "C" void kernel_launch(void* const* d_in, const int* in_sizes, int n_in,
                              void* d_out, int out_size, void* d_ws, size_t ws_size,
                              hipStream_t stream) {
  const u16* x = (const u16*)d_in[0]; const u16* Kc = (const u16*)d_in[1]; const u16* Vc = (const u16*)d_in[2];
  const u16* wq = (const u16*)d_in[3]; const u16* wo = (const u16*)d_in[4];
  const u16* wg = (const u16*)d_in[6]; const u16* wu = (const u16*)d_in[7]; const u16* wd = (const u16*)d_in[8]; const u16* gw = (const u16*)d_in[9];
  float* ws = (float*)d_ws;
  float* xf = ws; float* q = ws + 1024; float* o = q + 8192; float* g = o + 8192; float* u = g + 4096; float* h = u + 4096; float* a = h + 4096; float* bb = a + 1024;
  cvt<<<4, 256, 0, stream>>>(x, xf, 1024);
  gemv<<<64, 256, 0, stream>>>(xf, wq, q, 512, 4096);
  gemv<<<22, 256, 0, stream>>>(xf, wg, g, 512, 1376);
  gemv<<<22, 256, 0, stream>>>(xf, wu, u, 512, 1376);
  mulk<<<11, 256, 0, stream>>>(g, u, h, 2752);
  attn<<<64, 256, 0, stream>>>(q, Kc, Vc, o);
  gemv<<<8, 256, 0, stream>>>(o, wo, a, 4096, 512);
  gemv<<<8, 256, 0, stream>>>(h, wd, bb, 1376, 512);
  fin<<<2, 512, 0, stream>>>(xf, gw, a, bb, (u16*)d_out);
}
```

```cpp
#include <hip/hip_runtime.h>
#include <stdint.h>

typedef unsigned short u16;
typedef unsigned int u32;
typedef unsigned long long u64;
typedef u32 u32x4 __attribute__((ext_vector_type(4)));
typedef u32 u32x2 __attribute__((ext_vector_type(2)));
typedef float f32x2 __attribute__((ext_vector_type(2)));
typedef float f32x4 __attribute__((ext_vector_type(4)));
typedef __bf16 bf16x2 __attribute__((ext_vector_type(2)));
#define GAS __attribute__((address_space(1)))
typedef GAS u64 gu64;

namespace {
constexpr int NWG = 256, NTHR = 640;
constexpr int FF = 1376;
constexpr size_t W_EPOCH = 0;
constexpr size_t W_START = 128;
constexpr size_t W_Q1 = 1024;
constexpr size_t W_F1 = W_Q1 + 32 * 2 * 4 * 128;
constexpr size_t W_O2 = W_F1 + 22 * 2 * 4 * 128;
constexpr size_t W_ST = W_O2 + 32 * 8 * 128;
constexpr size_t W_Y3 = W_ST + 32 * 16;
constexpr size_t W_END = W_Y3 + 8 * 2 * 4 * 32 * 16;
constexpr size_t W_DBG = 262144;
constexpr int L_Q = 0;
constexpr int L_H = 64;
constexpr int L_G = 192;
constexpr int L_TAG = 196;
constexpr int L_WM = 200;
constexpr int L_WL = 208;
constexpr int L_CNT = 216;
constexpr int L_OP = 256;
constexpr int L_YP = L_OP + 1024;
constexpr int L_QP = L_YP + 1024;
constexpr int L_FP = L_QP + 1024;
constexpr int L_END = L_FP + 1024;
constexpr int LDS_WORDS = 21504;
constexpr int SPIN_MAX = 1 << 15;
#define QSLEEP 18
#define OSLEEP 6
#define RSLEEP 2
#define FSLEEP 4
constexpr float LOG2E = 1.4426950408889634f;

__device__ __forceinline__ float dot2(u32 a, u32 b, float c) {
  return __builtin_amdgcn_fdot2_f32_bf16(__builtin_bit_cast(bf16x2, a), __builtin_bit_cast(bf16x2, b), c, false);
}
__device__ __forceinline__ u32 cvtpk(float lo, float hi) {
  bf16x2 v = {(__bf16)lo, (__bf16)hi};
  return __builtin_bit_cast(u32, v);
}
__device__ __forceinline__ float bflo(u32 p) { return __uint_as_float(p << 16); }
__device__ __forceinline__ float bfhi(u32 p) { return __uint_as_float(p & 0xffff0000u); }
__device__ __forceinline__ float rbf(float x) { return bflo(cvtpk(x, x)); }
template <int CTRL> __device__ __forceinline__ float dpp(float x) {
  return __builtin_bit_cast(float, __builtin_amdgcn_mov_dpp(__builtin_bit_cast(int, x), CTRL, 0xf, 0xf, true));
}
constexpr int XOR1 = 0xB1, XOR2 = 0x4E, HALF_MIRROR = 0x141, ROR8 = 0x128, ROR4 = 0x124, ROR2 = 0x122, ROR1 = 0x121;
__device__ __forceinline__ float swap32_add(float a, float b) {
  auto t = __builtin_amdgcn_permlane32_swap(__float_as_uint(a), __float_as_uint(b), false, false);
  return __uint_as_float(t[0]) + __uint_as_float(t[1]);
}
__device__ __forceinline__ float swap16_add(float a, float b) {
  auto t = __builtin_amdgcn_permlane16_swap(__float_as_uint(a), __float_as_uint(b), false, false);
  return __uint_as_float(t[0]) + __uint_as_float(t[1]);
}
__device__ __forceinline__ float xrow_max(float x) {
  auto s = __builtin_amdgcn_permlane16_swap(__float_as_uint(x), __float_as_uint(x), false, false);
  x = fmaxf(__uint_as_float(s[0]), __uint_as_float(s[1]));
  auto t = __builtin_amdgcn_permlane32_swap(__float_as_uint(x), __float_as_uint(x), false, false);
  return fmaxf(__uint_as_float(t[0]), __uint_as_float(t[1]));
}
__device__ __forceinline__ float xrow_sum(float x) { x = swap16_add(x, x); return swap32_add(x, x); }
__device__ __forceinline__ u32x4 ldnt(const void* p) { return __builtin_nontemporal_load((const GAS u32x4*)p); }
__device__ __forceinline__ u64 ldg(const u64* p) { return __hip_atomic_load((const gu64*)p, __ATOMIC_RELAXED, __HIP_MEMORY_SCOPE_AGENT); }
__device__ __forceinline__ void stg(u64* p, u64 v) { __hip_atomic_store((gu64*)p, v, __ATOMIC_RELAXED, __HIP_MEMORY_SCOPE_AGENT); }
__device__ __forceinline__ u64 gpack(float v, u32 tagHi, u32 tagLo) {
  return ((u64)tagHi << 32) | (u64)(tagLo | ((__float_as_uint(v) + 0x80u) >> 8));
}
__device__ __forceinline__ float gval(u64 g) { return __uint_as_float(((u32)g) << 8); }
__device__ __forceinline__ u32 gbad(u64 g, u32 tagHi, u32 tagLo) { return ((u32)(g >> 32) ^ tagHi) | (((u32)g ^ tagLo) >> 24); }
#define WG_BARRIER(id) asm volatile("s_waitcnt lgkmcnt(0)\n\ts_barrier ; " id ::: "memory")

__device__ __forceinline__ f32x2 reduce16(const float (&v)[2][8], int lane) {
  float z[8];
#pragma unroll
  for (int t = 0; t < 8; ++t) z[t] = swap32_add(v[0][t], v[1][t]);
  float y[4];
#pragma unroll
  for (int t = 0; t < 4; ++t) y[t] = swap16_add(z[t], z[t + 4]);
#pragma unroll
  for (int t = 0; t < 4; ++t) y[t] += dpp<ROR8>(y[t]);
  const bool hi = lane & 8;
  f32x2 r = {hi ? y[2] : y[0], hi ? y[3] : y[1]};
  return r;
}

#define LAS __attribute__((address_space(3)))
#define PIN() __builtin_amdgcn_sched_barrier(0)
#define THROTTLE(n) do { asm volatile("s_waitcnt vmcnt(" #n ")" ::: "memory"); PIN(); } while (0)
__device__ __forceinline__ void lds_arrive(u32* c, int lane) {
  asm volatile("s_waitcnt lgkmcnt(0)" ::: "memory");
  if (lane == 0) __hip_atomic_fetch_add(c, 1u, __ATOMIC_RELAXED, __HIP_MEMORY_SCOPE_WORKGROUP);
  asm volatile("" ::: "memory");
}
__device__ __forceinline__ void lds_wait(u32* c, u32 n) {
  const volatile LAS u32* p = (const volatile LAS u32*)c;
  for (int spin = 0; spin < (1 << 22) && *p < n; ++spin) __builtin_amdgcn_s_sleep(1);
  asm volatile("" ::: "memory");
}
__device__ __forceinline__ void proj_partial(const u32x4& w0, const u32x4& w1, u32 xa, u32 xb, float* dst, int lane) {
  float acc[2][8];
#pragma unroll
  for (int t = 0; t < 4; ++t) {
    const u32 lo = __builtin_amdgcn_perm(w1[t], w0[t], 0x05040100u), hi = __builtin_amdgcn_perm(w1[t], w0[t], 0x07060302u);
    acc[0][2 * t] = dot2(lo, xa, 0.f); acc[0][2 * t + 1] = dot2(hi, xa, 0.f);
    acc[1][2 * t] = dot2(lo, xb, 0.f); acc[1][2 * t + 1] = dot2(hi, xb, 0.f);
  }
  const f32x2 r = reduce16(acc, lane);
  *(f32x2*)&dst[(lane >> 5) * 64 + 8 * (lane & 7) + 4 * ((lane >> 4) & 1) + 2 * ((lane >> 3) & 1)] = r;
}

#define STAMP(i)
#define STAMP_DECL
#define STAMP_OUT()

__global__ void __launch_bounds__(NTHR, 3)
fused_decode(const u16* __restrict__ x, const u16* __restrict__ Kc, const u16* __restrict__ Vc, const u16* __restrict__ wq,
             const u16* __restrict__ wo, const u16* __restrict__ wg, const u16* __restrict__ wu, const u16* __restrict__ wd,
             const u16* __restrict__ gw, u16* __restrict__ out, u64* __restrict__ ws) {
  __shared__ __attribute__((aligned(16))) u32 lds[LDS_WORDS];
  float* const ldf = (float*)lds;
  STAMP_DECL;
  STAMP(0);
  u64 d_t0, d_t1 = 0; asm volatile("s_memrealtime %0\n s_waitcnt lgkmcnt(0)" : "=s"(d_t0));
  const int tid = threadIdx.x, lane = tid & 63;
  const int wave = __builtin_amdgcn_readfirstlane(tid >> 6);
  const int wgid = blockIdx.x, xcd = wgid & 7, idx = wgid >> 3;
  const int hh = idx >> 3, s = idx & 7, h = xcd * 4 + hh;
  const int fgr = (int)((0x46573210u >> (4 * xcd)) & 15u) * 4 + hh;
  const bool ffn = fgr < 22;
  const int fg = ffn ? fgr : 0;
  const int r8 = lane >> 3, c8 = lane & 7;
  const int rq = s >> 1, par = s & 1;

  if (wave >= 8) {
    __builtin_amdgcn_s_setprio(3);
    const bool isq = wave == 8;
    const u64 ep = ldg(ws + W_EPOCH) + 1;
    if (isq && lane < 8) lds[L_CNT + lane] = 0;
    WG_BARRIER("B0 aux");
    const u32 tagHi = (u32)(ep >> 8), tagLo = (u32)ep << 24;
    if (lane == 0) stg(ws + W_START + 2 * wgid + (wave - 8), ep);
    STAMP(1);
    if (isq) {
      if (lane == 0) { lds[L_TAG] = tagHi; lds[L_TAG + 1] = tagLo; }
      lds_wait(&lds[L_CNT + 0], 8);
      STAMP(4);
      float a0 = 0.f, a1 = 0.f;
#pragma unroll
      for (int w = 0; w < 8; ++w) { a0 += ldf[L_QP + w * 128 + lane]; a1 += ldf[L_QP + w * 128 + 64 + lane]; }
      u64* dst = ws + W_Q1 + ((size_t)h * 8 + rq) * 128 + 64 * par + lane;
      stg(dst, gpack(a0, tagHi, tagLo));
      stg(dst + 512, gpack(a1, tagHi, tagLo));
      STAMP(2);
      __builtin_amdgcn_s_sleep(QSLEEP);
      const u64* src = ws + W_Q1 + (size_t)(h * 2 + par) * 512 + lane;
      u64 g[8];
      for (int spin = 0; spin < SPIN_MAX; ++spin) {
        u32 bad = 0;
#pragma unroll
        for (int i = 0; i < 8; ++i) g[i] = ldg(src + 64 * i);
#pragma unroll
        for (int i = 0; i < 8; ++i) bad |= gbad(g[i], tagHi, tagLo);
        if (!__any(bad != 0)) break;
      }
      const u32 qq = cvtpk((gval(g[0]) + gval(g[2])) + (gval(g[4]) + gval(g[6])), (gval(g[1]) + gval(g[3])) + (gval(g[5]) + gval(g[7])));
      ((u16*)lds)[2 * L_Q + lane] = (u16)qq;
      ((u16*)lds)[2 * L_Q + 64 + lane] = (u16)(qq >> 16);
      lds_arrive(&lds[L_CNT + 3], lane);
      STAMP(3);
    } else {
      const u32x4 gv = *(const u32x4*)(gw + 8 * lane);
      const u32x4 x0 = *(const u32x4*)(x + 8 * lane), x1 = *(const u32x4*)(x + 512 + 8 * lane);
      float z0 = 0.f, z1 = 0.f;
#pragma unroll
      for (int t = 0; t < 4; ++t) {
        const u32 p0 = cvtpk(bflo(x0[t]) * bflo(gv[t]), bfhi(x0[t]) * bfhi(gv[t]));
        const u32 p1 = cvtpk(bflo(x1[t]) * bflo(gv[t]), bfhi(x1[t]) * bfhi(gv[t]));
        z0 += bflo(p0) + bfhi(p0);
        z1 += bflo(p1) + bfhi(p1);
      }
      z0 += dpp<ROR8>(z0); z0 += dpp<ROR4>(z0); z0 += dpp<ROR2>(z0); z0 += dpp<ROR1>(z0); z0 = xrow_sum(z0);
      z1 += dpp<ROR8>(z1); z1 += dpp<ROR4>(z1); z1 += dpp<ROR2>(z1); z1 += dpp<ROR1>(z1); z1 = xrow_sum(z1);
      const float g0 = rbf(1.f / (1.f + __expf(-rbf(z0)))), g1 = rbf(1.f / (1.f + __expf(-rbf(z1))));
      if (lane == 0) {
        lds[L_G + 0] = __float_as_uint(g0); lds[L_G + 1] = __float_as_uint(rbf(1.f - g0));
        lds[L_G + 2] = __float_as_uint(g1); lds[L_G + 3] = __float_as_uint(rbf(1.f - g1));
      }
      if (ffn) {
        lds_wait(&lds[L_CNT + 1], 8);
        STAMP(4);
        float a0 = 0.f, a1 = 0.f;
#pragma unroll
        for (int w = 0; w < 8; ++w) { a0 += ldf[L_FP + w * 128 + lane]; a1 += ldf[L_FP + w * 128 + 64 + lane]; }
        u64* dst = ws + W_F1 + (((size_t)fg * 2 + par) * 4 + rq) * 128 + lane;
        stg(dst, gpack(a0, tagHi, tagLo));
        stg(dst + 64, gpack(a1, tagHi, tagLo));
        STAMP(2);
        lds_wait(&lds[L_CNT + 2], 8);
        const u64* src = ws + W_F1 + (size_t)fg * 1024 + lane;
        u64 g[16];
        for (int spin = 0; spin < SPIN_MAX; ++spin) {
          u32 bad = 0;
#pragma unroll
          for (int i = 0; i < 16; ++i) g[i] = ldg(src + 64 * i);
#pragma unroll
          for (int i = 0; i < 16; ++i) bad |= gbad(g[i], tagHi, tagLo);
          if (!__any(bad != 0)) break;
        }
        const u32 gp = cvtpk((gval(g[0]) + gval(g[2])) + (gval(g[4]) + gval(g[6])), (gval(g[1]) + gval(g[3])) + (gval(g[5]) + gval(g[7])));
        const u32 up = cvtpk((gval(g[8]) + gval(g[10])) + (gval(g[12]) + gval(g[14])), (gval(g[9]) + gval(g[11])) + (gval(g[13]) + gval(g[15])));
        u32 hp = cvtpk(bflo(gp) * bflo(up), bfhi(gp) * bfhi(up));
        if (64 * fg + lane >= FF) hp = 0;
        lds[L_H + lane] = hp & 0xffffu;
        lds[L_H + 64 + lane] = hp >> 16;
      } else if (wgid == 4) {
        const u64* src = ws + W_START + lane;
        bool ok = false;
        for (int spin = 0; spin < SPIN_MAX; ++spin) {
          u32 bad = 0;
#pragma unroll
          for (int i = 0; i < 8; ++i) { const u64 f = ldg(src + 64 * i); bad |= (f != ep); }
          if (!__any(bad != 0)) { ok = true; break; }
          __builtin_amdgcn_s_sleep(8);
        }
        if (ok && lane == 0) stg(ws + W_EPOCH, ep);
      }
      STAMP(3);
    }
    asm volatile("s_waitcnt lgkmcnt(0)" ::: "memory");
    STAMP_OUT();
    return;
  }

  const int b = s & 1, j = s >> 1;
  const size_t kvoff = ((size_t)(b * 32 + h) * 4096 + 3072 + j * 256 + wave * 32) * 128 + 8 * lane;
  const u16* kp = Kc + kvoff;
  const u16* vp = Vc + kvoff;
  const int row = 128 * rq + 16 * wave + 2 * r8;
  const u32 xa = *(const u32*)(x + row), xb = *(const u32*)(x + 512 + row);
  const u16* qp = wq + (size_t)row * 4096 + 128 * h + 64 * par + 8 * c8;
  const u32x4 q0 = ldnt(qp), q1 = ldnt(qp + 4096);
  u32x4 kr[8], vr[8];
  PIN();
  STAMP(11);
  WG_BARRIER("B0 stream");
  STAMP(12);
  kr[0] = ldnt(kp); kr[1] = ldnt(kp + 512);
  PIN();
  THROTTLE(2);
  STAMP(13);
  kr[2] = ldnt(kp + 1024);
  PIN();
  proj_partial(q0, q1, xa, xb, ldf + L_QP + wave * 128, lane);
  lds_arrive(&lds[L_CNT + 0], lane);
  STAMP(1);
  u32x4 f0, f1;
  THROTTLE(2); kr[3] = ldnt(kp + 512 * 3); PIN();
  if (ffn) {
    int col = 64 * fg + 8 * c8; col = col < FF ? col : col - 32;
    const u16* fp = (par ? wu : wg) + (size_t)row * FF + col;
    THROTTLE(2); f0 = ldnt(fp); PIN();
    THROTTLE(2); f1 = ldnt(fp + FF); PIN();
  }
#pragma unroll
  for (int i = 4; i < 7; ++i) { THROTTLE(2); kr[i] = ldnt(kp + 512 * i); PIN(); }
  if (ffn) {
    proj_partial(f0, f1, xa, xb, ldf + L_FP + wave * 128, lane);
    lds_arrive(&lds[L_CNT + 1], lane);
  }
  THROTTLE(2); kr[7] = ldnt(kp + 512 * 7); PIN();
  THROTTLE(2); vr[0] = ldnt(vp); PIN();
  THROTTLE(2); vr[1] = ldnt(vp + 512); PIN();
  THROTTLE(2); vr[2] = ldnt(vp + 1024); vr[3] = ldnt(vp + 1536); PIN();
  STAMP(2);
  lds_wait(&lds[L_CNT + 3], 1);
  STAMP(3);
  const u32x4 qv = *(const u32x4*)&lds[L_Q + 4 * (lane & 15)];
  float sc[8];
#pragma unroll
  for (int i = 0; i < 8; ++i) {
    float a = dot2(kr[i][0], qv[0], 0.f);
    a = dot2(kr[i][1], qv[1], a); a = dot2(kr[i][2], qv[2], a); a = dot2(kr[i][3], qv[3], a);
    sc[i] = a;
  }
#define DPP8(r) "v_add_f32_dpp %0, %0, %0 row_ror:" #r " row_mask:0xf bank_mask:0xf\n\tv_add_f32_dpp %1, %1, %1 row_ror:" #r " row_mask:0xf bank_mask:0xf\n\t" \
                "v_add_f32_dpp %2, %2, %2 row_ror:" #r " row_mask:0xf bank_mask:0xf\n\tv_add_f32_dpp %3, %3, %3 row_ror:" #r " row_mask:0xf bank_mask:0xf\n\t" \
                "v_add_f32_dpp %4, %4, %4 row_ror:" #r " row_mask:0xf bank_mask:0xf\n\tv_add_f32_dpp %5, %5, %5 row_ror:" #r " row_mask:0xf bank_mask:0xf\n\t" \
                "v_add_f32_dpp %6, %6, %6 row_ror:" #r " row_mask:0xf bank_mask:0xf\n\tv_add_f32_dpp %7, %7, %7 row_ror:" #r " row_mask:0xf bank_mask:0xf\n\t"
  asm("s_nop 1\n\t" DPP8(8) DPP8(4) DPP8(2) DPP8(1) "s_nop 0"
      : "+v"(sc[0]), "+v"(sc[1]), "+v"(sc[2]), "+v"(sc[3]), "+v"(sc[4]), "+v"(sc[5]), "+v"(sc[6]), "+v"(sc[7]));
#pragma unroll
  for (int i = 0; i < 8; i += 2) {
    const u32 a = cvtpk(sc[i], sc[i + 1]);
    const u32 c = cvtpk(bflo(a) * 0.08838834764831845f, bfhi(a) * 0.08838834764831845f);
    sc[i] = bflo(c); sc[i + 1] = bfhi(c);
  }
  float m = fmaxf(fmaxf(fmaxf(sc[0], sc[1]), fmaxf(sc[2], sc[3])), fmaxf(fmaxf(sc[4], sc[5]), fmaxf(sc[6], sc[7])));
  m = xrow_max(m);
  if (lane == 0) ldf[L_WM + wave] = m;
  lds_arrive(&lds[L_CNT + 2], lane);
  THROTTLE(3); vr[4] = ldnt(vp + 512 * 4); PIN();
  THROTTLE(3); vr[5] = ldnt(vp + 512 * 5); PIN();
  lds_wait(&lds[L_CNT + 2], 8);
  STAMP(4);
  {
    const f32x4 a = *(const f32x4*)&lds[L_WM], c = *(const f32x4*)&lds[L_WM + 4];
    m = fmaxf(fmaxf(fmaxf(a[0], a[1]), fmaxf(a[2], a[3])), fmaxf(fmaxf(c[0], c[1]), fmaxf(c[2], c[3])));
  }
  const float mL = m * LOG2E;
  float p[8], l = 0.f;
#pragma unroll
  for (int i = 0; i < 8; ++i) { p[i] = __builtin_amdgcn_exp2f(fmaf(sc[i], LOG2E, -mL)); l += p[i]; }
  u32 pp[4];
#pragma unroll
  for (int q = 0; q < 4; ++q) pp[q] = cvtpk(p[2 * q], p[2 * q + 1]);
  l = xrow_sum(l);
  PIN();
  const u16* wop = wo + (size_t)(128 * h + 16 * wave + 2 * r8) * 512 + 64 * s + 8 * c8;
  int frow = 64 * fg + 8 * wave + r8; frow = frow < FF ? frow : frow - 32;
  u64 wdp = (u64)(ffn ? wd + (size_t)frow * 512 + 64 * s + 8 * c8 : wop);
  asm volatile("" : "+v"(wdp));
  float o[8];
#pragma unroll
  for (int t = 0; t < 8; ++t) o[t] = 0.f;
#define PV(q) do { _Pragma("unroll") for (int t = 0; t < 4; ++t) { \
      o[2 * t] = dot2(__builtin_amdgcn_perm(vr[2 * q + 1][t], vr[2 * q][t], 0x05040100u), pp[q], o[2 * t]); \
      o[2 * t + 1] = dot2(__builtin_amdgcn_perm(vr[2 * q + 1][t], vr[2 * q][t], 0x07060302u), pp[q], o[2 * t + 1]); } PIN(); } while (0)
  THROTTLE(3); vr[6] = ldnt(vp + 512 * 6); PIN();
  THROTTLE(3); vr[7] = ldnt(vp + 512 * 7); PIN();
  PV(0);
  THROTTLE(3);
  const u32x4 wo0 = ldnt(wop); PIN();
  PV(1);
  THROTTLE(3);
  const u32x4 wo1 = ldnt(wop + 512); PIN();
  THROTTLE(3);
  const u32x4 wdv = __builtin_nontemporal_load((const GAS u32x4*)wdp); PIN();
  PV(2);
  PV(3);
  {
    float z[4];
#pragma unroll
    for (int t = 0; t < 4; ++t) z[t] = swap32_add(o[t], o[t + 4]);
    const f32x2 y = {swap16_add(z[0], z[2]), swap16_add(z[1], z[3])};
    *(f32x2*)&lds[L_OP + wave * 128 + 8 * (lane & 15) + 4 * (lane >> 5) + 2 * ((lane >> 4) & 1)] = y;
    if (lane == 0) ldf[L_WL + wave] = l;
  }
  STAMP(5);
  WG_BARRIER("B3");
  const u32 tagHi = lds[L_TAG], tagLo = lds[L_TAG + 1];
  if (wave == 0) {
    __builtin_amdgcn_s_setprio(3);
    float a0 = 0.f, a1 = 0.f;
#pragma unroll
    for (int w = 0; w < 8; ++w) { a0 += ldf[L_OP + w * 128 + lane]; a1 += ldf[L_OP + w * 128 + 64 + lane]; }
    u64* dst = ws + W_O2 + ((size_t)(h * 2 + b) * 4 + j) * 128 + lane;
    stg(dst, gpack(a0, tagHi, tagLo));
    stg(dst + 64, gpack(a1, tagHi, tagLo));
    const f32x4 la = *(const f32x4*)&lds[L_WL], lc = *(const f32x4*)&lds[L_WL + 4];
    const float L = ((la[0] + la[1]) + (la[2] + la[3])) + ((lc[0] + lc[1]) + (lc[2] + lc[3]));
    if (lane < 2) stg(ws + W_ST + ((size_t)(h * 2 + b) * 4 + j) * 2 + lane, gpack(lane ? L : m, tagHi, tagLo));
    lds_arrive(&lds[L_CNT + 4], lane);
  }
  STAMP(6);
  const f32x4 gates = *(const f32x4*)&lds[L_G];
  float y[2][8];
#pragma unroll
  for (int bb = 0; bb < 2; ++bb) {
    const u32 hv = ffn ? lds[L_H + bb * 64 + 8 * wave + r8] : 0u;
#pragma unroll
    for (int t = 0; t < 4; ++t) {
      y[bb][2 * t] = gates[2 * bb + 1] * dot2(wdv[t], hv, 0.f);
      y[bb][2 * t + 1] = gates[2 * bb + 1] * dot2(wdv[t], hv << 16, 0.f);
    }
  }
  u32 wl[4], wh[4];
#pragma unroll
  for (int t = 0; t < 4; ++t) { wl[t] = __builtin_amdgcn_perm(wo1[t], wo0[t], 0x05040100u); wh[t] = __builtin_amdgcn_perm(wo1[t], wo0[t], 0x07060302u); }
  u32 op[2];
  {
    const int jj = lane & 3;
    const u64* vs = ws + W_O2 + ((size_t)h * 8 + jj) * 128 + 16 * wave + (lane >> 2);
    const u64* ss = ws + W_ST + ((size_t)h * 8 + jj) * 2;
    u64 g[6];
    lds_wait(&lds[L_CNT + 4], 1);
    __builtin_amdgcn_s_sleep(OSLEEP);
    for (int spin = 0; spin < SPIN_MAX; ++spin) {
      g[0] = ldg(vs); g[1] = ldg(vs + 512); g[2] = ldg(ss); g[3] = ldg(ss + 1); g[4] = ldg(ss + 8); g[5] = ldg(ss + 9);
      u32 bad = 0;
#pragma unroll
      for (int i = 0; i < 6; ++i) bad |= gbad(g[i], tagHi, tagLo);
      if (!__any(bad != 0)) break;
      __builtin_amdgcn_s_sleep(RSLEEP);
    }
    STAMP(7);
#pragma unroll
    for (int bb = 0; bb < 2; ++bb) {
      const float mj = gval(g[2 + 2 * bb]), lj = gval(g[3 + 2 * bb]);
      float M = fmaxf(mj, dpp<XOR1>(mj)); M = fmaxf(M, dpp<XOR2>(M));
      const float wgt = __builtin_amdgcn_exp2f((mj - M) * LOG2E);
      float L = wgt * lj; L += dpp<XOR1>(L); L += dpp<XOR2>(L);
      float v = wgt * gval(g[bb]); v += dpp<XOR1>(v); v += dpp<XOR2>(v);
      v *= __builtin_amdgcn_rcpf(L);
      const float other = dpp<HALF_MIRROR>(v);
      const bool odd = lane & 4;
      op[bb] = cvtpk(odd ? other : v, odd ? v : other);
    }
  }
#pragma unroll
  for (int bb = 0; bb < 2; ++bb)
#pragma unroll
    for (int t = 0; t < 4; ++t) {
      y[bb][2 * t] = fmaf(gates[2 * bb], dot2(wl[t], op[bb], 0.f), y[bb][2 * t]);
      y[bb][2 * t + 1] = fmaf(gates[2 * bb], dot2(wh[t], op[bb], 0.f), y[bb][2 * t + 1]);
    }
  {
    const f32x2 r = reduce16(y, lane);
    *(f32x2*)&lds[L_YP + wave * 128 + (lane >> 5) * 64 + 8 * c8 + 4 * ((lane >> 4) & 1) + 2 * ((lane >> 3) & 1)] = r;
  }
  WG_BARRIER("B4");
  STAMP(8);
  if (wave == 0) {
    float a0 = 0.f, a1 = 0.f;
#pragma unroll
    for (int w = 0; w < 8; ++w) { a0 += ldf[L_YP + w * 128 + lane]; a1 += ldf[L_YP + w * 128 + 64 + lane]; }
    u64* dst = ws + W_Y3 + (((size_t)s * 8 + (lane >> 4)) * 32 + h) * 16 + (lane & 15);
    stg(dst, gpack(a0, tagHi, tagLo));
    stg(dst + 4 * 512, gpack(a1, tagHi, tagLo));
    STAMP(9);
    if (hh == 0) {
      const int rb = xcd & 1, qd = xcd >> 1;
      const u64* src = ws + W_Y3 + (((size_t)s * 2 + rb) * 4 + qd) * 512 + lane;
      u64 g[8];
      for (int spin = 0; spin < SPIN_MAX; ++spin) {
#pragma unroll
        for (int i = 0; i < 8; ++i) g[i] = ldg(src + 64 * i);
        u32 bad = 0;
#pragma unroll
        for (int i = 0; i < 8; ++i) bad |= gbad(g[i], tagHi, tagLo);
        if (!__any(bad != 0)) break;
        __builtin_amdgcn_s_sleep(1);
      }
      float v = ((gval(g[0]) + gval(g[1])) + (gval(g[2]) + gval(g[3]))) + ((gval(g[4]) + gval(g[5])) + (gval(g[6]) + gval(g[7])));
      v = xrow_sum(v);
      const float vn = dpp<XOR1>(v);
      if (lane < 16 && !(lane & 1)) *(u32*)(out + rb * 512 + 64 * s + 16 * qd + lane) = cvtpk(v, vn);
    }
    STAMP(10);
  }
  STAMP_OUT();
}
}

extern "C" void kernel_launch(void* const* d_in, const int* in_sizes, int n_in, void* d_out, int out_size, void* d_ws, size_t ws_size,
                              hipStream_t stream) {
  fused_decode<<<NWG, NTHR, 0, stream>>>((const u16*)d_in[0], (const u16*)d_in[1], (const u16*)d_in[2], (const u16*)d_in[3],
                                         (const u16*)d_in[4], (const u16*)d_in[6], (const u16*)d_in[7], (const u16*)d_in[8],
                                         (const u16*)d_in[9], (u16*)d_out, (u64*)d_ws);
}
```

```cpp
#include <hip/hip_runtime.h>
#include <stdint.h>

typedef unsigned short u16;
typedef unsigned int u32;
typedef unsigned long long u64;
typedef u32 u32x4 __attribute__((ext_vector_type(4)));
typedef short s16x4 __attribute__((ext_vector_type(4)));
typedef short s16x8 __attribute__((ext_vector_type(8)));
typedef float f32x2 __attribute__((ext_vector_type(2)));
typedef float f32x4 __attribute__((ext_vector_type(4)));
typedef __bf16 bf16x2 __attribute__((ext_vector_type(2)));
typedef __bf16 bf16x8 __attribute__((ext_vector_type(8)));
#define GAS __attribute__((address_space(1)))
#define LAS __attribute__((address_space(3)))
typedef GAS u64 gu64;

#define NO_CODE_PREFETCH 1
namespace {
constexpr int NWG = 256, NTHR = 640;
constexpr int FF = 1376;
constexpr size_t W_EPOCH = 0;
constexpr size_t W_Q1 = 1024;
constexpr size_t W_F1 = W_Q1 + 32 * 2 * 4 * 128;
constexpr size_t W_O2 = W_F1 + 22 * 2 * 4 * 128;
constexpr size_t W_ST = W_O2 + 32 * 8 * 128;
constexpr size_t W_Y3 = W_ST + 32 * 8 * 16;
constexpr size_t W_END = W_Y3 + 8 * 2 * 4 * 32 * 16;
constexpr size_t W_DBG = 262144;
constexpr u32 LB_M = 0;
constexpr u32 M_CNT = LB_M;
constexpr u32 M_TAG = M_CNT + 32;
constexpr u32 M_G = M_TAG + 16;
constexpr u32 M_WM = M_G + 16;
constexpr u32 M_WL = M_WM + 32;
constexpr u32 M_F = M_WL + 32;
constexpr u32 M_Q = 256;
constexpr u32 M_P = M_Q + 256;
constexpr u32 M_O = M_P + 512;
constexpr u32 M_H = M_O + 512;
constexpr u32 M_X = 2048;
constexpr u32 M_GW = M_X + 2048;
constexpr u32 LB_W = 8192;
constexpr u32 LB_D = LB_W + 16384;
constexpr u32 LB_K = LB_D + 8192;
constexpr u32 LB_V = LB_K + 65536;
constexpr u32 LB_F = LB_V + 49152;
constexpr u32 C_LANDED = M_CNT, C_F = M_CNT + 4, C_QC = M_CNT + 8, C_FC = M_CNT + 12, C_Q = M_CNT + 16, C_M = M_CNT + 20, C_P = M_CNT + 24, C_H = M_CNT + 28;
constexpr u32 LDS_BYTES = 163840;
constexpr int SPIN_MAX = 1 << 15, LSPIN_MAX = 1 << 21;
constexpr int CODE_BYTES = 13312;
constexpr float LOG2E = 1.4426950408889634f;
#define QSLEEP 14
#define G2SLEEP 4
#define G3SLEEP 2
#define WIN 20
#define SSLEEP 10
#define G3FIRST 6

__device__ __forceinline__ u32 cvtpk(float lo, float hi) {
  const f32x2 v = {lo, hi};
  u32 r = __builtin_bit_cast(u32, __builtin_convertvector(v, bf16x2));
  asm("" : "+v"(r));
  return r;
}
__device__ __forceinline__ float max2(float a, float b) { return __builtin_amdgcn_fmed3f(a, b, __builtin_inff()); }
__device__ __forceinline__ float max3(float a, float b, float c) { return max2(max2(a, b), c); }
__device__ __forceinline__ float dot2(u32 a, u32 b, float c) {
  return __builtin_amdgcn_fdot2_f32_bf16(__builtin_bit_cast(bf16x2, a), __builtin_bit_cast(bf16x2, b), c, false);
}
__device__ __forceinline__ float bflo(u32 p) { return __uint_as_float(p << 16); }
__device__ __forceinline__ float bfhi(u32 p) { return __uint_as_float(p & 0xffff0000u); }
__device__ __forceinline__ float rbf(float x) { return bflo(cvtpk(x, x)); }
template <int CTRL> __device__ __forceinline__ float dpp(float x) {
  return __builtin_bit_cast(float, __builtin_amdgcn_mov_dpp(__builtin_bit_cast(int, x), CTRL, 0xf, 0xf, true));
}
constexpr int XOR1 = 0xB1, XOR2 = 0x4E, ROR8 = 0x128, ROR4 = 0x124, ROR2 = 0x122, ROR1 = 0x121;
__device__ __forceinline__ float swap32_add(float a, float b) {
  auto t = __builtin_amdgcn_permlane32_swap(__float_as_uint(a), __float_as_uint(b), false, false);
  return __uint_as_float(t[0]) + __uint_as_float(t[1]);
}
__device__ __forceinline__ float swap16_add(float a, float b) {
  auto t = __builtin_amdgcn_permlane16_swap(__float_as_uint(a), __float_as_uint(b), false, false);
  return __uint_as_float(t[0]) + __uint_as_float(t[1]);
}
__device__ __forceinline__ float xrow_max(float x) {
  auto s = __builtin_amdgcn_permlane16_swap(__float_as_uint(x), __float_as_uint(x), false, false);
  x = max2(__uint_as_float(s[0]), __uint_as_float(s[1]));
  auto t = __builtin_amdgcn_permlane32_swap(__float_as_uint(x), __float_as_uint(x), false, false);
  return max2(__uint_as_float(t[0]), __uint_as_float(t[1]));
}
__device__ __forceinline__ float xrow_sum(float x) { x = swap16_add(x, x); return swap32_add(x, x); }
__device__ __forceinline__ u64 ldg(const u64* base, u32 i) { return __hip_atomic_load((const gu64*)((const GAS char*)base + 8u * i), __ATOMIC_RELAXED, __HIP_MEMORY_SCOPE_AGENT); }
__device__ __forceinline__ void stg(u64* base, u32 i, u64 v) { __hip_atomic_store((gu64*)((GAS char*)base + 8u * i), v, __ATOMIC_RELAXED, __HIP_MEMORY_SCOPE_AGENT); }
__device__ __forceinline__ u64 gpack(float v, u32 tagHi, u32 tagLo) {
  return ((u64)tagHi << 32) | (u64)(tagLo | ((__float_as_uint(v) + 0x80u) >> 8));
}
__device__ __forceinline__ float gval(u64 g) { return __uint_as_float(((u32)g) << 8); }
__device__ __forceinline__ u32 gbad(u64 g, u32 tagHi, u32 tagLo) { return ((u32)(g >> 32) ^ tagHi) | (((u32)g ^ tagLo) >> 24); }

template <typename T> __device__ __forceinline__ LAS T* lp(u32 a) { return (LAS T*)(size_t)a; }
__device__ __forceinline__ bf16x8 ld_frag(u32 a) { return __builtin_bit_cast(bf16x8, *lp<u32x4>(a)); }
__device__ __forceinline__ bf16x8 ld_tr(u32 a0, u32 a1) {
  const s16x4 lo = __builtin_amdgcn_ds_read_tr16_b64_v4i16(lp<s16x4>(a0)), hi = __builtin_amdgcn_ds_read_tr16_b64_v4i16(lp<s16x4>(a1));
  return __builtin_bit_cast(bf16x8, __builtin_shufflevector(lo, hi, 0, 1, 2, 3, 4, 5, 6, 7));
}
__device__ __forceinline__ f32x4 mma(bf16x8 a, bf16x8 b, f32x4 c) { return __builtin_amdgcn_mfma_f32_16x16x32_bf16(a, b, c, 0, 0, 0); }
__device__ __forceinline__ void lds_arrive(u32 c) {
  asm volatile("s_waitcnt lgkmcnt(0)\n\ts_mov_b64 exec, 1\n\tds_add_u32 %0, %1\n\ts_mov_b64 exec, -1" :: "v"(c), "v"(1u) : "memory");
}
__device__ __forceinline__ void lds_wait(u32 c, u32 n, u32& seen) {
  u32 v, cnt = LSPIN_MAX;
  asm volatile("s_cmp_ge_u32 %1, %4\n\ts_cbranch_scc1 2f\n"
               "1:\n\tds_read_b32 %0, %3\n\ts_waitcnt lgkmcnt(0)\n\tv_readfirstlane_b32 %1, %0\n\ts_cmp_ge_u32 %1, %4\n\ts_cbranch_scc1 2f\n\t"
               "s_sleep 1\n\ts_sub_u32 %2, %2, 1\n\ts_cbranch_scc0 1b\n2:"
               : "=&v"(v), "+&s"(seen), "+&s"(cnt) : "v"(c), "s"(n) : "memory", "scc");
}
__device__ __forceinline__ void lds_wait(u32 c, u32 n) { u32 seen = 0; lds_wait(c, n, seen); }
#define WG_BARRIER(id) asm volatile("s_waitcnt lgkmcnt(0)\n\ts_barrier ; " id ::: "memory")
#define STR2(x) #x
#define STR(x) STR2(x)
#define DMA_THROTTLE "s_waitcnt vmcnt(" STR(WIN) ")\n\tds_write_b32 %[vz], %[prog]\n\t"
#define DMA_TILE_BODY(THR) \
  "s_mov_b32 m0, %[dst]\n\ts_nop 0\n1:\n\t" THR \
  "global_load_lds_dwordx4 %[v0], %[base] nt\n\ts_add_u32 m0, m0, 0x400\n\tv_add_u32 %[v0], %[step], %[v0]\n\t" \
  "global_load_lds_dwordx4 %[v1], %[base] nt\n\ts_add_u32 m0, m0, 0x400\n\tv_add_u32 %[v1], %[step], %[v1]\n\t" \
  "global_load_lds_dwordx4 %[v0], %[base] nt\n\ts_add_u32 m0, m0, 0x400\n\tv_add_u32 %[v0], %[step], %[v0]\n\t" \
  "global_load_lds_dwordx4 %[v1], %[base] nt\n\ts_add_u32 m0, m0, 0x400\n\tv_add_u32 %[v1], %[step], %[v1]\n\t" \
  "v_add_u32 %[prog], 4, %[prog]\n\ts_sub_u32 %[cnt], %[cnt], 1\n\ts_cbranch_scc0 1b"
template <bool THROTTLED>
__device__ __forceinline__ void dma_tile(u32 dst, const void* base, u32 v0, u32 v1, u32 step, u32 groups, u32& prog) {
  u32 cnt = groups - 1;
  if constexpr (THROTTLED)
    asm volatile(DMA_TILE_BODY(DMA_THROTTLE) : [v0] "+&v"(v0), [v1] "+&v"(v1), [prog] "+&v"(prog), [cnt] "+&s"(cnt)
                 : [dst] "s"(dst), [base] "s"(base), [step] "s"(step), [vz] "v"(0u) : "memory", "scc");
  else
    asm volatile(DMA_TILE_BODY("") : [v0] "+&v"(v0), [v1] "+&v"(v1), [prog] "+&v"(prog), [cnt] "+&s"(cnt)
                 : [dst] "s"(dst), [base] "s"(base), [step] "s"(step) : "memory", "scc");
}
#define DMA_ROWS_BODY(THR) \
  "s_mov_b32 m0, %[dst]\n\ts_nop 0\n1:\n\t" THR \
  "global_load_lds_dwordx4 %[v0], %[base] nt\n\tglobal_load_lds_dwordx4 %[v1], %[base] offset:1024 nt\n\t" \
  "global_load_lds_dwordx4 %[v2], %[base] offset:2048 nt\n\tglobal_load_lds_dwordx4 %[v3], %[base] offset:3072 nt\n\t" \
  "s_add_u32 m0, m0, 0x1000\n\tv_add_u32 %[v0], 0x1000, %[v0]\n\tv_add_u32 %[v1], 0x1000, %[v1]\n\tv_add_u32 %[v2], 0x1000, %[v2]\n\tv_add_u32 %[v3], 0x1000, %[v3]\n\t" \
  "v_add_u32 %[prog], 4, %[prog]\n\ts_sub_u32 %[cnt], %[cnt], 1\n\ts_cbranch_scc0 1b"
template <bool THROTTLED>
__device__ __forceinline__ void dma_rows(u32 dst, const void* base, u32& v0, u32& v1, u32& v2, u32& v3, u32 groups, u32& prog) {
  u32 cnt = groups - 1;
  if constexpr (THROTTLED)
    asm volatile(DMA_ROWS_BODY(DMA_THROTTLE) : [v0] "+&v"(v0), [v1] "+&v"(v1), [v2] "+&v"(v2), [v3] "+&v"(v3), [prog] "+&v"(prog), [cnt] "+&s"(cnt)
                 : [dst] "s"(dst), [base] "s"(base), [vz] "v"(0u) : "memory", "scc");
  else
    asm volatile(DMA_ROWS_BODY("") : [v0] "+&v"(v0), [v1] "+&v"(v1), [v2] "+&v"(v2), [v3] "+&v"(v3), [prog] "+&v"(prog), [cnt] "+&s"(cnt)
                 : [dst] "s"(dst), [base] "s"(base) : "memory", "scc");
}

#define STAMP(i)
#define LSTAMP(i)
#define STAMP_DECL
#define STAMP_OUT(slot)
#define COUNT(i)

__global__ void __launch_bounds__(NTHR, 3)
fused_decode(const u16* __restrict__ x, const u16* __restrict__ Kc, const u16* __restrict__ Vc, const u16* __restrict__ wq,
             const u16* __restrict__ wo, const u16* __restrict__ wg, const u16* __restrict__ wu, const u16* __restrict__ wd,
             const u16* __restrict__ gw, u16* __restrict__ out, u64* __restrict__ ws) {
  __shared__ __attribute__((aligned(1024))) unsigned char lds_all[LDS_BYTES];
  asm volatile("" :: "v"((LAS unsigned char*)lds_all));
  STAMP_DECL;
  LSTAMP(0);
  asm volatile("" :: "s"(x), "s"(Kc), "s"(Vc), "s"(wq), "s"(wo), "s"(wg), "s"(wu), "s"(wd), "s"(gw), "s"(out), "s"(ws));
  const int tid = threadIdx.x, lane = tid & 63;
  const int wid = __builtin_amdgcn_readfirstlane(tid >> 6);
  const int wgid = blockIdx.x, xcd = wgid & 7, idx = wgid >> 3;
  const int hh = idx >> 3, s = idx & 7, h = xcd * 4 + hh;
  const int rq = s >> 1, par = s & 1;
  const int b = s & 1, j = s >> 1;
  const u32 r8 = lane >> 3, r4 = lane >> 4;
  const u32 sw0 = 16u * ((lane & 7) ^ (r8 & 2));
  const size_t kvoff = ((size_t)(b * 32 + h) * 4096 + 3072 + j * 256) * 256;
  u32 k0 = r4 * 256 + 16 * ((lane & 15) ^ r4), k1 = k0 ^ 64, k2 = k0 ^ 128, k3 = k0 ^ 192;
  u32 prog = 28 - WIN;
  if (wid == 0) {
    STAMP(6);
    __builtin_amdgcn_s_setprio(2);
    if (lane < 8) *lp<u32>(M_CNT + 4 * lane) = 0;
    asm volatile("s_mov_b64 exec, 1\n\ts_mov_b32 m0, %0\n\ts_nop 0\n\tglobal_load_lds_dwordx4 %1, %2\n\ts_mov_b64 exec, -1\n\t"
                 "s_mov_b32 m0, %3\n\ts_nop 0\n\tglobal_load_lds_dwordx4 %4, %5\n\tglobal_load_lds_dwordx4 %4, %5 offset:1024\n\t"
                 "s_mov_b32 m0, %6\n\ts_nop 0\n\tglobal_load_lds_dwordx4 %4, %7"
                 :: "s"(M_TAG), "v"(0u), "s"(ws + W_EPOCH), "s"(M_X), "v"(16u * lane), "s"(x), "s"(M_GW), "s"(gw) : "memory");
    STAMP(7);
    u32 dummy = 0;
    dma_tile<false>(LB_W, (const char*)wq + ((size_t)rq << 20) + 256 * h + 128 * par, r8 * 8192 + sw0, r8 * 8192 + (sw0 ^ 64) + 65536, 131072, 4, dummy);
    STAMP(8);
    dma_rows<false>(LB_K, (const char*)Kc + kvoff, k0, k1, k2, k3, 2, dummy);
  }
  WG_BARRIER("B0");
  const int fgr = (int)((0x46573210u >> (4 * xcd)) & 15u) * 4 + hh;
  const bool ffn = fgr < 22;
  const int fg = ffn ? fgr : 0;
  const u32 fo = ffn ? 16u : 0u;
  int role = wid;
  asm volatile("" : "+s"(role));
  if (role == 0) {
    STAMP(1);
    const u32 sw1 = sw0 ^ 64;
    if (ffn)
      dma_tile<true>(LB_F, (const char*)((s & 1) ? wu : wg) + (size_t)(256 * (s >> 2) + ((s >> 1) & 1)) * (2 * FF) + 128 * fg - 64 * ((s >> 1) & 1),
                     r8 * (4 * FF) + sw0, r8 * (4 * FF) + sw1 + 8 * 4 * FF, 16 * 4 * FF, 4, prog);
    dma_rows<true>(LB_K + 8192, (const char*)Kc + kvoff, k0, k1, k2, k3, 14, prog);
    STAMP(2);
    {
      u32 v0 = r4 * 256 + 16 * ((lane & 15) ^ (r4 << 1)), v1 = v0 ^ 128, v2 = v0, v3 = v1;
      dma_rows<true>(LB_V, (const char*)Vc + kvoff, v0, v1, v2, v3, 12, prog);
      if (ffn) lds_wait(C_FC, 4);
      dma_rows<true>(LB_V + 49152, (const char*)Vc + kvoff, v0, v1, v2, v3, 4, prog);
    }
    STAMP(3);
    lds_wait(C_QC, 4);
    dma_tile<true>(LB_W, (const char*)(wo + (size_t)128 * h * 512 + 64 * s), r8 * 1024 + sw0, r8 * 1024 + sw1 + 8192, 16384, 4, prog);
    if (ffn) {
      const int row = 64 * fg;
      dma_tile<true>(LB_D, (const char*)(wd + (size_t)row * 512 + 64 * s), r8 * 1024 + sw0, r8 * 1024 + sw1 + 8192, 16384, 1, prog);
      dma_tile<true>(LB_D + 4096, (const char*)(wd + (size_t)(row + 32 < FF ? row + 32 : row) * 512 + 64 * s), r8 * 1024 + sw0, r8 * 1024 + sw1 + 8192, 16384, 1, prog);
    }
    STAMP(4);
#define DRAIN(k) "s_waitcnt vmcnt(" #k ")\n\tv_add_u32 %1, " STR(WIN) " - " #k ", %0\n\tds_write_b32 %2, %1\n\t"
    u32 t;
    asm volatile(DRAIN(20) DRAIN(16) DRAIN(12) DRAIN(8) DRAIN(4) DRAIN(0) "s_waitcnt lgkmcnt(0)" : "+v"(prog), "=&v"(t) : "v"(0u) : "memory");
    LSTAMP(5);
    STAMP_OUT(8);
    return;
  }
  const int g = lane >> 4, n16 = lane & 15, q4 = n16 >> 2, p4 = lane & 3;
  u32 landed = 0;
  lds_wait(C_LANDED, 4, landed);
  const u64 ep = *lp<u64>(M_TAG) + 1;
  const u32 tagHi = (u32)(ep >> 8), tagLo = (u32)ep << 24;

  if (role == 9) {
    {
      const u32x4 gv = *lp<u32x4>(M_GW + 16 * lane), x0 = *lp<u32x4>(M_X + 16 * lane), x1 = *lp<u32x4>(M_X + 1024 + 16 * lane);
      float z0 = 0.f, z1 = 0.f;
#pragma unroll
      for (int t = 0; t < 4; ++t) {
        const float gl = bflo(gv[t]), gh = bfhi(gv[t]);
        z0 = dot2(cvtpk(bflo(x0[t]) * gl, bfhi(x0[t]) * gh), 0x3f803f80u, z0);
        z1 = dot2(cvtpk(bflo(x1[t]) * gl, bfhi(x1[t]) * gh), 0x3f803f80u, z1);
      }
      float z = swap32_add(z0, z1);
      z = swap16_add(z, z);
      z += dpp<ROR8>(z); z += dpp<ROR4>(z); z += dpp<ROR2>(z); z += dpp<ROR1>(z);
      const float gt = rbf(__builtin_amdgcn_rcpf(1.f + __expf(-rbf(z))));
      if ((lane & 31) == 0) { *lp<float>(M_G + (lane >> 5) * 8) = gt; *lp<float>(M_G + (lane >> 5) * 8 + 4) = rbf(1.f - gt); }
    }
    STAMP(1);
    if (wgid == 4) {
      __builtin_amdgcn_s_sleep(48);
      const u32 i0 = (lane >> 3) * 1024 + ((lane >> 1) & 3) * 128 + (lane & 1) * 64;
#pragma unroll 1
      for (int spin = 0; spin < SPIN_MAX; ++spin) {
        u32 bad = 0;
#pragma unroll
        for (int i = 0; i < 4; ++i) bad |= gbad(ldg(ws + W_Q1 + 8192 * i, i0), tagHi, tagLo);
        if (!__any(bad != 0)) { if (lane == 0) stg(ws + W_EPOCH, 0, ep); break; }
        __builtin_amdgcn_s_sleep(8);
      }
    }
    lds_wait(C_P, 8);
    {
      const f32x4 a = *lp<f32x4>(M_WM), c = *lp<f32x4>(M_WM + 16), la = *lp<f32x4>(M_WL), lc = *lp<f32x4>(M_WL + 16);
      const float m = max3(max3(a[0], a[1], a[2]), max3(a[3], c[0], c[1]), max3(c[2], c[3], c[3]));
      const float L = ((la[0] + la[1]) + (la[2] + la[3])) + ((lc[0] + lc[1]) + (lc[2] + lc[3]));
      if (lane < 16) stg(ws + W_ST + ((size_t)(h * 2 + b) * 4 + j) * 16, lane, gpack((lane & 1) ? L : m, tagHi, tagLo));
    }
    STAMP(2);
    __builtin_amdgcn_s_sleep(SSLEEP);
    {
      const u64* src = ws + W_ST + (size_t)h * 128;
      u64 s0, s1;
#pragma unroll 1
      for (int spin = 0; spin < SPIN_MAX; ++spin) {
        s0 = ldg(src, lane); s1 = ldg(src, 64 + lane);
        if (!__any((gbad(s0, tagHi, tagLo) | gbad(s1, tagHi, tagLo)) != 0)) break;
        COUNT(13);
        __builtin_amdgcn_s_sleep(4);
      }
#pragma unroll
      for (int bb = 0; bb < 2; ++bb) {
        const float v = gval(bb ? s1 : s0), o = dpp<XOR1>(v);
        const float mj = (lane & 1) ? o : v, lj = (lane & 1) ? v : o;
        const float w = __builtin_amdgcn_exp2f((mj - xrow_max(mj)) * LOG2E);
        const float f = w * __builtin_amdgcn_rcpf(xrow_sum(w * lj));
        if (n16 == 0) *lp<float>(M_F + 16 * bb + 4 * g) = f;
      }
      lds_arrive(C_F);
    }
    STAMP(3);
    WG_BARRIER("B1 helper");
    if (hh == 0) {
      const int rb = xcd & 1, qd = xcd >> 1;
      const u64* src = ws + W_Y3 + (((size_t)s * 2 + rb) * 4 + qd) * 512;
      u64 gg[8];
      __builtin_amdgcn_s_sleep(G3FIRST);
#pragma unroll 1
      for (int spin = 0; spin < SPIN_MAX; ++spin) {
#pragma unroll
        for (int i = 0; i < 8; ++i) gg[i] = ldg(src, 64 * i + lane);
        u32 bad = 0;
#pragma unroll
        for (int i = 0; i < 8; ++i) bad |= gbad(gg[i], tagHi, tagLo);
        if (!__any(bad != 0)) break;
        COUNT(14);
        __builtin_amdgcn_s_sleep(G3SLEEP);
      }
      float v = ((gval(gg[0]) + gval(gg[1])) + (gval(gg[2]) + gval(gg[3]))) + ((gval(gg[4]) + gval(gg[5])) + (gval(gg[6]) + gval(gg[7])));
      v = xrow_sum(v);
      const u32 o2 = cvtpk(v, dpp<XOR1>(v));
      if (lane < 16 && !(lane & 1)) *(u32*)(out + rb * 512 + 64 * s + 16 * qd + lane) = o2;
      LSTAMP(12);
    }
    STAMP_OUT(9);
    return;
  }

  const int wave = role - 1, ct = wave & 3;
  const int bm = q4 & 1;
  STAMP(1);
  const u32 tile_a = 128 * (8 * g + q4) + 16 * ((2 * ct + (p4 >> 1)) ^ (((q4 >> 1) | ((g & 1) << 1)) << 1)) + 8 * (p4 & 1);
  if (wave < 4) {
    const u32 xa = M_X + 1024 * bm + 256 * rq + 16 * g;
    bf16x8 xf[4];
#pragma unroll
    for (int ks = 0; ks < 4; ++ks) xf[ks] = ld_frag(xa + 64 * ks);
    u64* dst = ws + W_Q1 + ((size_t)h * 8 + rq) * 128 + 64 * par + 16 * ct;
    lds_wait(C_LANDED, 20, landed);
    f32x4 acc = {0.f, 0.f, 0.f, 0.f};
#pragma unroll
    for (int ks = 0; ks < 4; ++ks) acc = mma(xf[ks], ld_tr(LB_W + tile_a + 4096 * ks, LB_W + tile_a + 4096 * ks + 512), acc);
    if (lane < 32) stg(dst, 512 * g + n16, gpack(acc[0], tagHi, tagLo));
    lds_arrive(C_QC);
    STAMP(2);
    __builtin_amdgcn_s_sleep(QSLEEP);
    const u64* src = ws + W_Q1 + (size_t)(h * 2 + b) * 512 + 32 * wave;
    const u32 i0 = (lane >> 5) * 128 + (lane & 31);
    u64 g0, g1;
#pragma unroll 1
    for (int spin = 0; spin < SPIN_MAX; ++spin) {
      g0 = ldg(src, i0); g1 = ldg(src, i0 + 256);
      if (!__any((gbad(g0, tagHi, tagLo) | gbad(g1, tagHi, tagLo)) != 0)) break;
      COUNT(13);
    }
    float v = gval(g0) + gval(g1);
    v = swap32_add(v, v);
    if (lane < 32) *lp<u16>(M_Q + 2 * (32 * wave + lane)) = (u16)cvtpk(v, v);
    lds_arrive(C_Q);
  } else if (ffn) {
    const u32 xa = M_X + 1024 * bm + 512 * (s >> 2) + 32 * g, sel = (s & 2) ? 0x07060302u : 0x05040100u;
    u32x4 xf[4];
#pragma unroll
    for (int ks = 0; ks < 4; ++ks) {
      const u32x4 lo = *lp<u32x4>(xa + 128 * ks), hi = *lp<u32x4>(xa + 128 * ks + 16);
      xf[ks] = u32x4{__builtin_amdgcn_perm(lo[1], lo[0], sel), __builtin_amdgcn_perm(lo[3], lo[2], sel),
                     __builtin_amdgcn_perm(hi[1], hi[0], sel), __builtin_amdgcn_perm(hi[3], hi[2], sel)};
    }
    u64* dst = ws + W_F1 + (((size_t)fg * 2 + (s & 1)) * 4 + (s & 2) + (s >> 2)) * 128 + 16 * ct;
    lds_wait(C_LANDED, 44, landed);
    f32x4 acc = {0.f, 0.f, 0.f, 0.f};
#pragma unroll
    for (int ks = 0; ks < 4; ++ks) acc = mma(__builtin_bit_cast(bf16x8, xf[ks]), ld_tr(LB_F + tile_a + 4096 * ks, LB_F + tile_a + 4096 * ks + 512), acc);
    if (lane < 32) stg(dst, 64 * g + n16, gpack(acc[0], tagHi, tagLo));
    lds_arrive(C_FC);
    STAMP(2);
  }
  STAMP(3);
  lds_wait(C_LANDED, 28 + 8 * wave + (wave >= 1 ? fo : 0u), landed);
  u64 fgr8[4];
  const bool hval = 64 * fg + 16 * ct < FF;
  const u64* fsrc = ws + W_F1 + (size_t)fg * 1024 + 16 * ct - 32;
  const u32 fi = 128 * g + n16 + (g < 2 ? 32 : ct < 2 ? 64 : hval ? 1024 : 0);
  if (wave >= 4 && ffn) { fgr8[0] = ldg(fsrc, fi); fgr8[1] = ldg(fsrc, fi + 64); fgr8[2] = ldg(fsrc, fi + 512); fgr8[3] = ldg(fsrc, fi + 576); }
  lds_wait(C_Q, 4);
  STAMP(4);
  float sc[8];
  {
    const u32 ka = LB_K + 256 * (32 * wave + n16) + 16 * (g ^ n16);
    f32x4 d0 = {0.f, 0.f, 0.f, 0.f}, d1 = {0.f, 0.f, 0.f, 0.f};
#pragma unroll
    for (int ks = 0; ks < 4; ++ks) {
      const bf16x8 qf = ld_frag(M_Q + 64 * ks + 16 * g);
      d0 = mma(ld_frag(ka ^ (64 * ks)), qf, d0);
      d1 = mma(ld_frag((ka ^ (64 * ks)) + 4096), qf, d1);
    }
#pragma unroll
    for (int i = 0; i < 4; i += 2) {
      const u32 a = cvtpk(d0[i], d0[i + 1]), c = cvtpk(d1[i], d1[i + 1]);
      const u32 a2 = cvtpk(bflo(a) * 0.08838834764831845f, bfhi(a) * 0.08838834764831845f);
      const u32 c2 = cvtpk(bflo(c) * 0.08838834764831845f, bfhi(c) * 0.08838834764831845f);
      sc[i] = bflo(a2); sc[i + 1] = bfhi(a2); sc[4 + i] = bflo(c2); sc[5 + i] = bfhi(c2);
    }
  }
  {
    float m = xrow_max(max3(max3(sc[0], sc[1], sc[2]), max3(sc[3], sc[4], sc[5]), max3(sc[6], sc[7], sc[7])));
    if (lane == 0) *lp<float>(M_WM + 4 * wave) = m;
    lds_arrive(C_M);
    lds_wait(C_M, 8);
    STAMP(5);
    const f32x4 a = *lp<f32x4>(M_WM), c = *lp<f32x4>(M_WM + 16);
    m = max3(max3(a[0], a[1], a[2]), max3(a[3], c[0], c[1]), max3(c[2], c[3], c[3]));
    const float mL = m * LOG2E;
    float p[8], l = 0.f;
#pragma unroll
    for (int i = 0; i < 8; ++i) { p[i] = __builtin_amdgcn_exp2f(fmaf(sc[i], LOG2E, -mL)); l += p[i]; }
    l = xrow_sum(l);
    const u32x4 pf = {cvtpk(p[0], p[1]), cvtpk(p[2], p[3]), cvtpk(p[4], p[5]), cvtpk(p[6], p[7])};
    if (n16 == 0) *lp<u32x4>(M_P + 64 * wave + 16 * g) = pf;
    if (lane == 0) *lp<float>(M_WL + 4 * wave) = l;
  }
  lds_arrive(C_P);
  if (wave >= 4 && ffn) {
#pragma unroll 1
    for (int spin = 0; spin < SPIN_MAX; ++spin) {
      u32 bad = 0;
#pragma unroll
      for (int i = 0; i < 4; ++i) bad |= gbad(fgr8[i], tagHi, tagLo);
      if (!__any(bad != 0)) break;
      COUNT(14);
      fgr8[0] = ldg(fsrc, fi); fgr8[1] = ldg(fsrc, fi + 64); fgr8[2] = ldg(fsrc, fi + 512); fgr8[3] = ldg(fsrc, fi + 576);
    }
    const u32 gp = cvtpk(xrow_sum(gval(fgr8[0])), xrow_sum(gval(fgr8[1]))), up = cvtpk(xrow_sum(gval(fgr8[2])), xrow_sum(gval(fgr8[3])));
    u32 hp = cvtpk(bflo(gp) * bflo(up), bfhi(gp) * bfhi(up));
    if (!hval) hp = 0;
    if (lane < 32) *lp<u16>(M_H + 128 * g + 2 * (16 * ct + n16)) = (u16)(g ? hp >> 16 : hp);
    lds_arrive(C_H);
  }
  lds_wait(C_P, 8);
  STAMP(6);
  f32x4 oacc = {0.f, 0.f, 0.f, 0.f};
  {
    u32 va = LB_V + 256 * (4 * g + q4) + 16 * ((2 * wave + (p4 >> 1)) ^ ((4 * (g & 1) + q4) << 1)) + 8 * (p4 & 1), pa = M_P + 16 * g;
#pragma unroll 1
    for (int t = 0; t < 8; ++t) {
      lds_wait(C_LANDED, 92 + fo + 8 * t, landed);
      oacc = mma(ld_frag(pa), ld_tr(va, va + 4096), oacc);
      va += 8192; pa += 64;
    }
  }
  if (lane < 16) stg(ws + W_O2 + ((size_t)(h * 2 + b) * 4 + j) * 128 + 16 * wave, lane, gpack(oacc[0], tagHi, tagLo));
  STAMP(7);
  const u64* vs = ws + W_O2 + (size_t)h * 1024 + 16 * wave;
  const u32 vi = 128 * p4 + (lane >> 2);
  u64 gg[2] = {ldg(vs, vi), ldg(vs, vi + 512)};
  bf16x8 wof[4];
  float yb = 0.f, gate = 0.f;
  if (wave < 4) {
    lds_wait(C_LANDED, 164 + fo, landed);
#pragma unroll
    for (int ks = 0; ks < 4; ++ks) wof[ks] = ld_tr(LB_W + tile_a + 4096 * ks, LB_W + tile_a + 4096 * ks + 512);
    gate = *lp<float>(M_G + 8 * (g & 1));
    if (ffn) {
      lds_wait(C_H, 4);
      lds_wait(C_LANDED, 188, landed);
      f32x4 acc = {0.f, 0.f, 0.f, 0.f};
#pragma unroll
      for (int ks = 0; ks < 2; ++ks) acc = mma(ld_frag(M_H + 128 * bm + 64 * ks + 16 * g), ld_tr(LB_D + tile_a + 4096 * ks, LB_D + tile_a + 4096 * ks + 512), acc);
      yb = *lp<float>(M_G + 8 * (g & 1) + 4) * acc[0];
    }
  }
  lds_wait(C_F, 1);
  const float f0 = *lp<float>(M_F + 4 * p4), f1 = *lp<float>(M_F + 16 + 4 * p4);
  STAMP(8);
#pragma unroll 1
  for (int spin = 0; spin < SPIN_MAX; ++spin) {
    if (!__any((gbad(gg[0], tagHi, tagLo) | gbad(gg[1], tagHi, tagLo)) != 0)) break;
    COUNT(15);
    __builtin_amdgcn_s_sleep(G2SLEEP);
    gg[0] = ldg(vs, vi); gg[1] = ldg(vs, vi + 512);
  }
  STAMP(9);
  {
    float t0 = f0 * gval(gg[0]), t1 = f1 * gval(gg[1]);
    t0 += dpp<XOR1>(t0); t1 += dpp<XOR1>(t1); t0 += dpp<XOR2>(t0); t1 += dpp<XOR2>(t1);
    const u32 ob = cvtpk(t0, t1);
    if (p4 < 2) *lp<u16>(M_O + 256 * p4 + 2 * (16 * wave + (lane >> 2))) = (u16)(p4 ? ob >> 16 : ob);
  }
  WG_BARRIER("B1");
  STAMP(10);
  if (wave < 4) {
    f32x4 acc = {0.f, 0.f, 0.f, 0.f};
#pragma unroll
    for (int ks = 0; ks < 4; ++ks) acc = mma(ld_frag(M_O + 256 * bm + 64 * ks + 16 * g), wof[ks], acc);
    const float y = fmaf(gate, acc[0], yb);
    if (lane < 32) stg(ws + W_Y3 + (((size_t)s * 8 + ct) * 32 + h) * 16, 2048 * g + n16, gpack(y, tagHi, tagLo));
    STAMP(11);
  }
  STAMP_OUT(wave);
}
}

extern "C" void kernel_launch(void* const* d_in, const int* in_sizes, int n_in, void* d_out, int out_size, void* d_ws, size_t ws_size,
                              hipStream_t stream) {
  fused_decode<<<NWG, NTHR, 0, stream>>>((const u16*)d_in[0], (const u16*)d_in[1], (const u16*)d_in[2], (const u16*)d_in[3],
                                         (const u16*)d_in[4], (const u16*)d_in[6], (const u16*)d_in[7], (const u16*)d_in[8],
                                         (const u16*)d_in[9], (u16*)d_out, (u64*)d_ws);
}
```

```cpp
#include <hip/hip_runtime.h>
#include <stdint.h>

typedef unsigned short u16;
typedef unsigned int u32;
typedef unsigned long long u64;
typedef u32 u32x4 __attribute__((ext_vector_type(4)));
typedef short s16x4 __attribute__((ext_vector_type(4)));
typedef short s16x8 __attribute__((ext_vector_type(8)));
typedef float f32x2 __attribute__((ext_vector_type(2)));
typedef float f32x4 __attribute__((ext_vector_type(4)));
typedef __bf16 bf16x2 __attribute__((ext_vector_type(2)));
typedef __bf16 bf16x8 __attribute__((ext_vector_type(8)));
#define GAS __attribute__((address_space(1)))
#define LAS __attribute__((address_space(3)))
typedef GAS u64 gu64;

namespace {
constexpr int NWG = 256, NTHR = 640;
constexpr int FF = 1376;
constexpr size_t W_EPOCH = 0;
constexpr size_t W_Q1 = 1024;
constexpr size_t W_F1 = W_Q1 + 32 * 2 * 4 * 128;
constexpr size_t W_O2 = W_F1 + 22 * 2 * 4 * 128;
constexpr size_t W_ST = W_O2 + 32 * 8 * 128;
constexpr size_t W_Y3 = W_ST + 32 * 8 * 16;
constexpr size_t W_END = W_Y3 + 8 * 2 * 4 * 32 * 16;
constexpr size_t W_DBG = 262144;
constexpr u32 LB_M = 0;
constexpr u32 M_CNT = LB_M;
constexpr u32 M_TAG = M_CNT + 32;
constexpr u32 M_G = M_TAG + 16;
constexpr u32 M_WM = M_G + 16;
constexpr u32 M_WL = M_WM + 32;
constexpr u32 M_F = M_WL + 32;
constexpr u32 M_Q = 256;
constexpr u32 M_P = M_Q + 256;
constexpr u32 M_O = M_P + 512;
constexpr u32 M_H = M_O + 512;
constexpr u32 M_X = 2048;
constexpr u32 M_GW = M_X + 2048;
constexpr u32 LB_W = 8192;
constexpr u32 LB_D = LB_W + 16384;
constexpr u32 LB_K = LB_D + 8192;
constexpr u32 LB_V = LB_K + 65536;
constexpr u32 LB_F = LB_V + 49152;
constexpr u32 C_LANDED = M_CNT, C_F = M_CNT + 4, C_QC = M_CNT + 8, C_FC = M_CNT + 12, C_Q = M_CNT + 16, C_M = M_CNT + 20, C_P = M_CNT + 24, C_H = M_CNT + 28;
constexpr u32 LDS_BYTES = 163840;
constexpr int SPIN_MAX = 1 << 15, LSPIN_MAX = 1 << 21;
constexpr int CODE_BYTES = 13312;
constexpr float LOG2E = 1.4426950408889634f;
#define QSLEEP 14
#define G2SLEEP 4
#define G3SLEEP 2
#define WIN 20
#define SSLEEP 10
#define G3FIRST 6

__device__ __forceinline__ u32 cvtpk(float lo, float hi) {
  const f32x2 v = {lo, hi};
  u32 r = __builtin_bit_cast(u32, __builtin_convertvector(v, bf16x2));
  asm("" : "+v"(r));
  return r;
}
__device__ __forceinline__ float max2(float a, float b) { return __builtin_amdgcn_fmed3f(a, b, __builtin_inff()); }
__device__ __forceinline__ float max3(float a, float b, float c) { return max2(max2(a, b), c); }
__device__ __forceinline__ float dot2(u32 a, u32 b, float c) {
  return __builtin_amdgcn_fdot2_f32_bf16(__builtin_bit_cast(bf16x2, a), __builtin_bit_cast(bf16x2, b), c, false);
}
__device__ __forceinline__ float bflo(u32 p) { return __uint_as_float(p << 16); }
__device__ __forceinline__ float bfhi(u32 p) { return __uint_as_float(p & 0xffff0000u); }
__device__ __forceinline__ float rbf(float x) { return bflo(cvtpk(x, x)); }
template <int CTRL> __device__ __forceinline__ float dpp(float x) {
  return __builtin_bit_cast(float, __builtin_amdgcn_mov_dpp(__builtin_bit_cast(int, x), CTRL, 0xf, 0xf, true));
}
constexpr int XOR1 = 0xB1, XOR2 = 0x4E, ROR8 = 0x128, ROR4 = 0x124, ROR2 = 0x122, ROR1 = 0x121;
__device__ __forceinline__ float swap32_add(float a, float b) {
  auto t = __builtin_amdgcn_permlane32_swap(__float_as_uint(a), __float_as_uint(b), false, false);
  return __uint_as_float(t[0]) + __uint_as_float(t[1]);
}
__device__ __forceinline__ float swap16_add(float a, float b) {
  auto t = __builtin_amdgcn_permlane16_swap(__float_as_uint(a), __float_as_uint(b), false, false);
  return __uint_as_float(t[0]) + __uint_as_float(t[1]);
}
__device__ __forceinline__ float xrow_max(float x) {
  auto s = __builtin_amdgcn_permlane16_swap(__float_as_uint(x), __float_as_uint(x), false, false);
  x = max2(__uint_as_float(s[0]), __uint_as_float(s[1]));
  auto t = __builtin_amdgcn_permlane32_swap(__float_as_uint(x), __float_as_uint(x), false, false);
  return max2(__uint_as_float(t[0]), __uint_as_float(t[1]));
}
__device__ __forceinline__ float xrow_sum(float x) { x = swap16_add(x, x); return swap32_add(x, x); }
__device__ __forceinline__ u64 ldg(const u64* base, u32 i) { return __hip_atomic_load((const gu64*)((const GAS char*)base + 8u * i), __ATOMIC_RELAXED, __HIP_MEMORY_SCOPE_AGENT); }
__device__ __forceinline__ void stg(u64* base, u32 i, u64 v) { __hip_atomic_store((gu64*)((GAS char*)base + 8u * i), v, __ATOMIC_RELAXED, __HIP_MEMORY_SCOPE_AGENT); }
__device__ __forceinline__ u64 gpack(float v, u32 tagHi, u32 tagLo) {
  return ((u64)tagHi << 32) | (u64)(tagLo | ((__float_as_uint(v) + 0x80u) >> 8));
}
__device__ __forceinline__ float gval(u64 g) { return __uint_as_float(((u32)g) << 8); }
__device__ __forceinline__ u32 gbad(u64 g, u32 tagHi, u32 tagLo) { return ((u32)(g >> 32) ^ tagHi) | (((u32)g ^ tagLo) >> 24); }

template <typename T> __device__ __forceinline__ LAS T* lp(u32 a) { return (LAS T*)(size_t)a; }
__device__ __forceinline__ bf16x8 ld_frag(u32 a) { return __builtin_bit_cast(bf16x8, *lp<u32x4>(a)); }
__device__ __forceinline__ bf16x8 ld_tr(u32 a0, u32 a1) {
  const s16x4 lo = __builtin_amdgcn_ds_read_tr16_b64_v4i16(lp<s16x4>(a0)), hi = __builtin_amdgcn_ds_read_tr16_b64_v4i16(lp<s16x4>(a1));
  return __builtin_bit_cast(bf16x8, __builtin_shufflevector(lo, hi, 0, 1, 2, 3, 4, 5, 6, 7));
}
__device__ __forceinline__ f32x4 mma(bf16x8 a, bf16x8 b, f32x4 c) { return __builtin_amdgcn_mfma_f32_16x16x32_bf16(a, b, c, 0, 0, 0); }
__device__ __forceinline__ void lds_arrive(u32 c) {
  asm volatile("s_waitcnt lgkmcnt(0)\n\ts_mov_b64 exec, 1\n\tds_add_u32 %0, %1\n\ts_mov_b64 exec, -1" :: "v"(c), "v"(1u) : "memory");
}
__device__ __forceinline__ void lds_wait(u32 c, u32 n, u32& seen) {
  u32 v, cnt = LSPIN_MAX;
  asm volatile("s_cmp_ge_u32 %1, %4\n\ts_cbranch_scc1 2f\n"
               "1:\n\tds_read_b32 %0, %3\n\ts_waitcnt lgkmcnt(0)\n\tv_readfirstlane_b32 %1, %0\n\ts_cmp_ge_u32 %1, %4\n\ts_cbranch_scc1 2f\n\t"
               "s_sleep 1\n\ts_sub_u32 %2, %2, 1\n\ts_cbranch_scc0 1b\n2:"
               : "=&v"(v), "+&s"(seen), "+&s"(cnt) : "v"(c), "s"(n) : "memory", "scc");
}
__device__ __forceinline__ void lds_wait(u32 c, u32 n) { u32 seen = 0; lds_wait(c, n, seen); }
#define WG_BARRIER(id) asm volatile("s_waitcnt lgkmcnt(0)\n\ts_barrier ; " id ::: "memory")
#define STR2(x) #x
#define STR(x) STR2(x)
#define DMA_THROTTLE "s_waitcnt vmcnt(" STR(WIN) ")\n\tds_write_b32 %[vz], %[prog]\n\t"
#define DMA_TILE_BODY(THR) \
  "s_mov_b32 m0, %[dst]\n\ts_nop 0\n1:\n\t" THR \
  "global_load_lds_dwordx4 %[v0], %[base] nt\n\ts_add_u32 m0, m0, 0x400\n\tv_add_u32 %[v0], %[step], %[v0]\n\t" \
  "global_load_lds_dwordx4 %[v1], %[base] nt\n\ts_add_u32 m0, m0, 0x400\n\tv_add_u32 %[v1], %[step], %[v1]\n\t" \
  "global_load_lds_dwordx4 %[v0], %[base] nt\n\ts_add_u32 m0, m0, 0x400\n\tv_add_u32 %[v0], %[step], %[v0]\n\t" \
  "global_load_lds_dwordx4 %[v1], %[base] nt\n\ts_add_u32 m0, m0, 0x400\n\tv_add_u32 %[v1], %[step], %[v1]\n\t" \
  "v_add_u32 %[prog], 4, %[prog]\n\ts_sub_u32 %[cnt], %[cnt], 1\n\ts_cbranch_scc0 1b"
template <bool THROTTLED>
__device__ __forceinline__ void dma_tile(u32 dst, const void* base, u32 v0, u32 v1, u32 step, u32 groups, u32& prog) {
  u32 cnt = groups - 1;
  if constexpr (THROTTLED)
    asm volatile(DMA_TILE_BODY(DMA_THROTTLE) : [v0] "+&v"(v0), [v1] "+&v"(v1), [prog] "+&v"(prog), [cnt] "+&s"(cnt)
                 : [dst] "s"(dst), [base] "s"(base), [step] "s"(step), [vz] "v"(0u) : "memory", "scc");
  else
    asm volatile(DMA_TILE_BODY("") : [v0] "+&v"(v0), [v1] "+&v"(v1), [prog] "+&v"(prog), [cnt] "+&s"(cnt)
                 : [dst] "s"(dst), [base] "s"(base), [step] "s"(step) : "memory", "scc");
}
#define DMA_ROWS_BODY(THR) \
  "s_mov_b32 m0, %[dst]\n\ts_nop 0\n1:\n\t" THR \
  "global_load_lds_dwordx4 %[v0], %[base] nt\n\tglobal_load_lds_dwordx4 %[v1], %[base] offset:1024 nt\n\t" \
  "global_load_lds_dwordx4 %[v2], %[base] offset:2048 nt\n\tglobal_load_lds_dwordx4 %[v3], %[base] offset:3072 nt\n\t" \
  "s_add_u32 m0, m0, 0x1000\n\tv_add_u32 %[v0], 0x1000, %[v0]\n\tv_add_u32 %[v1], 0x1000, %[v1]\n\tv_add_u32 %[v2], 0x1000, %[v2]\n\tv_add_u32 %[v3], 0x1000, %[v3]\n\t" \
  "v_add_u32 %[prog], 4, %[prog]\n\ts_sub_u32 %[cnt], %[cnt], 1\n\ts_cbranch_scc0 1b"
template <bool THROTTLED>
__device__ __forceinline__ void dma_rows(u32 dst, const void* base, u32& v0, u32& v1, u32& v2, u32& v3, u32 groups, u32& prog) {
  u32 cnt = groups - 1;
  if constexpr (THROTTLED)
    asm volatile(DMA_ROWS_BODY(DMA_THROTTLE) : [v0] "+&v"(v0), [v1] "+&v"(v1), [v2] "+&v"(v2), [v3] "+&v"(v3), [prog] "+&v"(prog), [cnt] "+&s"(cnt)
                 : [dst] "s"(dst), [base] "s"(base), [vz] "v"(0u) : "memory", "scc");
  else
    asm volatile(DMA_ROWS_BODY("") : [v0] "+&v"(v0), [v1] "+&v"(v1), [v2] "+&v"(v2), [v3] "+&v"(v3), [prog] "+&v"(prog), [cnt] "+&s"(cnt)
                 : [dst] "s"(dst), [base] "s"(base) : "memory", "scc");
}

#define STAMP(i)
#define LSTAMP(i)
#define STAMP_DECL
#define STAMP_OUT(slot)
#define COUNT(i)

__global__ void __launch_bounds__(NTHR, 3)
fused_decode(const u16* __restrict__ x, const u16* __restrict__ Kc, const u16* __restrict__ Vc, const u16* __restrict__ wq,
             const u16* __restrict__ wo, const u16* __restrict__ wg, const u16* __restrict__ wu, const u16* __restrict__ wd,
             const u16* __restrict__ gw, u16* __restrict__ out, u64* __restrict__ ws) {
  __shared__ __attribute__((aligned(1024))) unsigned char lds_all[LDS_BYTES];
  asm volatile("" :: "v"((LAS unsigned char*)lds_all));
  STAMP_DECL;
  LSTAMP(0);
  asm volatile("" :: "s"(x), "s"(Kc), "s"(Vc), "s"(wq), "s"(wo), "s"(wg), "s"(wu), "s"(wd), "s"(gw), "s"(out), "s"(ws));
  const int tid = threadIdx.x, lane = tid & 63;
  const int wid = __builtin_amdgcn_readfirstlane(tid >> 6);
  const int wgid = blockIdx.x, xcd = wgid & 7, idx = wgid >> 3;
  const int hh = idx >> 3, s = idx & 7, h = xcd * 4 + hh;
  const int rq = s >> 1, par = s & 1;
  const int b = s & 1, j = s >> 1;
  const u32 r8 = lane >> 3, r4 = lane >> 4;
  const u32 sw0 = 16u * ((lane & 7) ^ (r8 & 2));
  const size_t kvoff = ((size_t)(b * 32 + h) * 4096 + 3072 + j * 256) * 256;
  u32 k0 = r4 * 256 + 16 * ((lane & 15) ^ r4), k1 = k0 ^ 64, k2 = k0 ^ 128, k3 = k0 ^ 192;
  u32 prog = 28 - WIN;
  if (wid == 0) {
    STAMP(6);
    __builtin_amdgcn_s_setprio(2);
    if (lane < 8) *lp<u32>(M_CNT + 4 * lane) = 0;
    asm volatile("s_mov_b64 exec, 1\n\ts_mov_b32 m0, %0\n\ts_nop 0\n\tglobal_load_lds_dwordx4 %1, %2\n\ts_mov_b64 exec, -1\n\t"
                 "s_mov_b32 m0, %3\n\ts_nop 0\n\tglobal_load_lds_dwordx4 %4, %5\n\tglobal_load_lds_dwordx4 %4, %5 offset:1024\n\t"
                 "s_mov_b32 m0, %6\n\ts_nop 0\n\tglobal_load_lds_dwordx4 %4, %7"
                 :: "s"(M_TAG), "v"(0u), "s"(ws + W_EPOCH), "s"(M_X), "v"(16u * lane), "s"(x), "s"(M_GW), "s"(gw) : "memory");
    STAMP(7);
    u32 dummy = 0;
    dma_tile<false>(LB_W, (const char*)wq + ((size_t)rq << 20) + 256 * h + 128 * par, r8 * 8192 + sw0, r8 * 8192 + (sw0 ^ 64) + 65536, 131072, 4, dummy);
    STAMP(8);
    dma_rows<false>(LB_K, (const char*)Kc + kvoff, k0, k1, k2, k3, 2, dummy);
  }
  u32 pf0;
  {
    u64 pc;
    asm volatile("s_getpc_b64 %0" : "=s"(pc));
    const u32 off = min((idx * 4 + (lane & 3)) * 128u, (u32)CODE_BYTES - 128u);
    pc &= ~127ull;
    if (tid >= 64 && tid < 68) asm volatile("global_load_dword %0, %1, %2" : "=v"(pf0) : "v"(off), "s"(pc) : "memory");
  }
  WG_BARRIER("B0");
  const int fgr = (int)((0x46573210u >> (4 * xcd)) & 15u) * 4 + hh;
  const bool ffn = fgr < 22;
  const int fg = ffn ? fgr : 0;
  const u32 fo = ffn ? 16u : 0u;
  int role = wid;
  asm volatile("" : "+s"(role));
  if (role == 0) {
    STAMP(1);
    const u32 sw1 = sw0 ^ 64;
    if (ffn)
      dma_tile<true>(LB_F, (const char*)((s & 1) ? wu : wg) + (size_t)(256 * (s >> 2) + ((s >> 1) & 1)) * (2 * FF) + 128 * fg - 64 * ((s >> 1) & 1),
                     r8 * (4 * FF) + sw0, r8 * (4 * FF) + sw1 + 8 * 4 * FF, 16 * 4 * FF, 4, prog);
    dma_rows<true>(LB_K + 8192, (const char*)Kc + kvoff, k0, k1, k2, k3, 14, prog);
    STAMP(2);
    {
      u32 v0 = r4 * 256 + 16 * ((lane & 15) ^ (r4 << 1)), v1 = v0 ^ 128, v2 = v0, v3 = v1;
      dma_rows<true>(LB_V, (const char*)Vc + kvoff, v0, v1, v2, v3, 12, prog);
      if (ffn) lds_wait(C_FC, 4);
      dma_rows<true>(LB_V + 49152, (const char*)Vc + kvoff, v0, v1, v2, v3, 4, prog);
    }
    STAMP(3);
    lds_wait(C_QC, 4);
    dma_tile<true>(LB_W, (const char*)(wo + (size_t)128 * h * 512 + 64 * s), r8 * 1024 + sw0, r8 * 1024 + sw1 + 8192, 16384, 4, prog);
    if (ffn) {
      const int row = 64 * fg;
      dma_tile<true>(LB_D, (const char*)(wd + (size_t)row * 512 + 64 * s), r8 * 1024 + sw0, r8 * 1024 + sw1 + 8192, 16384, 1, prog);
      dma_tile<true>(LB_D + 4096, (const char*)(wd + (size_t)(row + 32 < FF ? row + 32 : row) * 512 + 64 * s), r8 * 1024 + sw0, r8 * 1024 + sw1 + 8192, 16384, 1, prog);
    }
    STAMP(4);
#define DRAIN(k) "s_waitcnt vmcnt(" #k ")\n\tv_add_u32 %1, " STR(WIN) " - " #k ", %0\n\tds_write_b32 %2, %1\n\t"
    u32 t;
    asm volatile(DRAIN(20) DRAIN(16) DRAIN(12) DRAIN(8) DRAIN(4) DRAIN(0) "s_waitcnt lgkmcnt(0)" : "+v"(prog), "=&v"(t) : "v"(0u) : "memory");
    LSTAMP(5);
    STAMP_OUT(8);
    return;
  }
  const int g = lane >> 4, n16 = lane & 15, q4 = n16 >> 2, p4 = lane & 3;
  u32 landed = 0;
  lds_wait(C_LANDED, 4, landed);
  const u64 ep = *lp<u64>(M_TAG) + 1;
  const u32 tagHi = (u32)(ep >> 8), tagLo = (u32)ep << 24;

  if (role == 9) {
    {
      const u32x4 gv = *lp<u32x4>(M_GW + 16 * lane), x0 = *lp<u32x4>(M_X + 16 * lane), x1 = *lp<u32x4>(M_X + 1024 + 16 * lane);
      float z0 = 0.f, z1 = 0.f;
#pragma unroll
      for (int t = 0; t < 4; ++t) {
        const float gl = bflo(gv[t]), gh = bfhi(gv[t]);
        z0 = dot2(cvtpk(bflo(x0[t]) * gl, bfhi(x0[t]) * gh), 0x3f803f80u, z0);
        z1 = dot2(cvtpk(bflo(x1[t]) * gl, bfhi(x1[t]) * gh), 0x3f803f80u, z1);
      }
      float z = swap32_add(z0, z1);
      z = swap16_add(z, z);
      z += dpp<ROR8>(z); z += dpp<ROR4>(z); z += dpp<ROR2>(z); z += dpp<ROR1>(z);
      const float gt = rbf(__builtin_amdgcn_rcpf(1.f + __expf(-rbf(z))));
      if ((lane & 31) == 0) { *lp<float>(M_G + (lane >> 5) * 8) = gt; *lp<float>(M_G + (lane >> 5) * 8 + 4) = rbf(1.f - gt); }
    }
    STAMP(1);
    if (wgid == 4) {
      __builtin_amdgcn_s_sleep(48);
      const u32 i0 = (lane >> 3) * 1024 + ((lane >> 1) & 3) * 128 + (lane & 1) * 64;
#pragma unroll 1
      for (int spin = 0; spin < SPIN_MAX; ++spin) {
        u32 bad = 0;
#pragma unroll
        for (int i = 0; i < 4; ++i) bad |= gbad(ldg(ws + W_Q1 + 8192 * i, i0), tagHi, tagLo);
        if (!__any(bad != 0)) { if (lane == 0) stg(ws + W_EPOCH, 0, ep); break; }
        __builtin_amdgcn_s_sleep(8);
      }
    }
    lds_wait(C_P, 8);
    {
      const f32x4 a = *lp<f32x4>(M_WM), c = *lp<f32x4>(M_WM + 16), la = *lp<f32x4>(M_WL), lc = *lp<f32x4>(M_WL + 16);
      const float m = max3(max3(a[0], a[1], a[2]), max3(a[3], c[0], c[1]), max3(c[2], c[3], c[3]));
      const float L = ((la[0] + la[1]) + (la[2] + la[3])) + ((lc[0] + lc[1]) + (lc[2] + lc[3]));
      if (lane < 16) stg(ws + W_ST + ((size_t)(h * 2 + b) * 4 + j) * 16, lane, gpack((lane & 1) ? L : m, tagHi, tagLo));
    }
    STAMP(2);
    __builtin_amdgcn_s_sleep(SSLEEP);
    {
      const u64* src = ws + W_ST + (size_t)h * 128;
      u64 s0, s1;
#pragma unroll 1
      for (int spin = 0; spin < SPIN_MAX; ++spin) {
        s0 = ldg(src, lane); s1 = ldg(src, 64 + lane);
        if (!__any((gbad(s0, tagHi, tagLo) | gbad(s1, tagHi, tagLo)) != 0)) break;
        COUNT(13);
        __builtin_amdgcn_s_sleep(4);
      }
#pragma unroll
      for (int bb = 0; bb < 2; ++bb) {
        const float v = gval(bb ? s1 : s0), o = dpp<XOR1>(v);
        const float mj = (lane & 1) ? o : v, lj = (lane & 1) ? v : o;
        const float w = __builtin_amdgcn_exp2f((mj - xrow_max(mj)) * LOG2E);
        const float f = w * __builtin_amdgcn_rcpf(xrow_sum(w * lj));
        if (n16 == 0) *lp<float>(M_F + 16 * bb + 4 * g) = f;
      }
      lds_arrive(C_F);
    }
    STAMP(3);
    WG_BARRIER("B1 helper");
    if (hh == 0) {
      const int rb = s >> 2, qd = s & 3, rc = xcd;
      const u64* src = ws + W_Y3 + (((size_t)rc * 2 + rb) * 4 + qd) * 512;
      u64 gg[8];
      __builtin_amdgcn_s_sleep(G3FIRST);
#pragma unroll 1
      for (int spin = 0; spin < SPIN_MAX; ++spin) {
#pragma unroll
        for (int i = 0; i < 8; ++i) gg[i] = ldg(src, 64 * i + lane);
        u32 bad = 0;
#pragma unroll
        for (int i = 0; i < 8; ++i) bad |= gbad(gg[i], tagHi, tagLo);
        if (!__any(bad != 0)) break;
        COUNT(14);
        __builtin_amdgcn_s_sleep(G3SLEEP);
      }
      float v = ((gval(gg[0]) + gval(gg[1])) + (gval(gg[2]) + gval(gg[3]))) + ((gval(gg[4]) + gval(gg[5])) + (gval(gg[6]) + gval(gg[7])));
      v = xrow_sum(v);
      const u32 o2 = cvtpk(v, dpp<XOR1>(v));
      if (lane < 16 && !(lane & 1)) *(u32*)(out + rb * 512 + 64 * rc + 16 * qd + lane) = o2;
      LSTAMP(12);
    }
    STAMP_OUT(9);
    return;
  }

  const int wave = role - 1, ct = wave & 3;
  const int bm = q4 & 1;
  STAMP(1);
  const u32 tile_a = 128 * (8 * g + q4) + 16 * ((2 * ct + (p4 >> 1)) ^ (((q4 >> 1) | ((g & 1) << 1)) << 1)) + 8 * (p4 & 1);
  if (wave < 4) {
    const u32 xa = M_X + 1024 * bm + 256 * rq + 16 * g;
    bf16x8 xf[4];
#pragma unroll
    for (int ks = 0; ks < 4; ++ks) xf[ks] = ld_frag(xa + 64 * ks);
    u64* dst = ws + W_Q1 + ((size_t)h * 8 + rq) * 128 + 64 * par + 16 * ct;
    lds_wait(C_LANDED, 20, landed);
    f32x4 acc = {0.f, 0.f, 0.f, 0.f};
#pragma unroll
    for (int ks = 0; ks < 4; ++ks) acc = mma(xf[ks], ld_tr(LB_W + tile_a + 4096 * ks, LB_W + tile_a + 4096 * ks + 512), acc);
    if (lane < 32) stg(dst, 512 * g + n16, gpack(acc[0], tagHi, tagLo));
    lds_arrive(C_QC);
    STAMP(2);
    __builtin_amdgcn_s_sleep(QSLEEP);
    const u64* src = ws + W_Q1 + (size_t)(h * 2 + b) * 512 + 32 * wave;
    const u32 i0 = (lane >> 5) * 128 + (lane & 31);
    u64 g0, g1;
#pragma unroll 1
    for (int spin = 0; spin < SPIN_MAX; ++spin) {
      g0 = ldg(src, i0); g1 = ldg(src, i0 + 256);
      if (!__any((gbad(g0, tagHi, tagLo) | gbad(g1, tagHi, tagLo)) != 0)) break;
      COUNT(13);
    }
    float v = gval(g0) + gval(g1);
    v = swap32_add(v, v);
    if (lane < 32) *lp<u16>(M_Q + 2 * (32 * wave + lane)) = (u16)cvtpk(v, v);
    lds_arrive(C_Q);
  } else if (ffn) {
    const u32 xa = M_X + 1024 * bm + 512 * (s >> 2) + 32 * g, sel = (s & 2) ? 0x07060302u : 0x05040100u;
    u32x4 xf[4];
#pragma unroll
    for (int ks = 0; ks < 4; ++ks) {
      const u32x4 lo = *lp<u32x4>(xa + 128 * ks), hi = *lp<u32x4>(xa + 128 * ks + 16);
      xf[ks] = u32x4{__builtin_amdgcn_perm(lo[1], lo[0], sel), __builtin_amdgcn_perm(lo[3], lo[2], sel),
                     __builtin_amdgcn_perm(hi[1], hi[0], sel), __builtin_amdgcn_perm(hi[3], hi[2], sel)};
    }
    u64* dst = ws + W_F1 + (((size_t)fg * 2 + (s & 1)) * 4 + (s & 2) + (s >> 2)) * 128 + 16 * ct;
    lds_wait(C_LANDED, 44, landed);
    f32x4 acc = {0.f, 0.f, 0.f, 0.f};
#pragma unroll
    for (int ks = 0; ks < 4; ++ks) acc = mma(__builtin_bit_cast(bf16x8, xf[ks]), ld_tr(LB_F + tile_a + 4096 * ks, LB_F + tile_a + 4096 * ks + 512), acc);
    if (lane < 32) stg(dst, 64 * g + n16, gpack(acc[0], tagHi, tagLo));
    lds_arrive(C_FC);
    STAMP(2);
  }
  if (wave == 0) asm volatile("" : "+v"(pf0));
  STAMP(3);
  lds_wait(C_LANDED, 28 + 8 * wave + (wave >= 1 ? fo : 0u), landed);
  u64 fgr8[4];
  const bool hval = 64 * fg + 16 * ct < FF;
  const u64* fsrc = ws + W_F1 + (size_t)fg * 1024 + 16 * ct - 32;
  const u32 fi = 128 * g + n16 + (g < 2 ? 32 : ct < 2 ? 64 : hval ? 1024 : 0);
  if (wave >= 4 && ffn) { fgr8[0] = ldg(fsrc, fi); fgr8[1] = ldg(fsrc, fi + 64); fgr8[2] = ldg(fsrc, fi + 512); fgr8[3] = ldg(fsrc, fi + 576); }
  lds_wait(C_Q, 4);
  STAMP(4);
  float sc[8];
  {
    const u32 ka = LB_K + 256 * (32 * wave + n16) + 16 * (g ^ n16);
    f32x4 d0 = {0.f, 0.f, 0.f, 0.f}, d1 = {0.f, 0.f, 0.f, 0.f};
#pragma unroll
    for (int ks = 0; ks < 4; ++ks) {
      const bf16x8 qf = ld_frag(M_Q + 64 * ks + 16 * g);
      d0 = mma(ld_frag(ka ^ (64 * ks)), qf, d0);
      d1 = mma(ld_frag((ka ^ (64 * ks)) + 4096), qf, d1);
    }
#pragma unroll
    for (int i = 0; i < 4; i += 2) {
      const u32 a = cvtpk(d0[i], d0[i + 1]), c = cvtpk(d1[i], d1[i + 1]);
      const u32 a2 = cvtpk(bflo(a) * 0.08838834764831845f, bfhi(a) * 0.08838834764831845f);
      const u32 c2 = cvtpk(bflo(c) * 0.08838834764831845f, bfhi(c) * 0.08838834764831845f);
      sc[i] = bflo(a2); sc[i + 1] = bfhi(a2); sc[4 + i] = bflo(c2); sc[5 + i] = bfhi(c2);
    }
  }
  {
    float m = xrow_max(max3(max3(sc[0], sc[1], sc[2]), max3(sc[3], sc[4], sc[5]), max3(sc[6], sc[7], sc[7])));
    if (lane == 0) *lp<float>(M_WM + 4 * wave) = m;
    lds_arrive(C_M);
    lds_wait(C_M, 8);
    STAMP(5);
    const f32x4 a = *lp<f32x4>(M_WM), c = *lp<f32x4>(M_WM + 16);
    m = max3(max3(a[0], a[1], a[2]), max3(a[3], c[0], c[1]), max3(c[2], c[3], c[3]));
    const float mL = m * LOG2E;
    float p[8], l = 0.f;
#pragma unroll
    for (int i = 0; i < 8; ++i) { p[i] = __builtin_amdgcn_exp2f(fmaf(sc[i], LOG2E, -mL)); l += p[i]; }
    l = xrow_sum(l);
    const u32x4 pf = {cvtpk(p[0], p[1]), cvtpk(p[2], p[3]), cvtpk(p[4], p[5]), cvtpk(p[6], p[7])};
    if (n16 == 0) *lp<u32x4>(M_P + 64 * wave + 16 * g) = pf;
    if (lane == 0) *lp<float>(M_WL + 4 * wave) = l;
  }
  lds_arrive(C_P);
  if (wave >= 4 && ffn) {
#pragma unroll 1
    for (int spin = 0; spin < SPIN_MAX; ++spin) {
      u32 bad = 0;
#pragma unroll
      for (int i = 0; i < 4; ++i) bad |= gbad(fgr8[i], tagHi, tagLo);
      if (!__any(bad != 0)) break;
      COUNT(14);
      fgr8[0] = ldg(fsrc, fi); fgr8[1] = ldg(fsrc, fi + 64); fgr8[2] = ldg(fsrc, fi + 512); fgr8[3] = ldg(fsrc, fi + 576);
    }
    const u32 gp = cvtpk(xrow_sum(gval(fgr8[0])), xrow_sum(gval(fgr8[1]))), up = cvtpk(xrow_sum(gval(fgr8[2])), xrow_sum(gval(fgr8[3])));
    u32 hp = cvtpk(bflo(gp) * bflo(up), bfhi(gp) * bfhi(up));
    if (!hval) hp = 0;
    if (lane < 32) *lp<u16>(M_H + 128 * g + 2 * (16 * ct + n16)) = (u16)(g ? hp >> 16 : hp);
    lds_arrive(C_H);
  }
  lds_wait(C_P, 8);
  STAMP(6);
  f32x4 oacc = {0.f, 0.f, 0.f, 0.f};
  {
    u32 va = LB_V + 256 * (4 * g + q4) + 16 * ((2 * wave + (p4 >> 1)) ^ ((4 * (g & 1) + q4) << 1)) + 8 * (p4 & 1), pa = M_P + 16 * g;
#pragma unroll 1
    for (int t = 0; t < 8; ++t) {
      lds_wait(C_LANDED, 92 + fo + 8 * t, landed);
      oacc = mma(ld_frag(pa), ld_tr(va, va + 4096), oacc);
      va += 8192; pa += 64;
    }
  }
  if (lane < 16) stg(ws + W_O2 + ((size_t)(h * 2 + b) * 4 + j) * 128 + 16 * wave, lane, gpack(oacc[0], tagHi, tagLo));
  STAMP(7);
  const u64* vs = ws + W_O2 + (size_t)h * 1024 + 16 * wave;
  const u32 vi = 128 * p4 + (lane >> 2);
  u64 gg[2] = {ldg(vs, vi), ldg(vs, vi + 512)};
  bf16x8 wof[4];
  float yb = 0.f, gate = 0.f;
  if (wave < 4) {
    lds_wait(C_LANDED, 164 + fo, landed);
#pragma unroll
    for (int ks = 0; ks < 4; ++ks) wof[ks] = ld_tr(LB_W + tile_a + 4096 * ks, LB_W + tile_a + 4096 * ks + 512);
    gate = *lp<float>(M_G + 8 * (g & 1));
    if (ffn) {
      lds_wait(C_H, 4);
      lds_wait(C_LANDED, 188, landed);
      f32x4 acc = {0.f, 0.f, 0.f, 0.f};
#pragma unroll
      for (int ks = 0; ks < 2; ++ks) acc = mma(ld_frag(M_H + 128 * bm + 64 * ks + 16 * g), ld_tr(LB_D + tile_a + 4096 * ks, LB_D + tile_a + 4096 * ks + 512), acc);
      yb = *lp<float>(M_G + 8 * (g & 1) + 4) * acc[0];
    }
  }
  lds_wait(C_F, 1);
  const float f0 = *lp<float>(M_F + 4 * p4), f1 = *lp<float>(M_F + 16 + 4 * p4);
  STAMP(8);
#pragma unroll 1
  for (int spin = 0; spin < SPIN_MAX; ++spin) {
    if (!__any((gbad(gg[0], tagHi, tagLo) | gbad(gg[1], tagHi, tagLo)) != 0)) break;
    COUNT(15);
    __builtin_amdgcn_s_sleep(G2SLEEP);
    gg[0] = ldg(vs, vi); gg[1] = ldg(vs, vi + 512);
  }
  STAMP(9);
  {
    float t0 = f0 * gval(gg[0]), t1 = f1 * gval(gg[1]);
    t0 += dpp<XOR1>(t0); t1 += dpp<XOR1>(t1); t0 += dpp<XOR2>(t0); t1 += dpp<XOR2>(t1);
    const u32 ob = cvtpk(t0, t1);
    if (p4 < 2) *lp<u16>(M_O + 256 * p4 + 2 * (16 * wave + (lane >> 2))) = (u16)(p4 ? ob >> 16 : ob);
  }
  WG_BARRIER("B1");
  STAMP(10);
  if (wave < 4) {
    f32x4 acc = {0.f, 0.f, 0.f, 0.f};
#pragma unroll
    for (int ks = 0; ks < 4; ++ks) acc = mma(ld_frag(M_O + 256 * bm + 64 * ks + 16 * g), wof[ks], acc);
    const float y = fmaf(gate, acc[0], yb);
    if (lane < 32) stg(ws + W_Y3 + (((size_t)s * 8 + ct) * 32 + h) * 16, 2048 * g + n16, gpack(y, tagHi, tagLo));
    STAMP(11);
  }
  STAMP_OUT(wave);
}
}

extern "C" void kernel_launch(void* const* d_in, const int* in_sizes, int n_in, void* d_out, int out_size, void* d_ws, size_t ws_size,
                              hipStream_t stream) {
  fused_decode<<<NWG, NTHR, 0, stream>>>((const u16*)d_in[0], (const u16*)d_in[1], (const u16*)d_in[2], (const u16*)d_in[3],
                                         (const u16*)d_in[4], (const u16*)d_in[6], (const u16*)d_in[7], (const u16*)d_in[8],
                                         (const u16*)d_in[9], (u16*)d_out, (u64*)d_ws);
}
```

```cpp
#include <hip/hip_runtime.h>
#include <stdint.h>

typedef unsigned short u16;
typedef unsigned int u32;
typedef unsigned long long u64;
typedef u32 u32x4 __attribute__((ext_vector_type(4)));
typedef short s16x4 __attribute__((ext_vector_type(4)));
typedef short s16x8 __attribute__((ext_vector_type(8)));
typedef float f32x2 __attribute__((ext_vector_type(2)));
typedef float f32x4 __attribute__((ext_vector_type(4)));
typedef __bf16 bf16x2 __attribute__((ext_vector_type(2)));
typedef __bf16 bf16x8 __attribute__((ext_vector_type(8)));
#define GAS __attribute__((address_space(1)))
#define LAS __attribute__((address_space(3)))
typedef GAS u64 gu64;

namespace {
constexpr int NWG = 256, NTHR = 640;
constexpr int FF = 1376;
constexpr u32 W_EPOCH = 0;
constexpr u32 W_Q1 = 1024;
constexpr u32 F1_ROW = 1408;
constexpr u32 W_F1 = W_Q1 + 32 * 512;
constexpr u32 W_O2 = W_F1 + 8 * F1_ROW;
constexpr u32 W_ST = W_O2 + 32 * 512;
constexpr u32 W_Y3 = W_ST + 32 * 128;
constexpr u32 W_END = W_Y3 + 32 * 512;
constexpr size_t W_DBG = 262144;
constexpr u32 LB_M = 0;
constexpr u32 M_CNT = LB_M;
constexpr u32 M_TAG = M_CNT + 48;
constexpr u32 M_G = M_TAG + 16;
constexpr u32 M_WM = M_G + 16;
constexpr u32 M_WL = M_WM + 32;
constexpr u32 M_F = M_WL + 32;
constexpr u32 M_Q = 256;
constexpr u32 M_P = M_Q + 256;
constexpr u32 M_O = M_P + 512;
constexpr u32 M_H = M_O + 512;
constexpr u32 M_X = 2048;
constexpr u32 M_GW = M_X + 2048;
constexpr u32 LB_W = 8192;
constexpr u32 LB_D = LB_W + 16384;
constexpr u32 LB_K = LB_D + 8192;
constexpr u32 LB_V = LB_K + 65536;
constexpr u32 LB_F = LB_V + 49152;
constexpr u32 C_LANDED = M_CNT, C_F = M_CNT + 4, C_QC = M_CNT + 8, C_FC = M_CNT + 12, C_Q = M_CNT + 16, C_M = M_CNT + 20, C_P = M_CNT + 24, C_H = M_CNT + 28,
              C_ISS = M_CNT + 32;
constexpr u32 LDS_BYTES = 163840;
constexpr int SPIN_MAX = 1 << 15, LSPIN_MAX = 1 << 21;
constexpr int CODE_BYTES = 13824;
constexpr float LOG2E = 1.4426950408889634f;
#define QSLEEP 14
#define G2SLEEP 1
#define G3SLEEP 1
#define WIN 16
#define WIN2 28
#define KN1 8
#define G3DUAL 0
#define FBLK 5
#define STBLK 4
#define G3FIRST 2

__device__ __forceinline__ u32 cvtpk(float lo, float hi) {
  const f32x2 v = {lo, hi};
  u32 r = __builtin_bit_cast(u32, __builtin_convertvector(v, bf16x2));
  asm("" : "+v"(r));
  return r;
}
__device__ __forceinline__ float max2(float a, float b) { return __builtin_amdgcn_fmed3f(a, b, __builtin_inff()); }
__device__ __forceinline__ float max3(float a, float b, float c) { return max2(max2(a, b), c); }
__device__ __forceinline__ float dot2(u32 a, u32 b, float c) {
  return __builtin_amdgcn_fdot2_f32_bf16(__builtin_bit_cast(bf16x2, a), __builtin_bit_cast(bf16x2, b), c, false);
}
__device__ __forceinline__ float bflo(u32 p) { return __uint_as_float(p << 16); }
__device__ __forceinline__ float bfhi(u32 p) { return __uint_as_float(p & 0xffff0000u); }
__device__ __forceinline__ float rbf(float x) { return bflo(cvtpk(x, x)); }
template <int CTRL> __device__ __forceinline__ float dpp(float x) {
  return __builtin_bit_cast(float, __builtin_amdgcn_mov_dpp(__builtin_bit_cast(int, x), CTRL, 0xf, 0xf, true));
}
constexpr int XOR1 = 0xB1, XOR2 = 0x4E, ROR8 = 0x128, ROR4 = 0x124, ROR2 = 0x122, ROR1 = 0x121;
__device__ __forceinline__ float swap32_add(float a, float b) {
  auto t = __builtin_amdgcn_permlane32_swap(__float_as_uint(a), __float_as_uint(b), false, false);
  return __uint_as_float(t[0]) + __uint_as_float(t[1]);
}
__device__ __forceinline__ float swap16_add(float a, float b) {
  auto t = __builtin_amdgcn_permlane16_swap(__float_as_uint(a), __float_as_uint(b), false, false);
  return __uint_as_float(t[0]) + __uint_as_float(t[1]);
}
__device__ __forceinline__ float xrow_max(float x) {
  auto s = __builtin_amdgcn_permlane16_swap(__float_as_uint(x), __float_as_uint(x), false, false);
  x = max2(__uint_as_float(s[0]), __uint_as_float(s[1]));
  auto t = __builtin_amdgcn_permlane32_swap(__float_as_uint(x), __float_as_uint(x), false, false);
  return max2(__uint_as_float(t[0]), __uint_as_float(t[1]));
}
__device__ __forceinline__ float xrow_sum(float x) { x = swap16_add(x, x); return swap32_add(x, x); }
__device__ __forceinline__ u64 ldg(const u64* base, u32 i) { return __hip_atomic_load((const gu64*)((const GAS char*)base + 8u * i), __ATOMIC_RELAXED, __HIP_MEMORY_SCOPE_AGENT); }
__device__ __forceinline__ void stg(u64* base, u32 i, u64 v) { __hip_atomic_store((gu64*)((GAS char*)base + 8u * i), v, __ATOMIC_RELAXED, __HIP_MEMORY_SCOPE_AGENT); }
struct Tag { u32 a, b; };
__device__ __forceinline__ u64 gpack(float v0, float v1, Tag t) {
  const u32 lo = ((__float_as_uint(v0) + 0x200u) & 0xfffffc00u) | t.a, hi = ((__float_as_uint(v1) + 0x200u) & 0xfffffc00u) | t.b;
  return ((u64)hi << 32) | lo;
}
__device__ __forceinline__ float gval0(u64 g) { return __uint_as_float((u32)g & 0xfffffc00u); }
__device__ __forceinline__ float gval1(u64 g) { return __uint_as_float((u32)(g >> 32) & 0xfffffc00u); }
__device__ __forceinline__ u32 gbad(u64 g, Tag t) { return (((u32)g ^ t.a) | ((u32)(g >> 32) ^ t.b)) & 0x3ffu; }

template <typename T> __device__ __forceinline__ LAS T* lp(u32 a) { return (LAS T*)(size_t)a; }
__device__ __forceinline__ bf16x8 ld_frag(u32 a) { return __builtin_bit_cast(bf16x8, *lp<u32x4>(a)); }
__device__ __forceinline__ bf16x8 ld_tr(u32 a0, u32 a1) {
  const s16x4 lo = __builtin_amdgcn_ds_read_tr16_b64_v4i16(lp<s16x4>(a0)), hi = __builtin_amdgcn_ds_read_tr16_b64_v4i16(lp<s16x4>(a1));
  return __builtin_bit_cast(bf16x8, __builtin_shufflevector(lo, hi, 0, 1, 2, 3, 4, 5, 6, 7));
}
__device__ __forceinline__ f32x4 mma(bf16x8 a, bf16x8 b, f32x4 c) { return __builtin_amdgcn_mfma_f32_16x16x32_bf16(a, b, c, 0, 0, 0); }
__device__ __forceinline__ void lds_arrive(u32 c) {
  asm volatile("s_waitcnt lgkmcnt(0)\n\ts_mov_b64 exec, 1\n\tds_add_u32 %0, %1\n\ts_mov_b64 exec, -1" :: "v"(c), "v"(1u) : "memory");
}
__device__ __forceinline__ void lds_wait(u32 c, u32 n, u32& seen) {
  u32 v, cnt = LSPIN_MAX;
  asm volatile("s_cmp_ge_u32 %1, %4\n\ts_cbranch_scc1 2f\n"
               "1:\n\tds_read_b32 %0, %3\n\ts_waitcnt lgkmcnt(0)\n\tv_readfirstlane_b32 %1, %0\n\ts_cmp_ge_u32 %1, %4\n\ts_cbranch_scc1 2f\n\t"
               "s_sleep 1\n\ts_sub_u32 %2, %2, 1\n\ts_cbranch_scc0 1b\n2:"
               : "=&v"(v), "+&s"(seen), "+&s"(cnt) : "v"(c), "s"(n) : "memory", "scc");
}
__device__ __forceinline__ void lds_wait(u32 c, u32 n) { u32 seen = 0; lds_wait(c, n, seen); }
#define WG_BARRIER(id) asm volatile("s_waitcnt lgkmcnt(0)\n\ts_barrier ; " id ::: "memory")
#define STR2(x) #x
#define STR(x) STR2(x)
#define DMA_THROTTLE "s_waitcnt vmcnt(%[win])\n\tds_write_b32 %[vz], %[prog]\n\t"
#define DMA_TILE_BODY(THR) \
  "s_mov_b32 m0, %[dst]\n\ts_nop 0\n1:\n\t" THR \
  "global_load_lds_dwordx4 %[v0], %[base] nt\n\ts_add_u32 m0, m0, 0x400\n\tv_add_u32 %[v0], %[step], %[v0]\n\t" \
  "global_load_lds_dwordx4 %[v1], %[base] nt\n\ts_add_u32 m0, m0, 0x400\n\tv_add_u32 %[v1], %[step], %[v1]\n\t" \
  "global_load_lds_dwordx4 %[v0], %[base] nt\n\ts_add_u32 m0, m0, 0x400\n\tv_add_u32 %[v0], %[step], %[v0]\n\t" \
  "global_load_lds_dwordx4 %[v1], %[base] nt\n\ts_add_u32 m0, m0, 0x400\n\tv_add_u32 %[v1], %[step], %[v1]\n\t" \
  "v_add_u32 %[prog], 4, %[prog]\n\ts_sub_u32 %[cnt], %[cnt], 1\n\ts_cbranch_scc0 1b"
template <int W>
__device__ __forceinline__ void dma_tile(u32 dst, const void* base, u32 v0, u32 v1, u32 step, u32 groups, u32& prog) {
  u32 cnt = groups - 1;
  if constexpr (W > 0)
    asm volatile(DMA_TILE_BODY(DMA_THROTTLE) : [v0] "+&v"(v0), [v1] "+&v"(v1), [prog] "+&v"(prog), [cnt] "+&s"(cnt)
                 : [dst] "s"(dst), [base] "s"(base), [step] "s"(step), [vz] "v"(0u), [win] "n"(W) : "memory", "scc");
  else
    asm volatile(DMA_TILE_BODY("") : [v0] "+&v"(v0), [v1] "+&v"(v1), [prog] "+&v"(prog), [cnt] "+&s"(cnt)
                 : [dst] "s"(dst), [base] "s"(base), [step] "s"(step) : "memory", "scc");
}
template <int W>
__device__ __forceinline__ void dma_two(u32 dst, const void* base, u32 v0, u32 v1, u32& prog) {
  asm volatile("s_mov_b32 m0, %[dst]\n\t" DMA_THROTTLE "global_load_lds_dwordx4 %[v0], %[base] nt\n\ts_add_u32 m0, m0, 0x400\n\tv_add_u32 %[prog], 2, %[prog]\n\t"
               "global_load_lds_dwordx4 %[v1], %[base] nt"
               : [prog] "+&v"(prog) : [dst] "s"(dst), [base] "s"(base), [v0] "v"(v0), [v1] "v"(v1), [vz] "v"(0u), [win] "n"(W) : "memory", "scc");
}
#define DMA_ROWS_BODY(THR) \
  "s_mov_b32 m0, %[dst]\n\ts_nop 0\n1:\n\t" THR \
  "global_load_lds_dwordx4 %[v0], %[base] nt\n\tglobal_load_lds_dwordx4 %[v1], %[base] offset:1024 nt\n\t" \
  "global_load_lds_dwordx4 %[v2], %[base] offset:2048 nt\n\tglobal_load_lds_dwordx4 %[v3], %[base] offset:3072 nt\n\t" \
  "s_add_u32 m0, m0, 0x1000\n\tv_add_u32 %[v0], 0x1000, %[v0]\n\tv_add_u32 %[v1], 0x1000, %[v1]\n\tv_add_u32 %[v2], 0x1000, %[v2]\n\tv_add_u32 %[v3], 0x1000, %[v3]\n\t" \
  "v_add_u32 %[prog], 4, %[prog]\n\ts_sub_u32 %[cnt], %[cnt], 1\n\ts_cbranch_scc0 1b"
template <int W>
__device__ __forceinline__ void dma_rows(u32 dst, const void* base, u32& v0, u32& v1, u32& v2, u32& v3, u32 groups, u32& prog) {
  u32 cnt = groups - 1;
  if constexpr (W > 0)
    asm volatile(DMA_ROWS_BODY(DMA_THROTTLE) : [v0] "+&v"(v0), [v1] "+&v"(v1), [v2] "+&v"(v2), [v3] "+&v"(v3), [prog] "+&v"(prog), [cnt] "+&s"(cnt)
                 : [dst] "s"(dst), [base] "s"(base), [vz] "v"(0u), [win] "n"(W) : "memory", "scc");
  else
    asm volatile(DMA_ROWS_BODY("") : [v0] "+&v"(v0), [v1] "+&v"(v1), [v2] "+&v"(v2), [v3] "+&v"(v3), [prog] "+&v"(prog), [cnt] "+&s"(cnt)
                 : [dst] "s"(dst), [base] "s"(base) : "memory", "scc");
}

#define STAMP(i)
#define LSTAMP(i)
#define STAMP_DECL
#define STAMP_OUT(slot)
#define COUNT(i)
#define STAMP_PAR
#define STAMP_ARG

__device__ __forceinline__ void final_reduce(const u64* ws, u16* out, int c, int qd, int lane, Tag tag, int first STAMP_PAR) {
  const u64* src = ws + W_Y3 + (c * 4 + qd) * 512;
  u64 gg[8];
  for (int i = 0; i < first; ++i) __builtin_amdgcn_s_sleep(1);
#pragma unroll
  for (int i = 0; i < 8; ++i) gg[i] = ldg(src, 64 * i + lane);
#pragma unroll 1
  for (int spin = 0; spin < SPIN_MAX; ++spin) {
    bool again = false;
#pragma unroll
    for (int i = 0; i < 8; ++i)
      if (__any(gbad(gg[i], tag) != 0)) { gg[i] = ldg(src, 64 * i + lane); again = true; }
    if (!again) break;
    COUNT(15);
  }
  const float v0 = ((gval0(gg[0]) + gval0(gg[1])) + (gval0(gg[2]) + gval0(gg[3]))) + ((gval0(gg[4]) + gval0(gg[5])) + (gval0(gg[6]) + gval0(gg[7])));
  const float v1 = ((gval1(gg[0]) + gval1(gg[1])) + (gval1(gg[2]) + gval1(gg[3]))) + ((gval1(gg[4]) + gval1(gg[5])) + (gval1(gg[6]) + gval1(gg[7])));
  float v = swap16_add(v0, v1);
  v = swap32_add(v, v);
  const u32 o2 = cvtpk(v, dpp<XOR1>(v));
  if (lane < 32 && !(lane & 1)) *(u32*)(out + (lane >> 4) * 512 + 64 * c + 16 * qd + (lane & 15)) = o2;
  LSTAMP(12);
}

__global__ void __launch_bounds__(NTHR, 3)
fused_decode(const u16* __restrict__ x, const u16* __restrict__ Kc, const u16* __restrict__ Vc, const u16* __restrict__ wq,
             const u16* __restrict__ wo, const u16* __restrict__ wg, const u16* __restrict__ wu, const u16* __restrict__ wd,
             const u16* __restrict__ gw, u16* __restrict__ out, u64* __restrict__ ws) {
  __shared__ __attribute__((aligned(1024))) unsigned char lds_all[LDS_BYTES];
  asm volatile("" :: "v"((LAS unsigned char*)lds_all));
  STAMP_DECL;
  LSTAMP(0);
  asm volatile("" :: "s"(x), "s"(Kc), "s"(Vc), "s"(wq), "s"(wo), "s"(wg), "s"(wu), "s"(wd), "s"(gw), "s"(out), "s"(ws));
  const int tid = threadIdx.x, lane = tid & 63;
  const int wid = __builtin_amdgcn_readfirstlane(tid >> 6);
  const int wgid = blockIdx.x, xcd = wgid & 7, idx = wgid >> 3;
  const int hh = idx >> 3, s = idx & 7, h = xcd * 4 + hh;
  const int rq = s >> 1, par = s & 1;
  const int b = s & 1, j = s >> 1;
  const u32 r8 = lane >> 3, r4 = lane >> 4;
  const u32 sw0 = 16u * ((lane & 7) ^ (r8 & 2));
  const size_t kvoff = ((size_t)(b * 32 + h) * 4096 + 3072 + j * 256) * 256;
  u32 k0 = r4 * 256 + 16 * ((lane & 15) ^ r4), k1 = k0 ^ 64, k2 = k0 ^ 128, k3 = k0 ^ 192;
  u32 prog = 28 - WIN;
  if (wid == 0) {
    STAMP(6);
    __builtin_amdgcn_s_setprio(2);
    if (lane < 12) *lp<u32>(M_CNT + 4 * lane) = 0;
    asm volatile("s_mov_b64 exec, 1\n\ts_mov_b32 m0, %0\n\ts_nop 0\n\tglobal_load_lds_dwordx4 %1, %2\n\ts_mov_b64 exec, -1\n\t"
                 "s_mov_b32 m0, %3\n\ts_nop 0\n\tglobal_load_lds_dwordx4 %4, %5\n\tglobal_load_lds_dwordx4 %4, %5 offset:1024\n\t"
                 "s_mov_b32 m0, %6\n\ts_nop 0\n\tglobal_load_lds_dwordx4 %4, %7"
                 :: "s"(M_TAG), "v"(0u), "s"(ws + W_EPOCH), "s"(M_X), "v"(16u * lane), "s"(x), "s"(M_GW), "s"(gw) : "memory");
    STAMP(7);
    u32 dummy = 0;
    dma_tile<0>(LB_W, (const char*)wq + ((size_t)rq << 20) + 256 * h + 128 * par, r8 * 8192 + sw0, r8 * 8192 + (sw0 ^ 64) + 65536, 131072, 4, dummy);
    STAMP(8);
    dma_rows<0>(LB_K, (const char*)Kc + kvoff, k0, k1, k2, k3, 2, dummy);
  }
  u32 pf0;
  {
    u64 pc;
    asm volatile("s_getpc_b64 %0" : "=s"(pc));
    const u32 off = min((idx * 4 + (lane & 3)) * 128u, (u32)CODE_BYTES - 128u);
    pc &= ~127ull;
    if (tid >= 64 && tid < 68) asm volatile("global_load_dword %0, %1, %2" : "=v"(pf0) : "v"(off), "s"(pc) : "memory");
  }
  WG_BARRIER("B0");
  const int fgr = (int)((0x46573210u >> (4 * xcd)) & 15u) * 4 + hh;
  const bool ffn = fgr < 22;
  const int fg = ffn ? fgr : 0;
  const u32 fo = ffn ? 16u : 0u;
  int role = wid;
  asm volatile("" : "+s"(role));
  if (role == 0) {
    STAMP(1);
    const u32 sw1 = sw0 ^ 64;
    if (ffn)
      dma_tile<WIN>(LB_F, (const char*)((s & 1) ? wu : wg) + (size_t)(256 * (s >> 2) + ((s >> 1) & 1)) * (2 * FF) + 128 * fg - 64 * ((s >> 1) & 1),
                     r8 * (4 * FF) + sw0, r8 * (4 * FF) + sw1 + 8 * 4 * FF, 16 * 4 * FF, 4, prog);
    dma_rows<WIN>(LB_K + 8192, (const char*)Kc + kvoff, k0, k1, k2, k3, KN1, prog);
    prog -= WIN2 - WIN;
    dma_rows<WIN2>(LB_K + 8192 + 4096 * KN1, (const char*)Kc + kvoff, k0, k1, k2, k3, 14 - KN1, prog);
    STAMP(2);
    {
      u32 v0 = r4 * 256 + 16 * ((lane & 15) ^ (r4 << 1)), v1 = v0 ^ 128, v2 = v0, v3 = v1;
      dma_rows<WIN2>(LB_V, (const char*)Vc + kvoff, v0, v1, v2, v3, 12, prog);
      if (ffn) lds_wait(C_FC, 4);
      dma_rows<WIN2>(LB_V + 49152, (const char*)Vc + kvoff, v0, v1, v2, v3, 4, prog);
    }
    STAMP(3);
    {
      const char* base = (const char*)(wd + (size_t)43 * h * 512 + 64 * s);
      dma_tile<WIN2>(LB_D, base, r8 * 1024 + sw0, r8 * 1024 + sw1 + 8192, 16384, 1, prog);
      const u32 rmax = FF - 1 - 43 * h;
      dma_two<WIN2>(LB_D + 4096, base, min(32 + r8, rmax) * 1024 + sw0, min(40 + r8, rmax) * 1024 + sw1, prog);
    }
    lds_wait(C_QC, 4);
    dma_tile<WIN2>(LB_W, (const char*)(wo + (size_t)128 * h * 512 + 64 * s), r8 * 1024 + sw0, r8 * 1024 + sw1 + 8192, 16384, 4, prog);
    *(volatile LAS u32*)(size_t)C_ISS = 1;
    STAMP(4);
#define DRAIN(k) "s_waitcnt vmcnt(" #k ")\n\tv_add_u32 %1, " STR(WIN2) " - " #k ", %0\n\tds_write_b32 %2, %1\n\t"
    u32 t;
    asm volatile(DRAIN(28) DRAIN(24) DRAIN(20) DRAIN(16) DRAIN(12) DRAIN(8) DRAIN(4) DRAIN(0) "s_waitcnt lgkmcnt(0)" : "+v"(prog), "=&v"(t) : "v"(0u) : "memory");
    LSTAMP(5);
    STAMP_OUT(8);
    return;
  }
  const int g = lane >> 4, n16 = lane & 15, q4 = n16 >> 2, p4 = lane & 3;
  u32 landed = 0;
  if (role == 9) {
    const u32x4 z4 = {0u, 0u, 0u, 0u};
    *lp<u32x4>(LB_D + 6144 + 16 * lane) = z4; *lp<u32x4>(LB_D + 7168 + 16 * lane) = z4;
    if (lane < 16) *lp<u32x4>(M_H + 16 * lane) = z4;
  }
  lds_wait(C_LANDED, 4, landed);
  const u64 ep = *lp<u64>(M_TAG) + 1;
  const Tag tag = {(u32)ep & 0x3ffu, (u32)(ep >> 10) & 0x3ffu};

  if (role == 9 && wgid == 0) { u64 t0_; asm volatile("s_memrealtime %0\n\ts_waitcnt lgkmcnt(0)" : "=s"(t0_)); if (lane == 0) ws[200000] = t0_; }
  if (role == 9) {
    {
      const u32x4 gv = *lp<u32x4>(M_GW + 16 * lane), x0 = *lp<u32x4>(M_X + 16 * lane), x1 = *lp<u32x4>(M_X + 1024 + 16 * lane);
      float z0 = 0.f, z1 = 0.f;
#pragma unroll
      for (int t = 0; t < 4; ++t) {
        const float gl = bflo(gv[t]), gh = bfhi(gv[t]);
        z0 = dot2(cvtpk(bflo(x0[t]) * gl, bfhi(x0[t]) * gh), 0x3f803f80u, z0);
        z1 = dot2(cvtpk(bflo(x1[t]) * gl, bfhi(x1[t]) * gh), 0x3f803f80u, z1);
      }
      float z = swap32_add(z0, z1);
      z = swap16_add(z, z);
      z += dpp<ROR8>(z); z += dpp<ROR4>(z); z += dpp<ROR2>(z); z += dpp<ROR1>(z);
      const float gt = rbf(__builtin_amdgcn_rcpf(1.f + __expf(-rbf(z))));
      if ((lane & 31) == 0) { *lp<float>(M_G + (lane >> 5) * 8) = gt; *lp<float>(M_G + (lane >> 5) * 8 + 4) = rbf(1.f - gt); }
    }
    STAMP(1);
    lds_wait(C_LANDED, 28 + fo + 8 * FBLK, landed);
    const u32 fb = (43 * h) & ~15u, fe = fb + lane, fo2 = min(fb + 32 + lane, F1_ROW - 1);
    u64 fp[8];
#pragma unroll
    for (int i = 0; i < 8; ++i) fp[i] = ldg(ws + W_F1 + F1_ROW * i, (i & 2) ? fo2 : fe);
    lds_wait(C_P, 8);
    {
      const f32x4 a = *lp<f32x4>(M_WM), c = *lp<f32x4>(M_WM + 16), la = *lp<f32x4>(M_WL), lc = *lp<f32x4>(M_WL + 16);
      const float m = max3(max3(a[0], a[1], a[2]), max3(a[3], c[0], c[1]), max3(c[2], c[3], c[3]));
      const float L = ((la[0] + la[1]) + (la[2] + la[3])) + ((lc[0] + lc[1]) + (lc[2] + lc[3]));
      stg(ws + W_ST + ((h * 2 + b) * 4 + j) * 16, n16, gpack(m, L, tag));
    }
    STAMP(2);
    {
      u32 bad = 0;
#pragma unroll
      for (int i = 0; i < 8; ++i) bad |= gbad(fp[i], tag);
      if (__builtin_expect(__any(bad != 0), 0)) {
#pragma unroll 1
        for (int spin = 0; spin < SPIN_MAX; ++spin) {
          COUNT(14);
          bad = 0;
#pragma unroll
          for (int i = 0; i < 8; ++i) fp[i] = ldg(ws + W_F1 + F1_ROW * i, (i & 2) ? fo2 : fe);
#pragma unroll
          for (int i = 0; i < 8; ++i) bad |= gbad(fp[i], tag);
          if (!__any(bad != 0)) break;
        }
      }
      const u32 gp = cvtpk((gval0(fp[0]) + gval0(fp[1])) + (gval0(fp[2]) + gval0(fp[3])), (gval1(fp[0]) + gval1(fp[1])) + (gval1(fp[2]) + gval1(fp[3])));
      const u32 up = cvtpk((gval0(fp[4]) + gval0(fp[5])) + (gval0(fp[6]) + gval0(fp[7])), (gval1(fp[4]) + gval1(fp[5])) + (gval1(fp[6]) + gval1(fp[7])));
      const u32 hp = cvtpk(bflo(gp) * bflo(up), bfhi(gp) * bfhi(up));
      const u32 i = fe - 43 * h;
      if (i < 43) { *lp<u16>(M_H + 2 * i) = (u16)hp; *lp<u16>(M_H + 128 + 2 * i) = (u16)(hp >> 16); }
      lds_arrive(C_H);
    }
    STAMP(3);
    lds_wait(C_LANDED, 92 + fo + 8 * STBLK, landed);
    {
      const u64* src = ws + W_ST + h * 128;
      u64 s0, s1;
#pragma unroll 1
      for (int spin = 0; spin < SPIN_MAX; ++spin) {
        s0 = ldg(src, lane); s1 = ldg(src, 64 + lane);
        if (!__any((gbad(s0, tag) | gbad(s1, tag)) != 0)) break;
        COUNT(13);
      }
      const u64 sb = (lane & 8) ? s1 : s0;
      const float mj = gval0(sb);
      const float w = __builtin_amdgcn_exp2f((mj - xrow_max(mj)) * LOG2E);
      const float f = w * __builtin_amdgcn_rcpf(xrow_sum(w * gval1(sb)));
      if ((lane & 7) == 0) *lp<float>(M_F + 2 * (lane & 8) + 4 * g) = f;
      lds_arrive(C_F);
    }
    STAMP(4);
    WG_BARRIER("B1 helper");
    if (wgid == 68) {
      const u32 i0 = (lane >> 3) * 512 + ((lane >> 1) & 3) * 64 + (lane & 1) * 32;
#pragma unroll 1
      for (int spin = 0; spin < SPIN_MAX; ++spin) {
        u32 bad = 0;
#pragma unroll
        for (int i = 0; i < 4; ++i) bad |= gbad(ldg(ws + W_Q1 + 4096 * i, i0), tag);
        if (!__any(bad != 0)) { if (lane == 0) stg(ws + W_EPOCH, 0, ep); break; }
        __builtin_amdgcn_s_sleep(8);
      }
    }
    if (hh == 0 && s < 4) final_reduce(ws, out, xcd, s, lane, tag, G3FIRST STAMP_ARG);
    STAMP_OUT(9);
    return;
  }

  const int wave = role - 1, ct = wave & 3;
  const int bm = n16 & 1;
  STAMP(1);
  const u32 tile_a = 128 * (8 * g + q4) + 16 * ((2 * ct + (p4 >> 1)) ^ (((q4 >> 1) | ((g & 1) << 1)) << 1)) + 8 * (p4 & 1);
  if (wave < 4) {
    const u32 xa = M_X + 1024 * bm + 256 * rq + 16 * g;
    bf16x8 xf[4];
#pragma unroll
    for (int ks = 0; ks < 4; ++ks) xf[ks] = ld_frag(xa + 64 * ks);
    lds_wait(C_LANDED, 20, landed);
    f32x4 acc = {0.f, 0.f, 0.f, 0.f};
#pragma unroll
    for (int ks = 0; ks < 4; ++ks) acc = mma(xf[ks], ld_tr(LB_W + tile_a + 4096 * ks, LB_W + tile_a + 4096 * ks + 512), acc);
    {
      const float n0 = dpp<XOR1>(acc[0]), n1 = dpp<XOR1>(acc[1]);
      if (lane < 16) stg(ws + W_Q1 + h * 512 + rq * 64 + 32 * par + 8 * ct, 256 * bm + (n16 >> 1), gpack(bm ? n1 : acc[0], bm ? acc[1] : n0, tag));
    }
    lds_arrive(C_QC);
    STAMP(2);
    __builtin_amdgcn_s_sleep(QSLEEP);
    const u64* src = ws + W_Q1 + h * 512 + b * 256 + 16 * wave;
    u64 g0;
#pragma unroll 1
    for (int spin = 0; spin < SPIN_MAX; ++spin) {
      g0 = ldg(src, 64 * g + n16);
      if (!__any(gbad(g0, tag) != 0)) break;
      COUNT(13);
    }
    const u32 qq = cvtpk(xrow_sum(gval0(g0)), xrow_sum(gval1(g0)));
    if (lane < 16) *lp<u32>(M_Q + 4 * (16 * wave + lane)) = qq;
    lds_arrive(C_Q);
  } else if (ffn) {
    const u32 xa = M_X + 1024 * bm + 512 * (s >> 2) + 32 * g, sel = (s & 2) ? 0x07060302u : 0x05040100u;
    u32x4 xf[4];
#pragma unroll
    for (int ks = 0; ks < 4; ++ks) {
      const u32x4 lo = *lp<u32x4>(xa + 128 * ks), hi = *lp<u32x4>(xa + 128 * ks + 16);
      xf[ks] = u32x4{__builtin_amdgcn_perm(lo[1], lo[0], sel), __builtin_amdgcn_perm(lo[3], lo[2], sel),
                     __builtin_amdgcn_perm(hi[1], hi[0], sel), __builtin_amdgcn_perm(hi[3], hi[2], sel)};
    }
    lds_wait(C_LANDED, 44, landed);
    f32x4 acc = {0.f, 0.f, 0.f, 0.f};
#pragma unroll
    for (int ks = 0; ks < 4; ++ks) acc = mma(__builtin_bit_cast(bf16x8, xf[ks]), ld_tr(LB_F + tile_a + 4096 * ks, LB_F + tile_a + 4096 * ks + 512), acc);
    if (lane < 16) stg(ws + W_F1 + ((s & 1) * 4 + (s & 2) + (s >> 2)) * F1_ROW + 64 * fg + 16 * ct, lane, gpack(acc[0], acc[1], tag));
    lds_arrive(C_FC);
    STAMP(2);
  }
  if (wave == 0) asm volatile("" : "+v"(pf0));
  STAMP(3);
  lds_wait(C_LANDED, 28 + 8 * wave + (wave >= 1 ? fo : 0u), landed);
  lds_wait(C_Q, 4);
  STAMP(4);
  float sc[8];
  {
    const u32 ka = LB_K + 256 * (32 * wave + n16) + 16 * (g ^ n16);
    f32x4 d0 = {0.f, 0.f, 0.f, 0.f}, d1 = {0.f, 0.f, 0.f, 0.f};
#pragma unroll
    for (int ks = 0; ks < 4; ++ks) {
      const bf16x8 qf = ld_frag(M_Q + 64 * ks + 16 * g);
      d0 = mma(ld_frag(ka ^ (64 * ks)), qf, d0);
      d1 = mma(ld_frag((ka ^ (64 * ks)) + 4096), qf, d1);
    }
#pragma unroll
    for (int i = 0; i < 4; i += 2) {
      const u32 a = cvtpk(d0[i], d0[i + 1]), c = cvtpk(d1[i], d1[i + 1]);
      const u32 a2 = cvtpk(bflo(a) * 0.08838834764831845f, bfhi(a) * 0.08838834764831845f);
      const u32 c2 = cvtpk(bflo(c) * 0.08838834764831845f, bfhi(c) * 0.08838834764831845f);
      sc[i] = bflo(a2); sc[i + 1] = bfhi(a2); sc[4 + i] = bflo(c2); sc[5 + i] = bfhi(c2);
    }
  }
  {
    float m = xrow_max(max3(max3(sc[0], sc[1], sc[2]), max3(sc[3], sc[4], sc[5]), max3(sc[6], sc[7], sc[7])));
    if (lane == 0) *lp<float>(M_WM + 4 * wave) = m;
    lds_arrive(C_M);
    lds_wait(C_M, 8);
    STAMP(5);
    const f32x4 a = *lp<f32x4>(M_WM), c = *lp<f32x4>(M_WM + 16);
    m = max3(max3(a[0], a[1], a[2]), max3(a[3], c[0], c[1]), max3(c[2], c[3], c[3]));
    const float mL = m * LOG2E;
    float p[8], l = 0.f;
#pragma unroll
    for (int i = 0; i < 8; ++i) { p[i] = __builtin_amdgcn_exp2f(fmaf(sc[i], LOG2E, -mL)); l += p[i]; }
    l = xrow_sum(l);
    const u32x4 pf = {cvtpk(p[0], p[1]), cvtpk(p[2], p[3]), cvtpk(p[4], p[5]), cvtpk(p[6], p[7])};
    if (n16 == 0) *lp<u32x4>(M_P + 64 * wave + 16 * g) = pf;
    if (lane == 0) *lp<float>(M_WL + 4 * wave) = l;
  }
  lds_arrive(C_P);
  lds_wait(C_P, 8);
  STAMP(6);
  f32x4 oacc = {0.f, 0.f, 0.f, 0.f};
  {
    const u32 va = LB_V + 256 * (4 * g + q4) + 16 * ((2 * wave + (p4 >> 1)) ^ ((4 * (g & 1) + q4) << 1)) + 8 * (p4 & 1);
    bf16x8 pfr[8];
#pragma unroll
    for (int t = 0; t < 8; ++t) pfr[t] = ld_frag(M_P + 64 * t + 16 * g);
    lds_wait(C_LANDED, 92 + fo + 8 * 5, landed);
#pragma unroll
    for (int t = 0; t < 6; ++t) oacc = mma(pfr[t], ld_tr(va + 8192 * t, va + 8192 * t + 4096), oacc);
    lds_wait(C_LANDED, 92 + fo + 8 * 6, landed);
    oacc = mma(pfr[6], ld_tr(va + 8192 * 6, va + 8192 * 6 + 4096), oacc);
    lds_wait(C_LANDED, 92 + fo + 8 * 7, landed);
    oacc = mma(pfr[7], ld_tr(va + 8192 * 7, va + 8192 * 7 + 4096), oacc);
  }
  {
    const float on = dpp<XOR1>(oacc[0]);
    if (lane < 16 && !(lane & 1)) stg(ws + W_O2 + ((h * 2 + b) * 4 + j) * 64 + 8 * wave, lane >> 1, gpack(oacc[0], on, tag));
  }
  STAMP(7);
  const u64* vs = ws + W_O2 + h * 512 + (wave & 1) * 256 + 16 * (wave >> 1);
  const u32 vi = 64 * p4 + (lane >> 2);
  lds_wait(C_ISS, 1);
  u64 gg = ldg(vs, vi);
  bf16x8 wof[4];
  float yb0 = 0.f, yb1 = 0.f;
  f32x4 gates;
  if (wave < 4) {
    lds_wait(C_H, 1);
    gates = *lp<f32x4>(M_G);
    lds_wait(C_LANDED, 154 + fo, landed);
    f32x4 acc = {0.f, 0.f, 0.f, 0.f};
#pragma unroll
    for (int ks = 0; ks < 2; ++ks) acc = mma(ld_frag(M_H + 128 * bm + 64 * ks + 16 * g), ld_tr(LB_D + tile_a + 4096 * ks, LB_D + tile_a + 4096 * ks + 512), acc);
    yb0 = gates[1] * acc[0]; yb1 = gates[3] * acc[1];
    lds_wait(C_LANDED, 170 + fo, landed);
#pragma unroll
    for (int ks = 0; ks < 4; ++ks) wof[ks] = ld_tr(LB_W + tile_a + 4096 * ks, LB_W + tile_a + 4096 * ks + 512);
  }
  lds_wait(C_F, 1);
  const float fq = *lp<float>(M_F + 16 * (wave & 1) + 4 * p4);
  STAMP(8);
  if (__builtin_expect(__any(gbad(gg, tag) != 0), 0)) {
#pragma unroll 1
    for (int spin = 0; spin < SPIN_MAX; ++spin) {
      COUNT(15);
      gg = ldg(vs, vi);
      if (!__any(gbad(gg, tag) != 0)) break;
      __builtin_amdgcn_s_sleep(G2SLEEP);
    }
  }
  STAMP(9);
  {
    float t0 = fq * gval0(gg), t1 = fq * gval1(gg);
    t0 += dpp<XOR1>(t0); t1 += dpp<XOR1>(t1); t0 += dpp<XOR2>(t0); t1 += dpp<XOR2>(t1);
    const u32 ob = cvtpk(t0, t1);
    if (p4 == 0) *lp<u32>(M_O + 256 * (wave & 1) + 64 * (wave >> 1) + lane) = ob;
  }
  WG_BARRIER("B1");
  STAMP(10);
  if (wave < 4) {
    f32x4 acc = {0.f, 0.f, 0.f, 0.f};
#pragma unroll
    for (int ks = 0; ks < 4; ++ks) acc = mma(ld_frag(M_O + 256 * bm + 64 * ks + 16 * g), wof[ks], acc);
    if (lane < 16) stg(ws + W_Y3 + ((s * 4 + ct) * 32 + h) * 16, lane, gpack(fmaf(gates[0], acc[0], yb0), fmaf(gates[2], acc[1], yb1), tag));
    STAMP(11);
  }
  STAMP_OUT(wave);
}
__global__ void pad_kernel(u64* ws, int ticks) {
  u64 t0 = ws[200000]; u64 c; int it = 0;
  do { asm volatile("s_memrealtime %0\n\ts_waitcnt lgkmcnt(0)" : "=s"(c)); } while (c < t0 + ticks && ++it < 200000);
}
}

extern "C" void kernel_launch(void* const* d_in, const int* in_sizes, int n_in, void* d_out, int out_size, void* d_ws, size_t ws_size,
                              hipStream_t stream) {
  fused_decode<<<NWG, NTHR, 0, stream>>>((const u16*)d_in[0], (const u16*)d_in[1], (const u16*)d_in[2], (const u16*)d_in[3],
                                         (const u16*)d_in[4], (const u16*)d_in[6], (const u16*)d_in[7], (const u16*)d_in[8],
                                         (const u16*)d_in[9], (u16*)d_out, (u64*)d_ws);
  pad_kernel<<<1, 64, 0, stream>>>((u64*)d_ws, 0);
}
```

```cpp
#include <hip/hip_runtime.h>
#include <stdint.h>

typedef unsigned short u16;
typedef unsigned int u32;
typedef unsigned long long u64;
typedef u32 u32x4 __attribute__((ext_vector_type(4)));
typedef short s16x4 __attribute__((ext_vector_type(4)));
typedef short s16x8 __attribute__((ext_vector_type(8)));
typedef float f32x2 __attribute__((ext_vector_type(2)));
typedef float f32x4 __attribute__((ext_vector_type(4)));
typedef __bf16 bf16x2 __attribute__((ext_vector_type(2)));
typedef __bf16 bf16x8 __attribute__((ext_vector_type(8)));
#define GAS __attribute__((address_space(1)))
#define LAS __attribute__((address_space(3)))
typedef GAS u64 gu64;

namespace {
constexpr int NWG = 256, NTHR = 640;
constexpr int FF = 1376;
constexpr u32 W_EPOCH = 0;
constexpr u32 W_Q1 = 1024;
constexpr u32 F1_ROW = 1408;
constexpr u32 W_F1 = W_Q1 + 32 * 512;
constexpr u32 W_O2 = W_F1 + 8 * F1_ROW;
constexpr u32 W_ST = W_O2 + 32 * 512;
constexpr u32 W_Y3 = W_ST + 32 * 128;
constexpr u32 W_END = W_Y3 + 32 * 512;
constexpr size_t W_DBG = 262144;
constexpr u32 LB_M = 0;
constexpr u32 M_CNT = LB_M;
constexpr u32 M_TAG = M_CNT + 48;
constexpr u32 M_G = M_TAG + 16;
constexpr u32 M_WM = M_G + 16;
constexpr u32 M_WL = M_WM + 32;
constexpr u32 M_F = M_WL + 32;
constexpr u32 M_Q = 256;
constexpr u32 M_P = M_Q + 256;
constexpr u32 M_O = M_P + 512;
constexpr u32 M_H = M_O + 512;
constexpr u32 M_X = 2048;
constexpr u32 M_GW = M_X + 2048;
constexpr u32 LB_W = 8192;
constexpr u32 LB_D = LB_W + 16384;
constexpr u32 LB_K = LB_D + 8192;
constexpr u32 LB_V = LB_K + 65536;
constexpr u32 LB_F = LB_V + 49152;
constexpr u32 C_LANDED = M_CNT, C_F = M_CNT + 4, C_QC = M_CNT + 8, C_FC = M_CNT + 12, C_Q = M_CNT + 16, C_M = M_CNT + 20, C_P = M_CNT + 24, C_H = M_CNT + 28,
              C_ISS = M_CNT + 32;
constexpr u32 LDS_BYTES = 163840;
constexpr int SPIN_MAX = 1 << 15, LSPIN_MAX = 1 << 21;
constexpr int CODE_BYTES = 13824;
constexpr float LOG2E = 1.4426950408889634f;
#define QSLEEP 14
#define G2SLEEP 1
#define G3SLEEP 1
#define WIN 16
#define WIN2 28
#define KN1 8
#define G3DUAL 0
#define FBLK 5
#define STBLK 4
#define G3FIRST 2

__device__ __forceinline__ u32 cvtpk(float lo, float hi) {
  const f32x2 v = {lo, hi};
  u32 r = __builtin_bit_cast(u32, __builtin_convertvector(v, bf16x2));
  asm("" : "+v"(r));
  return r;
}
__device__ __forceinline__ float max2(float a, float b) { return __builtin_amdgcn_fmed3f(a, b, __builtin_inff()); }
__device__ __forceinline__ float max3(float a, float b, float c) { return max2(max2(a, b), c); }
__device__ __forceinline__ float dot2(u32 a, u32 b, float c) {
  return __builtin_amdgcn_fdot2_f32_bf16(__builtin_bit_cast(bf16x2, a), __builtin_bit_cast(bf16x2, b), c, false);
}
__device__ __forceinline__ float bflo(u32 p) { return __uint_as_float(p << 16); }
__device__ __forceinline__ float bfhi(u32 p) { return __uint_as_float(p & 0xffff0000u); }
__device__ __forceinline__ float rbf(float x) { return bflo(cvtpk(x, x)); }
template <int CTRL> __device__ __forceinline__ float dpp(float x) {
  return __builtin_bit_cast(float, __builtin_amdgcn_mov_dpp(__builtin_bit_cast(int, x), CTRL, 0xf, 0xf, true));
}
constexpr int XOR1 = 0xB1, XOR2 = 0x4E, ROR8 = 0x128, ROR4 = 0x124, ROR2 = 0x122, ROR1 = 0x121;
__device__ __forceinline__ float swap32_add(float a, float b) {
  auto t = __builtin_amdgcn_permlane32_swap(__float_as_uint(a), __float_as_uint(b), false, false);
  return __uint_as_float(t[0]) + __uint_as_float(t[1]);
}
__device__ __forceinline__ float swap16_add(float a, float b) {
  auto t = __builtin_amdgcn_permlane16_swap(__float_as_uint(a), __float_as_uint(b), false, false);
  return __uint_as_float(t[0]) + __uint_as_float(t[1]);
}
__device__ __forceinline__ float xrow_max(float x) {
  auto s = __builtin_amdgcn_permlane16_swap(__float_as_uint(x), __float_as_uint(x), false, false);
  x = max2(__uint_as_float(s[0]), __uint_as_float(s[1]));
  auto t = __builtin_amdgcn_permlane32_swap(__float_as_uint(x), __float_as_uint(x), false, false);
  return max2(__uint_as_float(t[0]), __uint_as_float(t[1]));
}
__device__ __forceinline__ float xrow_sum(float x) { x = swap16_add(x, x); return swap32_add(x, x); }
__device__ __forceinline__ u64 ldg(const u64* base, u32 i) { return __hip_atomic_load((const gu64*)((const GAS char*)base + 8u * i), __ATOMIC_RELAXED, __HIP_MEMORY_SCOPE_AGENT); }
__device__ __forceinline__ void stg(u64* base, u32 i, u64 v) { __hip_atomic_store((gu64*)((GAS char*)base + 8u * i), v, __ATOMIC_RELAXED, __HIP_MEMORY_SCOPE_AGENT); }
struct Tag { u32 a, b; };
__device__ __forceinline__ u64 gpack(float v0, float v1, Tag t) {
  const u32 lo = ((__float_as_uint(v0) + 0x200u) & 0xfffffc00u) | t.a, hi = ((__float_as_uint(v1) + 0x200u) & 0xfffffc00u) | t.b;
  return ((u64)hi << 32) | lo;
}
__device__ __forceinline__ float gval0(u64 g) { return __uint_as_float((u32)g & 0xfffffc00u); }
__device__ __forceinline__ float gval1(u64 g) { return __uint_as_float((u32)(g >> 32) & 0xfffffc00u); }
__device__ __forceinline__ u32 gbad(u64 g, Tag t) { return (((u32)g ^ t.a) | ((u32)(g >> 32) ^ t.b)) & 0x3ffu; }

template <typename T> __device__ __forceinline__ LAS T* lp(u32 a) { return (LAS T*)(size_t)a; }
__device__ __forceinline__ bf16x8 ld_frag(u32 a) { return __builtin_bit_cast(bf16x8, *lp<u32x4>(a)); }
__device__ __forceinline__ bf16x8 ld_tr(u32 a0, u32 a1) {
  const s16x4 lo = __builtin_amdgcn_ds_read_tr16_b64_v4i16(lp<s16x4>(a0)), hi = __builtin_amdgcn_ds_read_tr16_b64_v4i16(lp<s16x4>(a1));
  return __builtin_bit_cast(bf16x8, __builtin_shufflevector(lo, hi, 0, 1, 2, 3, 4, 5, 6, 7));
}
__device__ __forceinline__ f32x4 mma(bf16x8 a, bf16x8 b, f32x4 c) { return __builtin_amdgcn_mfma_f32_16x16x32_bf16(a, b, c, 0, 0, 0); }
__device__ __forceinline__ void lds_arrive(u32 c) {
  asm volatile("s_waitcnt lgkmcnt(0)\n\ts_mov_b64 exec, 1\n\tds_add_u32 %0, %1\n\ts_mov_b64 exec, -1" :: "v"(c), "v"(1u) : "memory");
}
__device__ __forceinline__ void lds_wait(u32 c, u32 n, u32& seen) {
  u32 v, cnt = LSPIN_MAX;
  asm volatile("s_cmp_ge_u32 %1, %4\n\ts_cbranch_scc1 2f\n"
               "1:\n\tds_read_b32 %0, %3\n\ts_waitcnt lgkmcnt(0)\n\tv_readfirstlane_b32 %1, %0\n\ts_cmp_ge_u32 %1, %4\n\ts_cbranch_scc1 2f\n\t"
               "s_sleep 1\n\ts_sub_u32 %2, %2, 1\n\ts_cbranch_scc0 1b\n2:"
               : "=&v"(v), "+&s"(seen), "+&s"(cnt) : "v"(c), "s"(n) : "memory", "scc");
}
__device__ __forceinline__ void lds_wait(u32 c, u32 n) { u32 seen = 0; lds_wait(c, n, seen); }
#define WG_BARRIER(id) asm volatile("s_waitcnt lgkmcnt(0)\n\ts_barrier ; " id ::: "memory")
#define STR2(x) #x
#define STR(x) STR2(x)
#define DMA_THROTTLE "s_waitcnt vmcnt(%[win])\n\tds_write_b32 %[vz], %[prog]\n\t"
#define DMA_TILE_BODY(THR) \
  "s_mov_b32 m0, %[dst]\n\ts_nop 0\n1:\n\t" THR \
  "global_load_lds_dwordx4 %[v0], %[base] nt\n\ts_add_u32 m0, m0, 0x400\n\tv_add_u32 %[v0], %[step], %[v0]\n\t" \
  "global_load_lds_dwordx4 %[v1], %[base] nt\n\ts_add_u32 m0, m0, 0x400\n\tv_add_u32 %[v1], %[step], %[v1]\n\t" \
  "global_load_lds_dwordx4 %[v0], %[base] nt\n\ts_add_u32 m0, m0, 0x400\n\tv_add_u32 %[v0], %[step], %[v0]\n\t" \
  "global_load_lds_dwordx4 %[v1], %[base] nt\n\ts_add_u32 m0, m0, 0x400\n\tv_add_u32 %[v1], %[step], %[v1]\n\t" \
  "v_add_u32 %[prog], 4, %[prog]\n\ts_sub_u32 %[cnt], %[cnt], 1\n\ts_cbranch_scc0 1b"
template <int W>
__device__ __forceinline__ void dma_tile(u32 dst, const void* base, u32 v0, u32 v1, u32 step, u32 groups, u32& prog) {
  u32 cnt = groups - 1;
  if constexpr (W > 0)
    asm volatile(DMA_TILE_BODY(DMA_THROTTLE) : [v0] "+&v"(v0), [v1] "+&v"(v1), [prog] "+&v"(prog), [cnt] "+&s"(cnt)
                 : [dst] "s"(dst), [base] "s"(base), [step] "s"(step), [vz] "v"(0u), [win] "n"(W) : "memory", "scc");
  else
    asm volatile(DMA_TILE_BODY("") : [v0] "+&v"(v0), [v1] "+&v"(v1), [prog] "+&v"(prog), [cnt] "+&s"(cnt)
                 : [dst] "s"(dst), [base] "s"(base), [step] "s"(step) : "memory", "scc");
}
template <int W>
__device__ __forceinline__ void dma_two(u32 dst, const void* base, u32 v0, u32 v1, u32& prog) {
  asm volatile("s_mov_b32 m0, %[dst]\n\t" DMA_THROTTLE "global_load_lds_dwordx4 %[v0], %[base] nt\n\ts_add_u32 m0, m0, 0x400\n\tv_add_u32 %[prog], 2, %[prog]\n\t"
               "global_load_lds_dwordx4 %[v1], %[base] nt"
               : [prog] "+&v"(prog) : [dst] "s"(dst), [base] "s"(base), [v0] "v"(v0), [v1] "v"(v1), [vz] "v"(0u), [win] "n"(W) : "memory", "scc");
}
#define DMA_ROWS_BODY(THR) \
  "s_mov_b32 m0, %[dst]\n\ts_nop 0\n1:\n\t" THR \
  "global_load_lds_dwordx4 %[v0], %[base] nt\n\tglobal_load_lds_dwordx4 %[v1], %[base] offset:1024 nt\n\t" \
  "global_load_lds_dwordx4 %[v2], %[base] offset:2048 nt\n\tglobal_load_lds_dwordx4 %[v3], %[base] offset:3072 nt\n\t" \
  "s_add_u32 m0, m0, 0x1000\n\tv_add_u32 %[v0], 0x1000, %[v0]\n\tv_add_u32 %[v1], 0x1000, %[v1]\n\tv_add_u32 %[v2], 0x1000, %[v2]\n\tv_add_u32 %[v3], 0x1000, %[v3]\n\t" \
  "v_add_u32 %[prog], 4, %[prog]\n\ts_sub_u32 %[cnt], %[cnt], 1\n\ts_cbranch_scc0 1b"
template <int W>
__device__ __forceinline__ void dma_rows(u32 dst, const void* base, u32& v0, u32& v1, u32& v2, u32& v3, u32 groups, u32& prog) {
  u32 cnt = groups - 1;
  if constexpr (W > 0)
    asm volatile(DMA_ROWS_BODY(DMA_THROTTLE) : [v0] "+&v"(v0), [v1] "+&v"(v1), [v2] "+&v"(v2), [v3] "+&v"(v3), [prog] "+&v"(prog), [cnt] "+&s"(cnt)
                 : [dst] "s"(dst), [base] "s"(base), [vz] "v"(0u), [win] "n"(W) : "memory", "scc");
  else
    asm volatile(DMA_ROWS_BODY("") : [v0] "+&v"(v0), [v1] "+&v"(v1), [v2] "+&v"(v2), [v3] "+&v"(v3), [prog] "+&v"(prog), [cnt] "+&s"(cnt)
                 : [dst] "s"(dst), [base] "s"(base) : "memory", "scc");
}

#define STAMP(i)
#define LSTAMP(i)
#define STAMP_DECL
#define STAMP_OUT(slot)
#define COUNT(i)
#define STAMP_PAR
#define STAMP_ARG

__device__ __forceinline__ void final_reduce(const u64* ws, u16* out, int c, int qd, int lane, Tag tag, int first STAMP_PAR) {
  const u64* src = ws + W_Y3 + (c * 4 + qd) * 512;
  u64 gg[8];
  for (int i = 0; i < first; ++i) __builtin_amdgcn_s_sleep(1);
#pragma unroll
  for (int i = 0; i < 8; ++i) gg[i] = ldg(src, 64 * i + lane);
#pragma unroll 1
  for (int spin = 0; spin < SPIN_MAX; ++spin) {
    bool again = false;
#pragma unroll
    for (int i = 0; i < 8; ++i)
      if (__any(gbad(gg[i], tag) != 0)) { gg[i] = ldg(src, 64 * i + lane); again = true; }
    if (!again) break;
    COUNT(15);
  }
  const float v0 = ((gval0(gg[0]) + gval0(gg[1])) + (gval0(gg[2]) + gval0(gg[3]))) + ((gval0(gg[4]) + gval0(gg[5])) + (gval0(gg[6]) + gval0(gg[7])));
  const float v1 = ((gval1(gg[0]) + gval1(gg[1])) + (gval1(gg[2]) + gval1(gg[3]))) + ((gval1(gg[4]) + gval1(gg[5])) + (gval1(gg[6]) + gval1(gg[7])));
  float v = swap16_add(v0, v1);
  v = swap32_add(v, v);
  const u32 o2 = cvtpk(v, dpp<XOR1>(v));
  if (lane < 32 && !(lane & 1)) *(u32*)(out + (lane >> 4) * 512 + 64 * c + 16 * qd + (lane & 15)) = o2;
  LSTAMP(12);
}

__global__ void __launch_bounds__(NTHR, 3)
fused_decode(const u16* __restrict__ x, const u16* __restrict__ Kc, const u16* __restrict__ Vc, const u16* __restrict__ wq,
             const u16* __restrict__ wo, const u16* __restrict__ wg, const u16* __restrict__ wu, const u16* __restrict__ wd,
             const u16* __restrict__ gw, u16* __restrict__ out, u64* __restrict__ ws) {
  __shared__ __attribute__((aligned(1024))) unsigned char lds_all[LDS_BYTES];
  asm volatile("" :: "v"((LAS unsigned char*)lds_all));
  STAMP_DECL;
  LSTAMP(0);
  asm volatile("" :: "s"(x), "s"(Kc), "s"(Vc), "s"(wq), "s"(wo), "s"(wg), "s"(wu), "s"(wd), "s"(gw), "s"(out), "s"(ws));
  const int tid = threadIdx.x, lane = tid & 63;
  const int wid = __builtin_amdgcn_readfirstlane(tid >> 6);
  const int wgid = blockIdx.x, xcd = wgid & 7, idx = wgid >> 3;
  const int hh = idx >> 3, s = idx & 7, h = xcd * 4 + hh;
  const int rq = s >> 1, par = s & 1;
  const int b = s & 1, j = s >> 1;
  const u32 r8 = lane >> 3, r4 = lane >> 4;
  const u32 sw0 = 16u * ((lane & 7) ^ (r8 & 2));
  const size_t kvoff = ((size_t)(b * 32 + h) * 4096 + 3072 + j * 256) * 256;
  u32 k0 = r4 * 256 + 16 * ((lane & 15) ^ r4), k1 = k0 ^ 64, k2 = k0 ^ 128, k3 = k0 ^ 192;
  u32 prog = 28 - WIN;
  if (wid == 0) {
    STAMP(6);
    __builtin_amdgcn_s_setprio(2);
    if (lane < 12) *lp<u32>(M_CNT + 4 * lane) = 0;
    asm volatile("s_mov_b64 exec, 1\n\ts_mov_b32 m0, %0\n\ts_nop 0\n\tglobal_load_lds_dwordx4 %1, %2\n\ts_mov_b64 exec, -1\n\t"
                 "s_mov_b32 m0, %3\n\ts_nop 0\n\tglobal_load_lds_dwordx4 %4, %5\n\tglobal_load_lds_dwordx4 %4, %5 offset:1024\n\t"
                 "s_mov_b32 m0, %6\n\ts_nop 0\n\tglobal_load_lds_dwordx4 %4, %7"
                 :: "s"(M_TAG), "v"(0u), "s"(ws + W_EPOCH), "s"(M_X), "v"(16u * lane), "s"(x), "s"(M_GW), "s"(gw) : "memory");
    STAMP(7);
    u32 dummy = 0;
    dma_tile<0>(LB_W, (const char*)wq + ((size_t)rq << 20) + 256 * h + 128 * par, r8 * 8192 + sw0, r8 * 8192 + (sw0 ^ 64) + 65536, 131072, 4, dummy);
    STAMP(8);
    dma_rows<0>(LB_K, (const char*)Kc + kvoff, k0, k1, k2, k3, 2, dummy);
  }
  u32 tv = 0;
  if (wid == 9) {
    const char* p = lane == 0 ? (const char*)Vc + kvoff : lane == 1 ? (const char*)(wo + (size_t)128 * h * 512) : lane == 2 ? (const char*)(wd + (size_t)43 * h * 512)
                  : lane == 3 ? (const char*)wg : lane == 4 ? (const char*)wu : (const char*)Vc + kvoff + 65535;
    if (lane < 6) asm volatile("global_load_ubyte %0, %1, off nt" : "=v"(tv) : "v"(p) : "memory");
  }
  u32 pf0;
  {
    u64 pc;
    asm volatile("s_getpc_b64 %0" : "=s"(pc));
    const u32 off = min((idx * 4 + (lane & 3)) * 128u, (u32)CODE_BYTES - 128u);
    pc &= ~127ull;
    if (tid >= 64 && tid < 68) asm volatile("global_load_dword %0, %1, %2" : "=v"(pf0) : "v"(off), "s"(pc) : "memory");
  }
  WG_BARRIER("B0");
  const int fgr = (int)((0x46573210u >> (4 * xcd)) & 15u) * 4 + hh;
  const bool ffn = fgr < 22;
  const int fg = ffn ? fgr : 0;
  const u32 fo = ffn ? 16u : 0u;
  int role = wid;
  asm volatile("" : "+s"(role));
  if (role == 0) {
    STAMP(1);
    const u32 sw1 = sw0 ^ 64;
    if (ffn)
      dma_tile<WIN>(LB_F, (const char*)((s & 1) ? wu : wg) + (size_t)(256 * (s >> 2) + ((s >> 1) & 1)) * (2 * FF) + 128 * fg - 64 * ((s >> 1) & 1),
                     r8 * (4 * FF) + sw0, r8 * (4 * FF) + sw1 + 8 * 4 * FF, 16 * 4 * FF, 4, prog);
    dma_rows<WIN>(LB_K + 8192, (const char*)Kc + kvoff, k0, k1, k2, k3, KN1, prog);
    prog -= WIN2 - WIN;
    dma_rows<WIN2>(LB_K + 8192 + 4096 * KN1, (const char*)Kc + kvoff, k0, k1, k2, k3, 14 - KN1, prog);
    STAMP(2);
    {
      u32 v0 = r4 * 256 + 16 * ((lane & 15) ^ (r4 << 1)), v1 = v0 ^ 128, v2 = v0, v3 = v1;
      dma_rows<WIN2>(LB_V, (const char*)Vc + kvoff, v0, v1, v2, v3, 12, prog);
      if (ffn) lds_wait(C_FC, 4);
      dma_rows<WIN2>(LB_V + 49152, (const char*)Vc + kvoff, v0, v1, v2, v3, 4, prog);
    }
    STAMP(3);
    {
      const char* base = (const char*)(wd + (size_t)43 * h * 512 + 64 * s);
      dma_tile<WIN2>(LB_D, base, r8 * 1024 + sw0, r8 * 1024 + sw1 + 8192, 16384, 1, prog);
      const u32 rmax = FF - 1 - 43 * h;
      dma_two<WIN2>(LB_D + 4096, base, min(32 + r8, rmax) * 1024 + sw0, min(40 + r8, rmax) * 1024 + sw1, prog);
    }
    lds_wait(C_QC, 4);
    dma_tile<WIN2>(LB_W, (const char*)(wo + (size_t)128 * h * 512 + 64 * s), r8 * 1024 + sw0, r8 * 1024 + sw1 + 8192, 16384, 4, prog);
    *(volatile LAS u32*)(size_t)C_ISS = 1;
    STAMP(4);
#define DRAIN(k) "s_waitcnt vmcnt(" #k ")\n\tv_add_u32 %1, " STR(WIN2) " - " #k ", %0\n\tds_write_b32 %2, %1\n\t"
    u32 t;
    asm volatile(DRAIN(28) DRAIN(24) DRAIN(20) DRAIN(16) DRAIN(12) DRAIN(8) DRAIN(4) DRAIN(0) "s_waitcnt lgkmcnt(0)" : "+v"(prog), "=&v"(t) : "v"(0u) : "memory");
    LSTAMP(5);
    STAMP_OUT(8);
    return;
  }
  const int g = lane >> 4, n16 = lane & 15, q4 = n16 >> 2, p4 = lane & 3;
  u32 landed = 0;
  if (role == 9) {
    const u32x4 z4 = {0u, 0u, 0u, 0u};
    *lp<u32x4>(LB_D + 6144 + 16 * lane) = z4; *lp<u32x4>(LB_D + 7168 + 16 * lane) = z4;
    if (lane < 16) *lp<u32x4>(M_H + 16 * lane) = z4;
  }
  lds_wait(C_LANDED, 4, landed);
  const u64 ep = *lp<u64>(M_TAG) + 1;
  const Tag tag = {(u32)ep & 0x3ffu, (u32)(ep >> 10) & 0x3ffu};

  if (role == 9) {
    asm volatile("s_waitcnt vmcnt(0)" : "+v"(tv) :: "memory");
    {
      const u32x4 gv = *lp<u32x4>(M_GW + 16 * lane), x0 = *lp<u32x4>(M_X + 16 * lane), x1 = *lp<u32x4>(M_X + 1024 + 16 * lane);
      float z0 = 0.f, z1 = 0.f;
#pragma unroll
      for (int t = 0; t < 4; ++t) {
        const float gl = bflo(gv[t]), gh = bfhi(gv[t]);
        z0 = dot2(cvtpk(bflo(x0[t]) * gl, bfhi(x0[t]) * gh), 0x3f803f80u, z0);
        z1 = dot2(cvtpk(bflo(x1[t]) * gl, bfhi(x1[t]) * gh), 0x3f803f80u, z1);
      }
      float z = swap32_add(z0, z1);
      z = swap16_add(z, z);
      z += dpp<ROR8>(z); z += dpp<ROR4>(z); z += dpp<ROR2>(z); z += dpp<ROR1>(z);
      const float gt = rbf(__builtin_amdgcn_rcpf(1.f + __expf(-rbf(z))));
      if ((lane & 31) == 0) { *lp<float>(M_G + (lane >> 5) * 8) = gt; *lp<float>(M_G + (lane >> 5) * 8 + 4) = rbf(1.f - gt); }
    }
    STAMP(1);
    lds_wait(C_LANDED, 28 + fo + 8 * FBLK, landed);
    const u32 fb = (43 * h) & ~15u, fe = fb + lane, fo2 = min(fb + 32 + lane, F1_ROW - 1);
    u64 fp[8];
#pragma unroll
    for (int i = 0; i < 8; ++i) fp[i] = ldg(ws + W_F1 + F1_ROW * i, (i & 2) ? fo2 : fe);
    lds_wait(C_P, 8);
    {
      const f32x4 a = *lp<f32x4>(M_WM), c = *lp<f32x4>(M_WM + 16), la = *lp<f32x4>(M_WL), lc = *lp<f32x4>(M_WL + 16);
      const float m = max3(max3(a[0], a[1], a[2]), max3(a[3], c[0], c[1]), max3(c[2], c[3], c[3]));
      const float L = ((la[0] + la[1]) + (la[2] + la[3])) + ((lc[0] + lc[1]) + (lc[2] + lc[3]));
      stg(ws + W_ST + ((h * 2 + b) * 4 + j) * 16, n16, gpack(m, L, tag));
    }
    STAMP(2);
    {
      u32 bad = 0;
#pragma unroll
      for (int i = 0; i < 8; ++i) bad |= gbad(fp[i], tag);
      if (__builtin_expect(__any(bad != 0), 0)) {
#pragma unroll 1
        for (int spin = 0; spin < SPIN_MAX; ++spin) {
          COUNT(14);
          bad = 0;
#pragma unroll
          for (int i = 0; i < 8; ++i) fp[i] = ldg(ws + W_F1 + F1_ROW * i, (i & 2) ? fo2 : fe);
#pragma unroll
          for (int i = 0; i < 8; ++i) bad |= gbad(fp[i], tag);
          if (!__any(bad != 0)) break;
        }
      }
      const u32 gp = cvtpk((gval0(fp[0]) + gval0(fp[1])) + (gval0(fp[2]) + gval0(fp[3])), (gval1(fp[0]) + gval1(fp[1])) + (gval1(fp[2]) + gval1(fp[3])));
      const u32 up = cvtpk((gval0(fp[4]) + gval0(fp[5])) + (gval0(fp[6]) + gval0(fp[7])), (gval1(fp[4]) + gval1(fp[5])) + (gval1(fp[6]) + gval1(fp[7])));
      const u32 hp = cvtpk(bflo(gp) * bflo(up), bfhi(gp) * bfhi(up));
      const u32 i = fe - 43 * h;
      if (i < 43) { *lp<u16>(M_H + 2 * i) = (u16)hp; *lp<u16>(M_H + 128 + 2 * i) = (u16)(hp >> 16); }
      lds_arrive(C_H);
    }
    STAMP(3);
    lds_wait(C_LANDED, 92 + fo + 8 * STBLK, landed);
    {
      const u64* src = ws + W_ST + h * 128;
      u64 s0, s1;
#pragma unroll 1
      for (int spin = 0; spin < SPIN_MAX; ++spin) {
        s0 = ldg(src, lane); s1 = ldg(src, 64 + lane);
        if (!__any((gbad(s0, tag) | gbad(s1, tag)) != 0)) break;
        COUNT(13);
      }
      const u64 sb = (lane & 8) ? s1 : s0;
      const float mj = gval0(sb);
      const float w = __builtin_amdgcn_exp2f((mj - xrow_max(mj)) * LOG2E);
      const float f = w * __builtin_amdgcn_rcpf(xrow_sum(w * gval1(sb)));
      if ((lane & 7) == 0) *lp<float>(M_F + 2 * (lane & 8) + 4 * g) = f;
      lds_arrive(C_F);
    }
    STAMP(4);
    WG_BARRIER("B1 helper");
    if (wgid == 68) {
      const u32 i0 = (lane >> 3) * 512 + ((lane >> 1) & 3) * 64 + (lane & 1) * 32;
#pragma unroll 1
      for (int spin = 0; spin < SPIN_MAX; ++spin) {
        u32 bad = 0;
#pragma unroll
        for (int i = 0; i < 4; ++i) bad |= gbad(ldg(ws + W_Q1 + 4096 * i, i0), tag);
        if (!__any(bad != 0)) { if (lane == 0) stg(ws + W_EPOCH, 0, ep); break; }
        __builtin_amdgcn_s_sleep(8);
      }
    }
    if (hh == 0 && s < 4) final_reduce(ws, out, xcd, s, lane, tag, G3FIRST STAMP_ARG);
    STAMP_OUT(9);
    return;
  }

  const int wave = role - 1, ct = wave & 3;
  const int bm = n16 & 1;
  STAMP(1);
  const u32 tile_a = 128 * (8 * g + q4) + 16 * ((2 * ct + (p4 >> 1)) ^ (((q4 >> 1) | ((g & 1) << 1)) << 1)) + 8 * (p4 & 1);
  if (wave < 4) {
    const u32 xa = M_X + 1024 * bm + 256 * rq + 16 * g;
    bf16x8 xf[4];
#pragma unroll
    for (int ks = 0; ks < 4; ++ks) xf[ks] = ld_frag(xa + 64 * ks);
    lds_wait(C_LANDED, 20, landed);
    f32x4 acc = {0.f, 0.f, 0.f, 0.f};
#pragma unroll
    for (int ks = 0; ks < 4; ++ks) acc = mma(xf[ks], ld_tr(LB_W + tile_a + 4096 * ks, LB_W + tile_a + 4096 * ks + 512), acc);
    {
      const float n0 = dpp<XOR1>(acc[0]), n1 = dpp<XOR1>(acc[1]);
      if (lane < 16) stg(ws + W_Q1 + h * 512 + rq * 64 + 32 * par + 8 * ct, 256 * bm + (n16 >> 1), gpack(bm ? n1 : acc[0], bm ? acc[1] : n0, tag));
    }
    lds_arrive(C_QC);
    STAMP(2);
    __builtin_amdgcn_s_sleep(QSLEEP);
    const u64* src = ws + W_Q1 + h * 512 + b * 256 + 16 * wave;
    u64 g0;
#pragma unroll 1
    for (int spin = 0; spin < SPIN_MAX; ++spin) {
      g0 = ldg(src, 64 * g + n16);
      if (!__any(gbad(g0, tag) != 0)) break;
      COUNT(13);
    }
    const u32 qq = cvtpk(xrow_sum(gval0(g0)), xrow_sum(gval1(g0)));
    if (lane < 16) *lp<u32>(M_Q + 4 * (16 * wave + lane)) = qq;
    lds_arrive(C_Q);
  } else if (ffn) {
    const u32 xa = M_X + 1024 * bm + 512 * (s >> 2) + 32 * g, sel = (s & 2) ? 0x07060302u : 0x05040100u;
    u32x4 xf[4];
#pragma unroll
    for (int ks = 0; ks < 4; ++ks) {
      const u32x4 lo = *lp<u32x4>(xa + 128 * ks), hi = *lp<u32x4>(xa + 128 * ks + 16);
      xf[ks] = u32x4{__builtin_amdgcn_perm(lo[1], lo[0], sel), __builtin_amdgcn_perm(lo[3], lo[2], sel),
                     __builtin_amdgcn_perm(hi[1], hi[0], sel), __builtin_amdgcn_perm(hi[3], hi[2], sel)};
    }
    lds_wait(C_LANDED, 44, landed);
    f32x4 acc = {0.f, 0.f, 0.f, 0.f};
#pragma unroll
    for (int ks = 0; ks < 4; ++ks) acc = mma(__builtin_bit_cast(bf16x8, xf[ks]), ld_tr(LB_F + tile_a + 4096 * ks, LB_F + tile_a + 4096 * ks + 512), acc);
    if (lane < 16) stg(ws + W_F1 + ((s & 1) * 4 + (s & 2) + (s >> 2)) * F1_ROW + 64 * fg + 16 * ct, lane, gpack(acc[0], acc[1], tag));
    lds_arrive(C_FC);
    STAMP(2);
  }
  if (wave == 0) asm volatile("" : "+v"(pf0));
  STAMP(3);
  lds_wait(C_LANDED, 28 + 8 * wave + (wave >= 1 ? fo : 0u), landed);
  lds_wait(C_Q, 4);
  STAMP(4);
  float sc[8];
  {
    const u32 ka = LB_K + 256 * (32 * wave + n16) + 16 * (g ^ n16);
    f32x4 d0 = {0.f, 0.f, 0.f, 0.f}, d1 = {0.f, 0.f, 0.f, 0.f};
#pragma unroll
    for (int ks = 0; ks < 4; ++ks) {
      const bf16x8 qf = ld_frag(M_Q + 64 * ks + 16 * g);
      d0 = mma(ld_frag(ka ^ (64 * ks)), qf, d0);
      d1 = mma(ld_frag((ka ^ (64 * ks)) + 4096), qf, d1);
    }
#pragma unroll
    for (int i = 0; i < 4; i += 2) {
      const u32 a = cvtpk(d0[i], d0[i + 1]), c = cvtpk(d1[i], d1[i + 1]);
      const u32 a2 = cvtpk(bflo(a) * 0.08838834764831845f, bfhi(a) * 0.08838834764831845f);
      const u32 c2 = cvtpk(bflo(c) * 0.08838834764831845f, bfhi(c) * 0.08838834764831845f);
      sc[i] = bflo(a2); sc[i + 1] = bfhi(a2); sc[4 + i] = bflo(c2); sc[5 + i] = bfhi(c2);
    }
  }
  {
    float m = xrow_max(max3(max3(sc[0], sc[1], sc[2]), max3(sc[3], sc[4], sc[5]), max3(sc[6], sc[7], sc[7])));
    if (lane == 0) *lp<float>(M_WM + 4 * wave) = m;
    lds_arrive(C_M);
    lds_wait(C_M, 8);
    STAMP(5);
    const f32x4 a = *lp<f32x4>(M_WM), c = *lp<f32x4>(M_WM + 16);
    m = max3(max3(a[0], a[1], a[2]), max3(a[3], c[0], c[1]), max3(c[2], c[3], c[3]));
    const float mL = m * LOG2E;
    float p[8], l = 0.f;
#pragma unroll
    for (int i = 0; i < 8; ++i) { p[i] = __builtin_amdgcn_exp2f(fmaf(sc[i], LOG2E, -mL)); l += p[i]; }
    l = xrow_sum(l);
    const u32x4 pf = {cvtpk(p[0], p[1]), cvtpk(p[2], p[3]), cvtpk(p[4], p[5]), cvtpk(p[6], p[7])};
    if (n16 == 0) *lp<u32x4>(M_P + 64 * wave + 16 * g) = pf;
    if (lane == 0) *lp<float>(M_WL + 4 * wave) = l;
  }
  lds_arrive(C_P);
  lds_wait(C_P, 8);
  STAMP(6);
  f32x4 oacc = {0.f, 0.f, 0.f, 0.f};
  {
    const u32 va = LB_V + 256 * (4 * g + q4) + 16 * ((2 * wave + (p4 >> 1)) ^ ((4 * (g & 1) + q4) << 1)) + 8 * (p4 & 1);
    bf16x8 pfr[8];
#pragma unroll
    for (int t = 0; t < 8; ++t) pfr[t] = ld_frag(M_P + 64 * t + 16 * g);
    lds_wait(C_LANDED, 92 + fo + 8 * 5, landed);
#pragma unroll
    for (int t = 0; t < 6; ++t) oacc = mma(pfr[t], ld_tr(va + 8192 * t, va + 8192 * t + 4096), oacc);
    lds_wait(C_LANDED, 92 + fo + 8 * 6, landed);
    oacc = mma(pfr[6], ld_tr(va + 8192 * 6, va + 8192 * 6 + 4096), oacc);
    lds_wait(C_LANDED, 92 + fo + 8 * 7, landed);
    oacc = mma(pfr[7], ld_tr(va + 8192 * 7, va + 8192 * 7 + 4096), oacc);
  }
  {
    const float on = dpp<XOR1>(oacc[0]);
    if (lane < 16 && !(lane & 1)) stg(ws + W_O2 + ((h * 2 + b) * 4 + j) * 64 + 8 * wave, lane >> 1, gpack(oacc[0], on, tag));
  }
  STAMP(7);
  const u64* vs = ws + W_O2 + h * 512 + (wave & 1) * 256 + 16 * (wave >> 1);
  const u32 vi = 64 * p4 + (lane >> 2);
  lds_wait(C_ISS, 1);
  u64 gg = ldg(vs, vi);
  bf16x8 wof[4];
  float yb0 = 0.f, yb1 = 0.f;
  f32x4 gates;
  if (wave < 4) {
    lds_wait(C_H, 1);
    gates = *lp<f32x4>(M_G);
    lds_wait(C_LANDED, 154 + fo, landed);
    f32x4 acc = {0.f, 0.f, 0.f, 0.f};
#pragma unroll
    for (int ks = 0; ks < 2; ++ks) acc = mma(ld_frag(M_H + 128 * bm + 64 * ks + 16 * g), ld_tr(LB_D + tile_a + 4096 * ks, LB_D + tile_a + 4096 * ks + 512), acc);
    yb0 = gates[1] * acc[0]; yb1 = gates[3] * acc[1];
    lds_wait(C_LANDED, 170 + fo, landed);
#pragma unroll
    for (int ks = 0; ks < 4; ++ks) wof[ks] = ld_tr(LB_W + tile_a + 4096 * ks, LB_W + tile_a + 4096 * ks + 512);
  }
  lds_wait(C_F, 1);
  const float fq = *lp<float>(M_F + 16 * (wave & 1) + 4 * p4);
  STAMP(8);
  if (__builtin_expect(__any(gbad(gg, tag) != 0), 0)) {
#pragma unroll 1
    for (int spin = 0; spin < SPIN_MAX; ++spin) {
      COUNT(15);
      gg = ldg(vs, vi);
      if (!__any(gbad(gg, tag) != 0)) break;
      __builtin_amdgcn_s_sleep(G2SLEEP);
    }
  }
  STAMP(9);
  {
    float t0 = fq * gval0(gg), t1 = fq * gval1(gg);
    t0 += dpp<XOR1>(t0); t1 += dpp<XOR1>(t1); t0 += dpp<XOR2>(t0); t1 += dpp<XOR2>(t1);
    const u32 ob = cvtpk(t0, t1);
    if (p4 == 0) *lp<u32>(M_O + 256 * (wave & 1) + 64 * (wave >> 1) + lane) = ob;
  }
  WG_BARRIER("B1");
  STAMP(10);
  if (wave < 4) {
    f32x4 acc = {0.f, 0.f, 0.f, 0.f};
#pragma unroll
    for (int ks = 0; ks < 4; ++ks) acc = mma(ld_frag(M_O + 256 * bm + 64 * ks + 16 * g), wof[ks], acc);
    if (lane < 16) stg(ws + W_Y3 + ((s * 4 + ct) * 32 + h) * 16, lane, gpack(fmaf(gates[0], acc[0], yb0), fmaf(gates[2], acc[1], yb1), tag));
    STAMP(11);
  }
  STAMP_OUT(wave);
}
}

extern "C" void kernel_launch(void* const* d_in, const int* in_sizes, int n_in, void* d_out, int out_size, void* d_ws, size_t ws_size,
                              hipStream_t stream) {
  fused_decode<<<NWG, NTHR, 0, stream>>>((const u16*)d_in[0], (const u16*)d_in[1], (const u16*)d_in[2], (const u16*)d_in[3],
                                         (const u16*)d_in[4], (const u16*)d_in[6], (const u16*)d_in[7], (const u16*)d_in[8],
                                         (const u16*)d_in[9], (u16*)d_out, (u64*)d_ws);
}
```

```cpp
#include <hip/hip_runtime.h>
#include <stdint.h>

typedef unsigned short u16;
typedef unsigned int u32;
typedef unsigned long long u64;
typedef u32 u32x4 __attribute__((ext_vector_type(4)));
typedef short s16x4 __attribute__((ext_vector_type(4)));
typedef short s16x8 __attribute__((ext_vector_type(8)));
typedef float f32x2 __attribute__((ext_vector_type(2)));
typedef float f32x4 __attribute__((ext_vector_type(4)));
typedef __bf16 bf16x2 __attribute__((ext_vector_type(2)));
typedef __bf16 bf16x8 __attribute__((ext_vector_type(8)));
#define GAS __attribute__((address_space(1)))
#define LAS __attribute__((address_space(3)))
typedef GAS u64 gu64;

namespace {
constexpr int NWG = 256, NTHR = 640;
constexpr int FF = 1376;
constexpr u32 W_EPOCH = 0;
constexpr u32 W_Q1 = 1024;
constexpr u32 F1_ROW = 1408;
constexpr u32 W_F1 = W_Q1 + 32 * 512;
constexpr u32 W_O2 = W_F1 + 8 * F1_ROW;
constexpr u32 W_ST = W_O2 + 32 * 512;
constexpr u32 W_Y3 = W_ST + 32 * 128;
constexpr u32 W_END = W_Y3 + 32 * 512;
constexpr size_t W_DBG = 262144;
constexpr u32 LB_M = 0;
constexpr u32 M_CNT = LB_M;
constexpr u32 M_TAG = M_CNT + 48;
constexpr u32 M_G = M_TAG + 16;
constexpr u32 M_WM = M_G + 16;
constexpr u32 M_WL = M_WM + 32;
constexpr u32 M_F = M_WL + 32;
constexpr u32 M_Q = 256;
constexpr u32 M_P = M_Q + 256;
constexpr u32 M_O = M_P + 512;
constexpr u32 M_H = M_O + 512;
constexpr u32 M_X = 2048;
constexpr u32 M_GW = M_X + 2048;
constexpr u32 LB_W = 8192;
constexpr u32 LB_D = LB_W + 16384;
constexpr u32 LB_K = LB_D + 8192;
constexpr u32 LB_V = LB_K + 65536;
constexpr u32 LB_F = LB_V + 49152;
constexpr u32 C_LANDED = M_CNT, C_F = M_CNT + 4, C_QC = M_CNT + 8, C_FC = M_CNT + 12, C_Q = M_CNT + 16, C_M = M_CNT + 20, C_P = M_CNT + 24, C_H = M_CNT + 28,
              C_ISS = M_CNT + 32;
constexpr u32 LDS_BYTES = 163840;
constexpr int SPIN_MAX = 1 << 15, LSPIN_MAX = 1 << 21;
constexpr int CODE_BYTES = 13824;
constexpr float LOG2E = 1.4426950408889634f;
#define QSLEEP 14
#define G2SLEEP 1
#define G3SLEEP 1
#define WIN 16
#define WIN2 28
#define KN1 8
#define G3DUAL 0
#define PACE 0
#define PACE_NF 100
#define TOUCH_F 3
#define TOUCH_STEP 350000
#define FBLK 5
#define STBLK 4
#define G3FIRST 2

__device__ __forceinline__ u32 cvtpk(float lo, float hi) {
  const f32x2 v = {lo, hi};
  u32 r = __builtin_bit_cast(u32, __builtin_convertvector(v, bf16x2));
  asm("" : "+v"(r));
  return r;
}
__device__ __forceinline__ float max2(float a, float b) { return __builtin_amdgcn_fmed3f(a, b, __builtin_inff()); }
__device__ __forceinline__ float max3(float a, float b, float c) { return max2(max2(a, b), c); }
__device__ __forceinline__ float dot2(u32 a, u32 b, float c) {
  return __builtin_amdgcn_fdot2_f32_bf16(__builtin_bit_cast(bf16x2, a), __builtin_bit_cast(bf16x2, b), c, false);
}
__device__ __forceinline__ float bflo(u32 p) { return __uint_as_float(p << 16); }
__device__ __forceinline__ float bfhi(u32 p) { return __uint_as_float(p & 0xffff0000u); }
__device__ __forceinline__ float rbf(float x) { return bflo(cvtpk(x, x)); }
template <int CTRL> __device__ __forceinline__ float dpp(float x) {
  return __builtin_bit_cast(float, __builtin_amdgcn_mov_dpp(__builtin_bit_cast(int, x), CTRL, 0xf, 0xf, true));
}
constexpr int XOR1 = 0xB1, XOR2 = 0x4E, ROR8 = 0x128, ROR4 = 0x124, ROR2 = 0x122, ROR1 = 0x121;
__device__ __forceinline__ float swap32_add(float a, float b) {
  auto t = __builtin_amdgcn_permlane32_swap(__float_as_uint(a), __float_as_uint(b), false, false);
  return __uint_as_float(t[0]) + __uint_as_float(t[1]);
}
__device__ __forceinline__ float swap16_add(float a, float b) {
  auto t = __builtin_amdgcn_permlane16_swap(__float_as_uint(a), __float_as_uint(b), false, false);
  return __uint_as_float(t[0]) + __uint_as_float(t[1]);
}
__device__ __forceinline__ float xrow_max(float x) {
  auto s = __builtin_amdgcn_permlane16_swap(__float_as_uint(x), __float_as_uint(x), false, false);
  x = max2(__uint_as_float(s[0]), __uint_as_float(s[1]));
  auto t = __builtin_amdgcn_permlane32_swap(__float_as_uint(x), __float_as_uint(x), false, false);
  return max2(__uint_as_float(t[0]), __uint_as_float(t[1]));
}
__device__ __forceinline__ float xrow_sum(float x) { x = swap16_add(x, x); return swap32_add(x, x); }
__device__ __forceinline__ u64 ldg(const u64* base, u32 i) { return __hip_atomic_load((const gu64*)((const GAS char*)base + 8u * i), __ATOMIC_RELAXED, __HIP_MEMORY_SCOPE_AGENT); }
__device__ __forceinline__ void stg(u64* base, u32 i, u64 v) { __hip_atomic_store((gu64*)((GAS char*)base + 8u * i), v, __ATOMIC_RELAXED, __HIP_MEMORY_SCOPE_AGENT); }
struct Tag { u32 a, b; };
__device__ __forceinline__ u64 gpack(float v0, float v1, Tag t) {
  const u32 lo = ((__float_as_uint(v0) + 0x200u) & 0xfffffc00u) | t.a, hi = ((__float_as_uint(v1) + 0x200u) & 0xfffffc00u) | t.b;
  return ((u64)hi << 32) | lo;
}
__device__ __forceinline__ float gval0(u64 g) { return __uint_as_float((u32)g & 0xfffffc00u); }
__device__ __forceinline__ float gval1(u64 g) { return __uint_as_float((u32)(g >> 32) & 0xfffffc00u); }
__device__ __forceinline__ u32 gbad(u64 g, Tag t) { return (((u32)g ^ t.a) | ((u32)(g >> 32) ^ t.b)) & 0x3ffu; }

template <typename T> __device__ __forceinline__ LAS T* lp(u32 a) { return (LAS T*)(size_t)a; }
__device__ __forceinline__ bf16x8 ld_frag(u32 a) { return __builtin_bit_cast(bf16x8, *lp<u32x4>(a)); }
__device__ __forceinline__ bf16x8 ld_tr(u32 a0, u32 a1) {
  const s16x4 lo = __builtin_amdgcn_ds_read_tr16_b64_v4i16(lp<s16x4>(a0)), hi = __builtin_amdgcn_ds_read_tr16_b64_v4i16(lp<s16x4>(a1));
  return __builtin_bit_cast(bf16x8, __builtin_shufflevector(lo, hi, 0, 1, 2, 3, 4, 5, 6, 7));
}
__device__ __forceinline__ f32x4 mma(bf16x8 a, bf16x8 b, f32x4 c) { return __builtin_amdgcn_mfma_f32_16x16x32_bf16(a, b, c, 0, 0, 0); }
__device__ __forceinline__ void lds_arrive(u32 c) {
  asm volatile("s_waitcnt lgkmcnt(0)\n\ts_mov_b64 exec, 1\n\tds_add_u32 %0, %1\n\ts_mov_b64 exec, -1" :: "v"(c), "v"(1u) : "memory");
}
__device__ __forceinline__ void lds_wait(u32 c, u32 n, u32& seen) {
  u32 v, cnt = LSPIN_MAX;
  asm volatile("s_cmp_ge_u32 %1, %4\n\ts_cbranch_scc1 2f\n"
               "1:\n\tds_read_b32 %0, %3\n\ts_waitcnt lgkmcnt(0)\n\tv_readfirstlane_b32 %1, %0\n\ts_cmp_ge_u32 %1, %4\n\ts_cbranch_scc1 2f\n\t"
               "s_sleep 1\n\ts_sub_u32 %2, %2, 1\n\ts_cbranch_scc0 1b\n2:"
               : "=&v"(v), "+&s"(seen), "+&s"(cnt) : "v"(c), "s"(n) : "memory", "scc");
}
__device__ __forceinline__ void lds_wait(u32 c, u32 n) { u32 seen = 0; lds_wait(c, n, seen); }
#define WG_BARRIER(id) asm volatile("s_waitcnt lgkmcnt(0)\n\ts_barrier ; " id ::: "memory")
#define STR2(x) #x
#define STR(x) STR2(x)
#define DMA_PACE "2:\n\ts_memrealtime s[90:91]\n\ts_waitcnt lgkmcnt(0)\n\ts_lshl_b32 s90, s90, 4\n\ts_sub_u32 s90, s90, %[tn]\n\ts_cmp_lt_i32 s90, 0\n\ts_cbranch_scc1 2b\n\ts_add_u32 %[tn], %[tn], %[slot]\n\t"
#define DMA_THROTTLE0 "s_waitcnt vmcnt(%[win])\n\tds_write_b32 %[vz], %[prog]\n\t"
#define DMA_THROTTLE DMA_THROTTLE0
#define DMA_TILE_BODY(THR) \
  "s_mov_b32 m0, %[dst]\n\ts_nop 0\n1:\n\t" THR \
  "global_load_lds_dwordx4 %[v0], %[base] nt\n\ts_add_u32 m0, m0, 0x400\n\tv_add_u32 %[v0], %[step], %[v0]\n\t" \
  "global_load_lds_dwordx4 %[v1], %[base] nt\n\ts_add_u32 m0, m0, 0x400\n\tv_add_u32 %[v1], %[step], %[v1]\n\t" \
  "global_load_lds_dwordx4 %[v0], %[base] nt\n\ts_add_u32 m0, m0, 0x400\n\tv_add_u32 %[v0], %[step], %[v0]\n\t" \
  "global_load_lds_dwordx4 %[v1], %[base] nt\n\ts_add_u32 m0, m0, 0x400\n\tv_add_u32 %[v1], %[step], %[v1]\n\t" \
  "v_add_u32 %[prog], 4, %[prog]\n\ts_sub_u32 %[cnt], %[cnt], 1\n\ts_cbranch_scc0 1b"
template <int W>
__device__ __forceinline__ void dma_tile(u32 dst, const void* base, u32 v0, u32 v1, u32 step, u32 groups, u32& prog, u32& tn, u32 slot) {
  u32 cnt = groups - 1;
  if constexpr (W > 0)
    asm volatile(DMA_TILE_BODY(DMA_THROTTLE) : [v0] "+&v"(v0), [v1] "+&v"(v1), [prog] "+&v"(prog), [cnt] "+&s"(cnt), [tn] "+&s"(tn)
                 : [dst] "s"(dst), [base] "s"(base), [step] "s"(step), [vz] "v"(0u), [win] "n"(W), [slot] "s"(slot) : "memory", "scc", "s90", "s91");
  else
    asm volatile(DMA_TILE_BODY("") : [v0] "+&v"(v0), [v1] "+&v"(v1), [prog] "+&v"(prog), [cnt] "+&s"(cnt)
                 : [dst] "s"(dst), [base] "s"(base), [step] "s"(step) : "memory", "scc");
}
template <int W>
__device__ __forceinline__ void dma_two(u32 dst, const void* base, u32 v0, u32 v1, u32& prog) {
  asm volatile("s_mov_b32 m0, %[dst]\n\t" DMA_THROTTLE0 "global_load_lds_dwordx4 %[v0], %[base] nt\n\ts_add_u32 m0, m0, 0x400\n\tv_add_u32 %[prog], 2, %[prog]\n\t"
               "global_load_lds_dwordx4 %[v1], %[base] nt"
               : [prog] "+&v"(prog) : [dst] "s"(dst), [base] "s"(base), [v0] "v"(v0), [v1] "v"(v1), [vz] "v"(0u), [win] "n"(W) : "memory", "scc");
}
#define DMA_ROWS_BODY(THR) \
  "s_mov_b32 m0, %[dst]\n\ts_nop 0\n1:\n\t" THR \
  "global_load_lds_dwordx4 %[v0], %[base] nt\n\tglobal_load_lds_dwordx4 %[v1], %[base] offset:1024 nt\n\t" \
  "global_load_lds_dwordx4 %[v2], %[base] offset:2048 nt\n\tglobal_load_lds_dwordx4 %[v3], %[base] offset:3072 nt\n\t" \
  "s_add_u32 m0, m0, 0x1000\n\tv_add_u32 %[v0], 0x1000, %[v0]\n\tv_add_u32 %[v1], 0x1000, %[v1]\n\tv_add_u32 %[v2], 0x1000, %[v2]\n\tv_add_u32 %[v3], 0x1000, %[v3]\n\t" \
  "v_add_u32 %[prog], 4, %[prog]\n\ts_sub_u32 %[cnt], %[cnt], 1\n\ts_cbranch_scc0 1b"
template <int W>
__device__ __forceinline__ void dma_rows(u32 dst, const void* base, u32& v0, u32& v1, u32& v2, u32& v3, u32 groups, u32& prog, u32& tn, u32 slot) {
  u32 cnt = groups - 1;
  if constexpr (W > 0)
    asm volatile(DMA_ROWS_BODY(DMA_THROTTLE) : [v0] "+&v"(v0), [v1] "+&v"(v1), [v2] "+&v"(v2), [v3] "+&v"(v3), [prog] "+&v"(prog), [cnt] "+&s"(cnt), [tn] "+&s"(tn)
                 : [dst] "s"(dst), [base] "s"(base), [vz] "v"(0u), [win] "n"(W), [slot] "s"(slot) : "memory", "scc", "s90", "s91");
  else
    asm volatile(DMA_ROWS_BODY("") : [v0] "+&v"(v0), [v1] "+&v"(v1), [v2] "+&v"(v2), [v3] "+&v"(v3), [prog] "+&v"(prog), [cnt] "+&s"(cnt)
                 : [dst] "s"(dst), [base] "s"(base) : "memory", "scc");
}

#define STAMP(i)
#define LSTAMP(i)
#define STAMP_DECL
#define STAMP_OUT(slot)
#define COUNT(i)
#define STAMP_PAR
#define STAMP_ARG

__device__ __forceinline__ void final_reduce(const u64* ws, u16* out, int c, int qd, int lane, Tag tag, int first STAMP_PAR) {
  const u64* src = ws + W_Y3 + (c * 4 + qd) * 512;
  u64 gg[8];
  for (int i = 0; i < first; ++i) __builtin_amdgcn_s_sleep(1);
#pragma unroll
  for (int i = 0; i < 8; ++i) gg[i] = ldg(src, 64 * i + lane);
#pragma unroll 1
  for (int spin = 0; spin < SPIN_MAX; ++spin) {
    bool again = false;
#pragma unroll
    for (int i = 0; i < 8; ++i)
      if (__any(gbad(gg[i], tag) != 0)) { gg[i] = ldg(src, 64 * i + lane); again = true; }
    if (!again) break;
    COUNT(15);
  }
  const float v0 = ((gval0(gg[0]) + gval0(gg[1])) + (gval0(gg[2]) + gval0(gg[3]))) + ((gval0(gg[4]) + gval0(gg[5])) + (gval0(gg[6]) + gval0(gg[7])));
  const float v1 = ((gval1(gg[0]) + gval1(gg[1])) + (gval1(gg[2]) + gval1(gg[3]))) + ((gval1(gg[4]) + gval1(gg[5])) + (gval1(gg[6]) + gval1(gg[7])));
  float v = swap16_add(v0, v1);
  v = swap32_add(v, v);
  const u32 o2 = cvtpk(v, dpp<XOR1>(v));
  if (lane < 32 && !(lane & 1)) *(u32*)(out + (lane >> 4) * 512 + 64 * c + 16 * qd + (lane & 15)) = o2;
  LSTAMP(12);
}

__global__ void __launch_bounds__(NTHR, 3)
fused_decode(const u16* __restrict__ x, const u16* __restrict__ Kc, const u16* __restrict__ Vc, const u16* __restrict__ wq,
             const u16* __restrict__ wo, const u16* __restrict__ wg, const u16* __restrict__ wu, const u16* __restrict__ wd,
             const u16* __restrict__ gw, u16* __restrict__ out, u64* __restrict__ ws) {
  __shared__ __attribute__((aligned(1024))) unsigned char lds_all[LDS_BYTES];
  asm volatile("" :: "v"((LAS unsigned char*)lds_all));
  STAMP_DECL;
  LSTAMP(0);
  asm volatile("" :: "s"(x), "s"(Kc), "s"(Vc), "s"(wq), "s"(wo), "s"(wg), "s"(wu), "s"(wd), "s"(gw), "s"(out), "s"(ws));
  const int tid = threadIdx.x, lane = tid & 63;
  const int wid = __builtin_amdgcn_readfirstlane(tid >> 6);
  const int wgid = blockIdx.x, xcd = wgid & 7, idx = wgid >> 3;
  const int hh = idx >> 3, s = idx & 7, h = xcd * 4 + hh;
  const int rq = s >> 1, par = s & 1;
  const int b = s & 1, j = s >> 1;
  const u32 r8 = lane >> 3, r4 = lane >> 4;
  const u32 sw0 = 16u * ((lane & 7) ^ (r8 & 2));
  const size_t kvoff = ((size_t)(b * 32 + h) * 4096 + 3072 + j * 256) * 256;
  u32 k0 = r4 * 256 + 16 * ((lane & 15) ^ r4), k1 = k0 ^ 64, k2 = k0 ^ 128, k3 = k0 ^ 192;
  u32 prog = 28 - WIN;
  if (wid == 0) {
    STAMP(6);
    __builtin_amdgcn_s_setprio(2);
    if (lane < 12) *lp<u32>(M_CNT + 4 * lane) = 0;
    asm volatile("s_mov_b64 exec, 1\n\ts_mov_b32 m0, %0\n\ts_nop 0\n\tglobal_load_lds_dwordx4 %1, %2\n\ts_mov_b64 exec, -1\n\t"
                 "s_mov_b32 m0, %3\n\ts_nop 0\n\tglobal_load_lds_dwordx4 %4, %5\n\tglobal_load_lds_dwordx4 %4, %5 offset:1024\n\t"
                 "s_mov_b32 m0, %6\n\ts_nop 0\n\tglobal_load_lds_dwordx4 %4, %7"
                 :: "s"(M_TAG), "v"(0u), "s"(ws + W_EPOCH), "s"(M_X), "v"(16u * lane), "s"(x), "s"(M_GW), "s"(gw) : "memory");
    STAMP(7);
    u32 dummy = 0, dummy2 = 0;
    dma_tile<0>(LB_W, (const char*)wq + ((size_t)rq << 20) + 256 * h + 128 * par, r8 * 8192 + sw0, r8 * 8192 + (sw0 ^ 64) + 65536, 131072, 4, dummy, dummy2, 0);
    STAMP(8);
    dma_rows<0>(LB_K, (const char*)Kc + kvoff, k0, k1, k2, k3, 2, dummy, dummy2, 0);
  }
  u32 tv = 0;
  if (wid == 9) {
    const char* vb = (const char*)Vc + kvoff;
    const char* fb0 = (const char*)((s & 1) ? wu : wg) + (size_t)256 * (s >> 2) * (2 * FF);
    const char* p = lane == 0 ? vb : lane == 1 ? vb + 65535 : lane == 2 ? (const char*)Kc + kvoff + 65535
                  : lane == 3 ? (const char*)(wo + (size_t)128 * h * 512) : lane == 4 ? (const char*)(wo + (size_t)(128 * h + 127) * 512)
                  : lane == 5 ? (const char*)(wd + (size_t)43 * h * 512) : lane == 6 ? (const char*)(wd + (size_t)(43 * h + 42) * 512)
                  : lane == 7 ? (const char*)(out + 64 * xcd) : fb0 + (size_t)(lane - 8) * TOUCH_STEP;
    if (lane < 8 + TOUCH_F) asm volatile("global_load_ubyte %0, %1, off nt" : "=v"(tv) : "v"(p) : "memory");
  }
  u32 pf0;
  {
    u64 pc;
    asm volatile("s_getpc_b64 %0" : "=s"(pc));
    const u32 off = min((idx * 4 + (lane & 3)) * 128u, (u32)CODE_BYTES - 128u);
    pc &= ~127ull;
    if (tid >= 64 && tid < 68) asm volatile("global_load_dword %0, %1, %2" : "=v"(pf0) : "v"(off), "s"(pc) : "memory");
  }
  WG_BARRIER("B0");
  const int fgr = (int)((0x46573210u >> (4 * xcd)) & 15u) * 4 + hh;
  const bool ffn = fgr < 22;
  const int fg = ffn ? fgr : 0;
  const u32 fo = ffn ? 16u : 0u;
  int role = wid;
  asm volatile("" : "+s"(role));
  if (role == 0) {
    STAMP(1);
    const u32 sw1 = sw0 ^ 64;
    u32 tn = 0;
    const u32 slot = ffn ? PACE : PACE * PACE_NF / 100;
    if (ffn)
      dma_tile<WIN>(LB_F, (const char*)((s & 1) ? wu : wg) + (size_t)(256 * (s >> 2) + ((s >> 1) & 1)) * (2 * FF) + 128 * fg - 64 * ((s >> 1) & 1),
                     r8 * (4 * FF) + sw0, r8 * (4 * FF) + sw1 + 8 * 4 * FF, 16 * 4 * FF, 4, prog, tn, slot);
    dma_rows<WIN>(LB_K + 8192, (const char*)Kc + kvoff, k0, k1, k2, k3, KN1, prog, tn, slot);
    prog -= WIN2 - WIN;
    dma_rows<WIN2>(LB_K + 8192 + 4096 * KN1, (const char*)Kc + kvoff, k0, k1, k2, k3, 14 - KN1, prog, tn, slot);
    STAMP(2);
    {
      u32 v0 = r4 * 256 + 16 * ((lane & 15) ^ (r4 << 1)), v1 = v0 ^ 128, v2 = v0, v3 = v1;
      dma_rows<WIN2>(LB_V, (const char*)Vc + kvoff, v0, v1, v2, v3, 12, prog, tn, slot);
      if (ffn) lds_wait(C_FC, 4);
      dma_rows<WIN2>(LB_V + 49152, (const char*)Vc + kvoff, v0, v1, v2, v3, 4, prog, tn, slot);
    }
    STAMP(3);
    {
      const char* base = (const char*)(wd + (size_t)43 * h * 512 + 64 * s);
      dma_tile<WIN2>(LB_D, base, r8 * 1024 + sw0, r8 * 1024 + sw1 + 8192, 16384, 1, prog, tn, slot);
      const u32 rmax = FF - 1 - 43 * h;
      dma_two<WIN2>(LB_D + 4096, base, min(32 + r8, rmax) * 1024 + sw0, min(40 + r8, rmax) * 1024 + sw1, prog);
    }
    lds_wait(C_QC, 4);
    dma_tile<WIN2>(LB_W, (const char*)(wo + (size_t)128 * h * 512 + 64 * s), r8 * 1024 + sw0, r8 * 1024 + sw1 + 8192, 16384, 4, prog, tn, slot);
    *(volatile LAS u32*)(size_t)C_ISS = 1;
    STAMP(4);
#define DRAIN(k) "s_waitcnt vmcnt(" #k ")\n\tv_add_u32 %1, " STR(WIN2) " - " #k ", %0\n\tds_write_b32 %2, %1\n\t"
    u32 t;
    asm volatile(DRAIN(28) DRAIN(24) DRAIN(20) DRAIN(16) DRAIN(12) DRAIN(8) DRAIN(4) DRAIN(0) "s_waitcnt lgkmcnt(0)" : "+v"(prog), "=&v"(t) : "v"(0u) : "memory");
    LSTAMP(5);
    STAMP_OUT(8);
    return;
  }
  const int g = lane >> 4, n16 = lane & 15, q4 = n16 >> 2, p4 = lane & 3;
  u32 landed = 0;
  if (role == 9) {
    const u32x4 z4 = {0u, 0u, 0u, 0u};
    *lp<u32x4>(LB_D + 6144 + 16 * lane) = z4; *lp<u32x4>(LB_D + 7168 + 16 * lane) = z4;
    if (lane < 16) *lp<u32x4>(M_H + 16 * lane) = z4;
  }
  lds_wait(C_LANDED, 4, landed);
  const u64 ep = *lp<u64>(M_TAG) + 1;
  const Tag tag = {(u32)ep & 0x3ffu, (u32)(ep >> 10) & 0x3ffu};

  if (role == 9) {
    asm volatile("s_waitcnt vmcnt(0)" : "+v"(tv) :: "memory");
    {
      const u32x4 gv = *lp<u32x4>(M_GW + 16 * lane), x0 = *lp<u32x4>(M_X + 16 * lane), x1 = *lp<u32x4>(M_X + 1024 + 16 * lane);
      float z0 = 0.f, z1 = 0.f;
#pragma unroll
      for (int t = 0; t < 4; ++t) {
        const float gl = bflo(gv[t]), gh = bfhi(gv[t]);
        z0 = dot2(cvtpk(bflo(x0[t]) * gl, bfhi(x0[t]) * gh), 0x3f803f80u, z0);
        z1 = dot2(cvtpk(bflo(x1[t]) * gl, bfhi(x1[t]) * gh), 0x3f803f80u, z1);
      }
      float z = swap32_add(z0, z1);
      z = swap16_add(z, z);
      z += dpp<ROR8>(z); z += dpp<ROR4>(z); z += dpp<ROR2>(z); z += dpp<ROR1>(z);
      const float gt = rbf(__builtin_amdgcn_rcpf(1.f + __expf(-rbf(z))));
      if ((lane & 31) == 0) { *lp<float>(M_G + (lane >> 5) * 8) = gt; *lp<float>(M_G + (lane >> 5) * 8 + 4) = rbf(1.f - gt); }
    }
    STAMP(1);
    lds_wait(C_LANDED, 28 + fo + 8 * FBLK, landed);
    const u32 fb = (43 * h) & ~15u, fe = fb + lane, fo2 = min(fb + 32 + lane, F1_ROW - 1);
    u64 fp[8];
#pragma unroll
    for (int i = 0; i < 8; ++i) fp[i] = ldg(ws + W_F1 + F1_ROW * i, (i & 2) ? fo2 : fe);
    lds_wait(C_P, 8);
    {
      const f32x4 a = *lp<f32x4>(M_WM), c = *lp<f32x4>(M_WM + 16), la = *lp<f32x4>(M_WL), lc = *lp<f32x4>(M_WL + 16);
      const float m = max3(max3(a[0], a[1], a[2]), max3(a[3], c[0], c[1]), max3(c[2], c[3], c[3]));
      const float L = ((la[0] + la[1]) + (la[2] + la[3])) + ((lc[0] + lc[1]) + (lc[2] + lc[3]));
      stg(ws + W_ST + ((h * 2 + b) * 4 + j) * 16, n16, gpack(m, L, tag));
    }
    STAMP(2);
    {
      u32 bad = 0;
#pragma unroll
      for (int i = 0; i < 8; ++i) bad |= gbad(fp[i], tag);
      if (__builtin_expect(__any(bad != 0), 0)) {
#pragma unroll 1
        for (int spin = 0; spin < SPIN_MAX; ++spin) {
          COUNT(14);
          bad = 0;
#pragma unroll
          for (int i = 0; i < 8; ++i) fp[i] = ldg(ws + W_F1 + F1_ROW * i, (i & 2) ? fo2 : fe);
#pragma unroll
          for (int i = 0; i < 8; ++i) bad |= gbad(fp[i], tag);
          if (!__any(bad != 0)) break;
        }
      }
      const u32 gp = cvtpk((gval0(fp[0]) + gval0(fp[1])) + (gval0(fp[2]) + gval0(fp[3])), (gval1(fp[0]) + gval1(fp[1])) + (gval1(fp[2]) + gval1(fp[3])));
      const u32 up = cvtpk((gval0(fp[4]) + gval0(fp[5])) + (gval0(fp[6]) + gval0(fp[7])), (gval1(fp[4]) + gval1(fp[5])) + (gval1(fp[6]) + gval1(fp[7])));
      const u32 hp = cvtpk(bflo(gp) * bflo(up), bfhi(gp) * bfhi(up));
      const u32 i = fe - 43 * h;
      if (i < 43) { *lp<u16>(M_H + 2 * i) = (u16)hp; *lp<u16>(M_H + 128 + 2 * i) = (u16)(hp >> 16); }
      lds_arrive(C_H);
    }
    STAMP(3);
    lds_wait(C_LANDED, 92 + fo + 8 * STBLK, landed);
    {
      const u64* src = ws + W_ST + h * 128;
      u64 s0, s1;
#pragma unroll 1
      for (int spin = 0; spin < SPIN_MAX; ++spin) {
        s0 = ldg(src, lane); s1 = ldg(src, 64 + lane);
        if (!__any((gbad(s0, tag) | gbad(s1, tag)) != 0)) break;
        COUNT(13);
      }
      const u64 sb = (lane & 8) ? s1 : s0;
      const float mj = gval0(sb);
      const float w = __builtin_amdgcn_exp2f((mj - xrow_max(mj)) * LOG2E);
      const float f = w * __builtin_amdgcn_rcpf(xrow_sum(w * gval1(sb)));
      if ((lane & 7) == 0) *lp<float>(M_F + 2 * (lane & 8) + 4 * g) = f;
      lds_arrive(C_F);
    }
    STAMP(4);
    WG_BARRIER("B1 helper");
    if (wgid == 68) {
      const u32 i0 = (lane >> 3) * 512 + ((lane >> 1) & 3) * 64 + (lane & 1) * 32;
#pragma unroll 1
      for (int spin = 0; spin < SPIN_MAX; ++spin) {
        u32 bad = 0;
#pragma unroll
        for (int i = 0; i < 4; ++i) bad |= gbad(ldg(ws + W_Q1 + 4096 * i, i0), tag);
        if (!__any(bad != 0)) { if (lane == 0) stg(ws + W_EPOCH, 0, ep); break; }
        __builtin_amdgcn_s_sleep(8);
      }
    }
    if (hh == 0 && s < 4) final_reduce(ws, out, xcd, s, lane, tag, G3FIRST STAMP_ARG);
    STAMP_OUT(9);
    return;
  }

  const int wave = role - 1, ct = wave & 3;
  const int bm = n16 & 1;
  STAMP(1);
  const u32 tile_a = 128 * (8 * g + q4) + 16 * ((2 * ct + (p4 >> 1)) ^ (((q4 >> 1) | ((g & 1) << 1)) << 1)) + 8 * (p4 & 1);
  if (wave < 4) {
    const u32 xa = M_X + 1024 * bm + 256 * rq + 16 * g;
    bf16x8 xf[4];
#pragma unroll
    for (int ks = 0; ks < 4; ++ks) xf[ks] = ld_frag(xa + 64 * ks);
    lds_wait(C_LANDED, 20, landed);
    f32x4 acc = {0.f, 0.f, 0.f, 0.f};
#pragma unroll
    for (int ks = 0; ks < 4; ++ks) acc = mma(xf[ks], ld_tr(LB_W + tile_a + 4096 * ks, LB_W + tile_a + 4096 * ks + 512), acc);
    {
      const float n0 = dpp<XOR1>(acc[0]), n1 = dpp<XOR1>(acc[1]);
      if (lane < 16) stg(ws + W_Q1 + h * 512 + rq * 64 + 32 * par + 8 * ct, 256 * bm + (n16 >> 1), gpack(bm ? n1 : acc[0], bm ? acc[1] : n0, tag));
    }
    lds_arrive(C_QC);
    STAMP(2);
    __builtin_amdgcn_s_sleep(QSLEEP);
    const u64* src = ws + W_Q1 + h * 512 + b * 256 + 16 * wave;
    u64 g0;
#pragma unroll 1
    for (int spin = 0; spin < SPIN_MAX; ++spin) {
      g0 = ldg(src, 64 * g + n16);
      if (!__any(gbad(g0, tag) != 0)) break;
      COUNT(13);
    }
    const u32 qq = cvtpk(xrow_sum(gval0(g0)), xrow_sum(gval1(g0)));
    if (lane < 16) *lp<u32>(M_Q + 4 * (16 * wave + lane)) = qq;
    lds_arrive(C_Q);
  } else if (ffn) {
    const u32 xa = M_X + 1024 * bm + 512 * (s >> 2) + 32 * g, sel = (s & 2) ? 0x07060302u : 0x05040100u;
    u32x4 xf[4];
#pragma unroll
    for (int ks = 0; ks < 4; ++ks) {
      const u32x4 lo = *lp<u32x4>(xa + 128 * ks), hi = *lp<u32x4>(xa + 128 * ks + 16);
      xf[ks] = u32x4{__builtin_amdgcn_perm(lo[1], lo[0], sel), __builtin_amdgcn_perm(lo[3], lo[2], sel),
                     __builtin_amdgcn_perm(hi[1], hi[0], sel), __builtin_amdgcn_perm(hi[3], hi[2], sel)};
    }
    lds_wait(C_LANDED, 44, landed);
    f32x4 acc = {0.f, 0.f, 0.f, 0.f};
#pragma unroll
    for (int ks = 0; ks < 4; ++ks) acc = mma(__builtin_bit_cast(bf16x8, xf[ks]), ld_tr(LB_F + tile_a + 4096 * ks, LB_F + tile_a + 4096 * ks + 512), acc);
    if (lane < 16) stg(ws + W_F1 + ((s & 1) * 4 + (s & 2) + (s >> 2)) * F1_ROW + 64 * fg + 16 * ct, lane, gpack(acc[0], acc[1], tag));
    lds_arrive(C_FC);
    STAMP(2);
  }
  if (wave == 0) asm volatile("" : "+v"(pf0));
  STAMP(3);
  lds_wait(C_LANDED, 28 + 8 * wave + (wave >= 1 ? fo : 0u), landed);
  lds_wait(C_Q, 4);
  STAMP(4);
  float sc[8];
  {
    const u32 ka = LB_K + 256 * (32 * wave + n16) + 16 * (g ^ n16);
    f32x4 d0 = {0.f, 0.f, 0.f, 0.f}, d1 = {0.f, 0.f, 0.f, 0.f};
#pragma unroll
    for (int ks = 0; ks < 4; ++ks) {
      const bf16x8 qf = ld_frag(M_Q + 64 * ks + 16 * g);
      d0 = mma(ld_frag(ka ^ (64 * ks)), qf, d0);
      d1 = mma(ld_frag((ka ^ (64 * ks)) + 4096), qf, d1);
    }
#pragma unroll
    for (int i = 0; i < 4; i += 2) {
      const u32 a = cvtpk(d0[i], d0[i + 1]), c = cvtpk(d1[i], d1[i + 1]);
      const u32 a2 = cvtpk(bflo(a) * 0.08838834764831845f, bfhi(a) * 0.08838834764831845f);
      const u32 c2 = cvtpk(bflo(c) * 0.08838834764831845f, bfhi(c) * 0.08838834764831845f);
      sc[i] = bflo(a2); sc[i + 1] = bfhi(a2); sc[4 + i] = bflo(c2); sc[5 + i] = bfhi(c2);
    }
  }
  {
    float m = xrow_max(max3(max3(sc[0], sc[1], sc[2]), max3(sc[3], sc[4], sc[5]), max3(sc[6], sc[7], sc[7])));
    if (lane == 0) *lp<float>(M_WM + 4 * wave) = m;
    lds_arrive(C_M);
    lds_wait(C_M, 8);
    STAMP(5);
    const f32x4 a = *lp<f32x4>(M_WM), c = *lp<f32x4>(M_WM + 16);
    m = max3(max3(a[0], a[1], a[2]), max3(a[3], c[0], c[1]), max3(c[2], c[3], c[3]));
    const float mL = m * LOG2E;
    float p[8], l = 0.f;
#pragma unroll
    for (int i = 0; i < 8; ++i) { p[i] = __builtin_amdgcn_exp2f(fmaf(sc[i], LOG2E, -mL)); l += p[i]; }
    l = xrow_sum(l);
    const u32x4 pf = {cvtpk(p[0], p[1]), cvtpk(p[2], p[3]), cvtpk(p[4], p[5]), cvtpk(p[6], p[7])};
    if (n16 == 0) *lp<u32x4>(M_P + 64 * wave + 16 * g) = pf;
    if (lane == 0) *lp<float>(M_WL + 4 * wave) = l;
  }
  lds_arrive(C_P);
  lds_wait(C_P, 8);
  STAMP(6);
  f32x4 oacc = {0.f, 0.f, 0.f, 0.f};
  {
    const u32 va = LB_V + 256 * (4 * g + q4) + 16 * ((2 * wave + (p4 >> 1)) ^ ((4 * (g & 1) + q4) << 1)) + 8 * (p4 & 1);
    bf16x8 pfr[8];
#pragma unroll
    for (int t = 0; t < 8; ++t) pfr[t] = ld_frag(M_P + 64 * t + 16 * g);
    lds_wait(C_LANDED, 92 + fo + 8 * 5, landed);
#pragma unroll
    for (int t = 0; t < 6; ++t) oacc = mma(pfr[t], ld_tr(va + 8192 * t, va + 8192 * t + 4096), oacc);
    lds_wait(C_LANDED, 92 + fo + 8 * 6, landed);
    oacc = mma(pfr[6], ld_tr(va + 8192 * 6, va + 8192 * 6 + 4096), oacc);
    lds_wait(C_LANDED, 92 + fo + 8 * 7, landed);
    oacc = mma(pfr[7], ld_tr(va + 8192 * 7, va + 8192 * 7 + 4096), oacc);
  }
  {
    const float on = dpp<XOR1>(oacc[0]);
    if (lane < 16 && !(lane & 1)) stg(ws + W_O2 + ((h * 2 + b) * 4 + j) * 64 + 8 * wave, lane >> 1, gpack(oacc[0], on, tag));
  }
  STAMP(7);
  const u64* vs = ws + W_O2 + h * 512 + (wave & 1) * 256 + 16 * (wave >> 1);
  const u32 vi = 64 * p4 + (lane >> 2);
  lds_wait(C_ISS, 1);
  u64 gg = ldg(vs, vi);
  bf16x8 wof[4];
  float yb0 = 0.f, yb1 = 0.f;
  f32x4 gates;
  if (wave < 4) {
    lds_wait(C_H, 1);
    gates = *lp<f32x4>(M_G);
    lds_wait(C_LANDED, 154 + fo, landed);
    f32x4 acc = {0.f, 0.f, 0.f, 0.f};
#pragma unroll
    for (int ks = 0; ks < 2; ++ks) acc = mma(ld_frag(M_H + 128 * bm + 64 * ks + 16 * g), ld_tr(LB_D + tile_a + 4096 * ks, LB_D + tile_a + 4096 * ks + 512), acc);
    yb0 = gates[1] * acc[0]; yb1 = gates[3] * acc[1];
    lds_wait(C_LANDED, 170 + fo, landed);
#pragma unroll
    for (int ks = 0; ks < 4; ++ks) wof[ks] = ld_tr(LB_W + tile_a + 4096 * ks, LB_W + tile_a + 4096 * ks + 512);
  }
  lds_wait(C_F, 1);
  const float fq = *lp<float>(M_F + 16 * (wave & 1) + 4 * p4);
  STAMP(8);
  if (__builtin_expect(__any(gbad(gg, tag) != 0), 0)) {
#pragma unroll 1
    for (int spin = 0; spin < SPIN_MAX; ++spin) {
      COUNT(15);
      gg = ldg(vs, vi);
      if (!__any(gbad(gg, tag) != 0)) break;
      __builtin_amdgcn_s_sleep(G2SLEEP);
    }
  }
  STAMP(9);
  {
    float t0 = fq * gval0(gg), t1 = fq * gval1(gg);
    t0 += dpp<XOR1>(t0); t1 += dpp<XOR1>(t1); t0 += dpp<XOR2>(t0); t1 += dpp<XOR2>(t1);
    const u32 ob = cvtpk(t0, t1);
    if (p4 == 0) *lp<u32>(M_O + 256 * (wave & 1) + 64 * (wave >> 1) + lane) = ob;
  }
  WG_BARRIER("B1");
  STAMP(10);
  if (wave < 4) {
    f32x4 acc = {0.f, 0.f, 0.f, 0.f};
#pragma unroll
    for (int ks = 0; ks < 4; ++ks) acc = mma(ld_frag(M_O + 256 * bm + 64 * ks + 16 * g), wof[ks], acc);
    if (lane < 16) stg(ws + W_Y3 + ((s * 4 + ct) * 32 + h) * 16, lane, gpack(fmaf(gates[0], acc[0], yb0), fmaf(gates[2], acc[1], yb1), tag));
    STAMP(11);
  }
  STAMP_OUT(wave);
}
}

extern "C" void kernel_launch(void* const* d_in, const int* in_sizes, int n_in, void* d_out, int out_size, void* d_ws, size_t ws_size,
                              hipStream_t stream) {
  fused_decode<<<NWG, NTHR, 0, stream>>>((const u16*)d_in[0], (const u16*)d_in[1], (const u16*)d_in[2], (const u16*)d_in[3],
                                         (const u16*)d_in[4], (const u16*)d_in[6], (const u16*)d_in[7], (const u16*)d_in[8],
                                         (const u16*)d_in[9], (u16*)d_out, (u64*)d_ws);
}
```

```cpp
#include <hip/hip_runtime.h>
#include <stdint.h>

typedef unsigned short u16;
typedef unsigned int u32;
typedef unsigned long long u64;
typedef u32 u32x4 __attribute__((ext_vector_type(4)));
typedef short s16x4 __attribute__((ext_vector_type(4)));
typedef short s16x8 __attribute__((ext_vector_type(8)));
typedef float f32x2 __attribute__((ext_vector_type(2)));
typedef float f32x4 __attribute__((ext_vector_type(4)));
typedef __bf16 bf16x2 __attribute__((ext_vector_type(2)));
typedef __bf16 bf16x8 __attribute__((ext_vector_type(8)));
#define GAS __attribute__((address_space(1)))
#define LAS __attribute__((address_space(3)))
typedef GAS u64 gu64;

namespace {
constexpr int NWG = 256, NTHR = 640;
constexpr int FF = 1376;
constexpr u32 W_EPOCH = 0;
constexpr u32 W_Q1 = 1024;
constexpr u32 F1_ROW = 1408;
constexpr u32 W_F1 = W_Q1 + 32 * 512;
constexpr u32 W_O2 = W_F1 + 8 * F1_ROW;
constexpr u32 W_ST = W_O2 + 32 * 512;
constexpr u32 W_Y3 = W_ST + 32 * 128;
constexpr u32 W_END = W_Y3 + 32 * 512;
constexpr size_t W_DBG = 262144;
constexpr u32 LB_M = 0;
constexpr u32 M_CNT = LB_M;
constexpr u32 M_TAG = M_CNT + 48;
constexpr u32 M_G = M_TAG + 16;
constexpr u32 M_WM = M_G + 16;
constexpr u32 M_WL = M_WM + 32;
constexpr u32 M_F = M_WL + 32;
constexpr u32 M_Q = 256;
constexpr u32 M_P = M_Q + 256;
constexpr u32 M_O = M_P + 512;
constexpr u32 M_H = M_O + 512;
constexpr u32 M_X = 2048;
constexpr u32 M_GW = M_X + 2048;
constexpr u32 LB_W = 8192;
constexpr u32 LB_D = LB_W + 16384;
constexpr u32 LB_K = LB_D + 8192;
constexpr u32 LB_V = LB_K + 65536;
constexpr u32 LB_F = LB_V + 49152;
constexpr u32 C_LANDED = M_CNT, C_F = M_CNT + 4, C_QC = M_CNT + 8, C_FC = M_CNT + 12, C_Q = M_CNT + 16, C_M = M_CNT + 20, C_P = M_CNT + 24, C_H = M_CNT + 28,
              C_ISS = M_CNT + 32;
constexpr u32 LDS_BYTES = 163840;
constexpr int SPIN_MAX = 1 << 15, LSPIN_MAX = 1 << 21;
constexpr int CODE_BYTES = 13824;
constexpr float LOG2E = 1.4426950408889634f;
#define QSLEEP 14
#define G2SLEEP 1
#define G3SLEEP 1
#define WIN 16
#define WIN2 28
#define KN1 8
#define G3DUAL 0
#define FBLK 5
#define STBLK 4
#define G3FIRST 2

__device__ __forceinline__ u32 cvtpk(float lo, float hi) {
  const f32x2 v = {lo, hi};
  u32 r = __builtin_bit_cast(u32, __builtin_convertvector(v, bf16x2));
  asm("" : "+v"(r));
  return r;
}
__device__ __forceinline__ float max2(float a, float b) { return __builtin_amdgcn_fmed3f(a, b, __builtin_inff()); }
__device__ __forceinline__ float max3(float a, float b, float c) { return max2(max2(a, b), c); }
__device__ __forceinline__ float dot2(u32 a, u32 b, float c) {
  return __builtin_amdgcn_fdot2_f32_bf16(__builtin_bit_cast(bf16x2, a), __builtin_bit_cast(bf16x2, b), c, false);
}
__device__ __forceinline__ float bflo(u32 p) { return __uint_as_float(p << 16); }
__device__ __forceinline__ float bfhi(u32 p) { return __uint_as_float(p & 0xffff0000u); }
__device__ __forceinline__ float rbf(float x) { return bflo(cvtpk(x, x)); }
template <int CTRL> __device__ __forceinline__ float dpp(float x) {
  return __builtin_bit_cast(float, __builtin_amdgcn_mov_dpp(__builtin_bit_cast(int, x), CTRL, 0xf, 0xf, true));
}
constexpr int XOR1 = 0xB1, XOR2 = 0x4E, ROR8 = 0x128, ROR4 = 0x124, ROR2 = 0x122, ROR1 = 0x121;
__device__ __forceinline__ float swap32_add(float a, float b) {
  auto t = __builtin_amdgcn_permlane32_swap(__float_as_uint(a), __float_as_uint(b), false, false);
  return __uint_as_float(t[0]) + __uint_as_float(t[1]);
}
__device__ __forceinline__ float swap16_add(float a, float b) {
  auto t = __builtin_amdgcn_permlane16_swap(__float_as_uint(a), __float_as_uint(b), false, false);
  return __uint_as_float(t[0]) + __uint_as_float(t[1]);
}
__device__ __forceinline__ float xrow_max(float x) {
  auto s = __builtin_amdgcn_permlane16_swap(__float_as_uint(x), __float_as_uint(x), false, false);
  x = max2(__uint_as_float(s[0]), __uint_as_float(s[1]));
  auto t = __builtin_amdgcn_permlane32_swap(__float_as_uint(x), __float_as_uint(x), false, false);
  return max2(__uint_as_float(t[0]), __uint_as_float(t[1]));
}
__device__ __forceinline__ float xrow_sum(float x) { x = swap16_add(x, x); return swap32_add(x, x); }
__device__ __forceinline__ u64 ldg(const u64* base, u32 i) { return __hip_atomic_load((const gu64*)((const GAS char*)base + 8u * i), __ATOMIC_RELAXED, __HIP_MEMORY_SCOPE_AGENT); }
__device__ __forceinline__ void stg(u64* base, u32 i, u64 v) { __hip_atomic_store((gu64*)((GAS char*)base + 8u * i), v, __ATOMIC_RELAXED, __HIP_MEMORY_SCOPE_AGENT); }
struct Tag { u32 a, b; };
__device__ __forceinline__ u64 gpack(float v0, float v1, Tag t) {
  const u32 lo = ((__float_as_uint(v0) + 0x200u) & 0xfffffc00u) | t.a, hi = ((__float_as_uint(v1) + 0x200u) & 0xfffffc00u) | t.b;
  return ((u64)hi << 32) | lo;
}
__device__ __forceinline__ float gval0(u64 g) { return __uint_as_float((u32)g & 0xfffffc00u); }
__device__ __forceinline__ float gval1(u64 g) { return __uint_as_float((u32)(g >> 32) & 0xfffffc00u); }
__device__ __forceinline__ u32 gbad(u64 g, Tag t) { return (((u32)g ^ t.a) | ((u32)(g >> 32) ^ t.b)) & 0x3ffu; }

template <typename T> __device__ __forceinline__ LAS T* lp(u32 a) { return (LAS T*)(size_t)a; }
__device__ __forceinline__ bf16x8 ld_frag(u32 a) { return __builtin_bit_cast(bf16x8, *lp<u32x4>(a)); }
__device__ __forceinline__ bf16x8 ld_tr(u32 a0, u32 a1) {
  const s16x4 lo = __builtin_amdgcn_ds_read_tr16_b64_v4i16(lp<s16x4>(a0)), hi = __builtin_amdgcn_ds_read_tr16_b64_v4i16(lp<s16x4>(a1));
  return __builtin_bit_cast(bf16x8, __builtin_shufflevector(lo, hi, 0, 1, 2, 3, 4, 5, 6, 7));
}
__device__ __forceinline__ f32x4 mma(bf16x8 a, bf16x8 b, f32x4 c) { return __builtin_amdgcn_mfma_f32_16x16x32_bf16(a, b, c, 0, 0, 0); }
__device__ __forceinline__ void lds_arrive(u32 c) {
  asm volatile("s_waitcnt lgkmcnt(0)\n\ts_mov_b64 exec, 1\n\tds_add_u32 %0, %1\n\ts_mov_b64 exec, -1" :: "v"(c), "v"(1u) : "memory");
}
__device__ __forceinline__ void lds_wait(u32 c, u32 n, u32& seen) {
  u32 v, cnt = LSPIN_MAX;
  asm volatile("s_cmp_ge_u32 %1, %4\n\ts_cbranch_scc1 2f\n"
               "1:\n\tds_read_b32 %0, %3\n\ts_waitcnt lgkmcnt(0)\n\tv_readfirstlane_b32 %1, %0\n\ts_cmp_ge_u32 %1, %4\n\ts_cbranch_scc1 2f\n\t"
               "s_sleep 1\n\ts_sub_u32 %2, %2, 1\n\ts_cbranch_scc0 1b\n2:"
               : "=&v"(v), "+&s"(seen), "+&s"(cnt) : "v"(c), "s"(n) : "memory", "scc");
}
__device__ __forceinline__ void lds_wait(u32 c, u32 n) { u32 seen = 0; lds_wait(c, n, seen); }
#define WG_BARRIER(id) asm volatile("s_waitcnt lgkmcnt(0)\n\ts_barrier ; " id ::: "memory")
#define STR2(x) #x
#define STR(x) STR2(x)
#define DMA_THROTTLE "s_waitcnt vmcnt(%[win])\n\tds_write_b32 %[vz], %[prog]\n\t"
#define DMA_TILE_BODY(THR) \
  "s_mov_b32 m0, %[dst]\n\ts_nop 0\n1:\n\t" THR \
  "global_load_lds_dwordx4 %[v0], %[base] nt\n\ts_add_u32 m0, m0, 0x400\n\tv_add_u32 %[v0], %[step], %[v0]\n\t" \
  "global_load_lds_dwordx4 %[v1], %[base] nt\n\ts_add_u32 m0, m0, 0x400\n\tv_add_u32 %[v1], %[step], %[v1]\n\t" \
  "global_load_lds_dwordx4 %[v0], %[base] nt\n\ts_add_u32 m0, m0, 0x400\n\tv_add_u32 %[v0], %[step], %[v0]\n\t" \
  "global_load_lds_dwordx4 %[v1], %[base] nt\n\ts_add_u32 m0, m0, 0x400\n\tv_add_u32 %[v1], %[step], %[v1]\n\t" \
  "v_add_u32 %[prog], 4, %[prog]\n\ts_sub_u32 %[cnt], %[cnt], 1\n\ts_cbranch_scc0 1b"
template <int W>
__device__ __forceinline__ void dma_tile(u32 dst, const void* base, u32 v0, u32 v1, u32 step, u32 groups, u32& prog) {
  u32 cnt = groups - 1;
  if constexpr (W > 0)
    asm volatile(DMA_TILE_BODY(DMA_THROTTLE) : [v0] "+&v"(v0), [v1] "+&v"(v1), [prog] "+&v"(prog), [cnt] "+&s"(cnt)
                 : [dst] "s"(dst), [base] "s"(base), [step] "s"(step), [vz] "v"(0u), [win] "n"(W) : "memory", "scc");
  else
    asm volatile(DMA_TILE_BODY("") : [v0] "+&v"(v0), [v1] "+&v"(v1), [prog] "+&v"(prog), [cnt] "+&s"(cnt)
                 : [dst] "s"(dst), [base] "s"(base), [step] "s"(step) : "memory", "scc");
}
template <int W>
__device__ __forceinline__ void dma_two(u32 dst, const void* base, u32 v0, u32 v1, u32& prog) {
  asm volatile("s_mov_b32 m0, %[dst]\n\t" DMA_THROTTLE "global_load_lds_dwordx4 %[v0], %[base] nt\n\ts_add_u32 m0, m0, 0x400\n\tv_add_u32 %[prog], 2, %[prog]\n\t"
               "global_load_lds_dwordx4 %[v1], %[base] nt"
               : [prog] "+&v"(prog) : [dst] "s"(dst), [base] "s"(base), [v0] "v"(v0), [v1] "v"(v1), [vz] "v"(0u), [win] "n"(W) : "memory", "scc");
}
#define DMA_ROWS_BODY(THR) \
  "s_mov_b32 m0, %[dst]\n\ts_nop 0\n1:\n\t" THR \
  "global_load_lds_dwordx4 %[v0], %[base] nt\n\tglobal_load_lds_dwordx4 %[v1], %[base] offset:1024 nt\n\t" \
  "global_load_lds_dwordx4 %[v2], %[base] offset:2048 nt\n\tglobal_load_lds_dwordx4 %[v3], %[base] offset:3072 nt\n\t" \
  "s_add_u32 m0, m0, 0x1000\n\tv_add_u32 %[v0], 0x1000, %[v0]\n\tv_add_u32 %[v1], 0x1000, %[v1]\n\tv_add_u32 %[v2], 0x1000, %[v2]\n\tv_add_u32 %[v3], 0x1000, %[v3]\n\t" \
  "v_add_u32 %[prog], 4, %[prog]\n\ts_sub_u32 %[cnt], %[cnt], 1\n\ts_cbranch_scc0 1b"
template <int W>
__device__ __forceinline__ void dma_rows(u32 dst, const void* base, u32& v0, u32& v1, u32& v2, u32& v3, u32 groups, u32& prog) {
  u32 cnt = groups - 1;
  if constexpr (W > 0)
    asm volatile(DMA_ROWS_BODY(DMA_THROTTLE) : [v0] "+&v"(v0), [v1] "+&v"(v1), [v2] "+&v"(v2), [v3] "+&v"(v3), [prog] "+&v"(prog), [cnt] "+&s"(cnt)
                 : [dst] "s"(dst), [base] "s"(base), [vz] "v"(0u), [win] "n"(W) : "memory", "scc");
  else
    asm volatile(DMA_ROWS_BODY("") : [v0] "+&v"(v0), [v1] "+&v"(v1), [v2] "+&v"(v2), [v3] "+&v"(v3), [prog] "+&v"(prog), [cnt] "+&s"(cnt)
                 : [dst] "s"(dst), [base] "s"(base) : "memory", "scc");
}

#define STAMP(i)
#define LSTAMP(i)
#define STAMP_DECL
#define STAMP_OUT(slot)
#define COUNT(i)
#define STAMP_PAR
#define STAMP_ARG

__device__ __forceinline__ void final_reduce(const u64* ws, u16* out, int c, int qd, int lane, Tag tag, int first STAMP_PAR) {
  const u64* src = ws + W_Y3 + (c * 4 + qd) * 512;
  u64 gg[8];
  for (int i = 0; i < first; ++i) __builtin_amdgcn_s_sleep(1);
#pragma unroll
  for (int i = 0; i < 8; ++i) gg[i] = ldg(src, 64 * i + lane);
#pragma unroll 1
  for (int spin = 0; spin < SPIN_MAX; ++spin) {
    bool again = false;
#pragma unroll
    for (int i = 0; i < 8; ++i)
      if (__any(gbad(gg[i], tag) != 0)) { gg[i] = ldg(src, 64 * i + lane); again = true; }
    if (!again) break;
    COUNT(15);
  }
  const float v0 = ((gval0(gg[0]) + gval0(gg[1])) + (gval0(gg[2]) + gval0(gg[3]))) + ((gval0(gg[4]) + gval0(gg[5])) + (gval0(gg[6]) + gval0(gg[7])));
  const float v1 = ((gval1(gg[0]) + gval1(gg[1])) + (gval1(gg[2]) + gval1(gg[3]))) + ((gval1(gg[4]) + gval1(gg[5])) + (gval1(gg[6]) + gval1(gg[7])));
  float v = swap16_add(v0, v1);
  v = swap32_add(v, v);
  const u32 o2 = cvtpk(v, dpp<XOR1>(v));
  if (lane < 32 && !(lane & 1)) *(u32*)(out + (lane >> 4) * 512 + 64 * c + 16 * qd + (lane & 15)) = o2;
  LSTAMP(12);
}

__global__ void __launch_bounds__(NTHR, 3)
fused_decode(const u16* __restrict__ x, const u16* __restrict__ Kc, const u16* __restrict__ Vc, const u16* __restrict__ wq,
             const u16* __restrict__ wo, const u16* __restrict__ wg, const u16* __restrict__ wu, const u16* __restrict__ wd,
             const u16* __restrict__ gw, u16* __restrict__ out, u64* __restrict__ ws) {
  __shared__ __attribute__((aligned(1024))) unsigned char lds_all[LDS_BYTES];
  asm volatile("" :: "v"((LAS unsigned char*)lds_all));
  STAMP_DECL;
  LSTAMP(0);
  u64 d_t0; asm volatile("s_memrealtime %0\n s_waitcnt lgkmcnt(0)" : "=s"(d_t0));
  asm volatile("" :: "s"(x), "s"(Kc), "s"(Vc), "s"(wq), "s"(wo), "s"(wg), "s"(wu), "s"(wd), "s"(gw), "s"(out), "s"(ws));
  const int tid = threadIdx.x, lane = tid & 63;
  const int wid = __builtin_amdgcn_readfirstlane(tid >> 6);
  const int wgid = blockIdx.x, xcd = wgid & 7, idx = wgid >> 3;
  const int hh = idx >> 3, s = idx & 7, h = xcd * 4 + hh;
  const int rq = s >> 1, par = s & 1;
  const int b = s & 1, j = s >> 1;
  const u32 r8 = lane >> 3, r4 = lane >> 4;
  const u32 sw0 = 16u * ((lane & 7) ^ (r8 & 2));
  const size_t kvoff = ((size_t)(b * 32 + h) * 4096 + 3072 + j * 256) * 256;
  u32 k0 = r4 * 256 + 16 * ((lane & 15) ^ r4), k1 = k0 ^ 64, k2 = k0 ^ 128, k3 = k0 ^ 192;
  u32 prog = 28 - WIN;
  if (wid == 0) {
    STAMP(6);
    __builtin_amdgcn_s_setprio(2);
    if (lane < 12) *lp<u32>(M_CNT + 4 * lane) = 0;
    asm volatile("s_mov_b64 exec, 1\n\ts_mov_b32 m0, %0\n\ts_nop 0\n\tglobal_load_lds_dwordx4 %1, %2\n\ts_mov_b64 exec, -1\n\t"
                 "s_mov_b32 m0, %3\n\ts_nop 0\n\tglobal_load_lds_dwordx4 %4, %5\n\tglobal_load_lds_dwordx4 %4, %5 offset:1024\n\t"
                 "s_mov_b32 m0, %6\n\ts_nop 0\n\tglobal_load_lds_dwordx4 %4, %7"
                 :: "s"(M_TAG), "v"(0u), "s"(ws + W_EPOCH), "s"(M_X), "v"(16u * lane), "s"(x), "s"(M_GW), "s"(gw) : "memory");
    STAMP(7);
    u32 dummy = 0;
    dma_tile<0>(LB_W, (const char*)wq + ((size_t)rq << 20) + 256 * h + 128 * par, r8 * 8192 + sw0, r8 * 8192 + (sw0 ^ 64) + 65536, 131072, 4, dummy);
    STAMP(8);
    dma_rows<0>(LB_K, (const char*)Kc + kvoff, k0, k1, k2, k3, 2, dummy);
  }
  u32 pf0;
  {
    u64 pc;
    asm volatile("s_getpc_b64 %0" : "=s"(pc));
    const u32 off = min((idx * 4 + (lane & 3)) * 128u, (u32)CODE_BYTES - 128u);
    pc &= ~127ull;
    if (tid >= 64 && tid < 68) asm volatile("global_load_dword %0, %1, %2" : "=v"(pf0) : "v"(off), "s"(pc) : "memory");
  }
  WG_BARRIER("B0");
  const int fgr = (int)((0x46573210u >> (4 * xcd)) & 15u) * 4 + hh;
  const bool ffn = fgr < 22;
  const int fg = ffn ? fgr : 0;
  const u32 fo = ffn ? 16u : 0u;
  int role = wid;
  asm volatile("" : "+s"(role));
  if (role == 0) {
    STAMP(1);
    const u32 sw1 = sw0 ^ 64;
    if (ffn)
      dma_tile<WIN>(LB_F, (const char*)((s & 1) ? wu : wg) + (size_t)(256 * (s >> 2) + ((s >> 1) & 1)) * (2 * FF) + 128 * fg - 64 * ((s >> 1) & 1),
                     r8 * (4 * FF) + sw0, r8 * (4 * FF) + sw1 + 8 * 4 * FF, 16 * 4 * FF, 4, prog);
    dma_rows<WIN>(LB_K + 8192, (const char*)Kc + kvoff, k0, k1, k2, k3, KN1, prog);
    prog -= WIN2 - WIN;
    dma_rows<WIN2>(LB_K + 8192 + 4096 * KN1, (const char*)Kc + kvoff, k0, k1, k2, k3, 14 - KN1, prog);
    { u64 c_; asm volatile("s_memrealtime %0\n s_waitcnt lgkmcnt(0)" : "=s"(c_)); asm volatile("" : "+s"(d_t0)); *(volatile LAS u32*)(size_t)(192) = (u32)(c_ - d_t0); }
    {
      u32 v0 = r4 * 256 + 16 * ((lane & 15) ^ (r4 << 1)), v1 = v0 ^ 128, v2 = v0, v3 = v1;
      dma_rows<WIN2>(LB_V, (const char*)Vc + kvoff, v0, v1, v2, v3, 12, prog);
      if (ffn) lds_wait(C_FC, 4);
      dma_rows<WIN2>(LB_V + 49152, (const char*)Vc + kvoff, v0, v1, v2, v3, 4, prog);
    }
    { u64 c_; asm volatile("s_memrealtime %0\n s_waitcnt lgkmcnt(0)" : "=s"(c_)); asm volatile("" : "+s"(d_t0)); *(volatile LAS u32*)(size_t)(196) = (u32)(c_ - d_t0); }
    {
      const char* base = (const char*)(wd + (size_t)43 * h * 512 + 64 * s);
      dma_tile<WIN2>(LB_D, base, r8 * 1024 + sw0, r8 * 1024 + sw1 + 8192, 16384, 1, prog);
      const u32 rmax = FF - 1 - 43 * h;
      dma_two<WIN2>(LB_D + 4096, base, min(32 + r8, rmax) * 1024 + sw0, min(40 + r8, rmax) * 1024 + sw1, prog);
    }
    lds_wait(C_QC, 4);
    dma_tile<WIN2>(LB_W, (const char*)(wo + (size_t)128 * h * 512 + 64 * s), r8 * 1024 + sw0, r8 * 1024 + sw1 + 8192, 16384, 4, prog);
    *(volatile LAS u32*)(size_t)C_ISS = 1;
    STAMP(4);
#define DRAIN(k) "s_waitcnt vmcnt(" #k ")\n\tv_add_u32 %1, " STR(WIN2) " - " #k ", %0\n\tds_write_b32 %2, %1\n\t"
    u32 t;
    asm volatile(DRAIN(28) DRAIN(24) DRAIN(20) DRAIN(16) DRAIN(12) DRAIN(8) DRAIN(4) DRAIN(0) "s_waitcnt lgkmcnt(0)" : "+v"(prog), "=&v"(t) : "v"(0u) : "memory");
    { u64 c_; asm volatile("s_memrealtime %0\n s_waitcnt lgkmcnt(0)" : "=s"(c_)); asm volatile("" : "+s"(d_t0)); *(volatile LAS u32*)(size_t)(200) = (u32)(c_ - d_t0); }
    asm volatile("s_waitcnt lgkmcnt(0)" ::: "memory");
    STAMP_OUT(8);
    return;
  }
  const int g = lane >> 4, n16 = lane & 15, q4 = n16 >> 2, p4 = lane & 3;
  u32 landed = 0;
  if (role == 9) {
    const u32x4 z4 = {0u, 0u, 0u, 0u};
    *lp<u32x4>(LB_D + 6144 + 16 * lane) = z4; *lp<u32x4>(LB_D + 7168 + 16 * lane) = z4;
    if (lane < 16) *lp<u32x4>(M_H + 16 * lane) = z4;
  }
  lds_wait(C_LANDED, 4, landed);
  if (role == 9) { u64 c_; asm volatile("s_memrealtime %0\n s_waitcnt lgkmcnt(0)" : "=s"(c_)); asm volatile("" : "+s"(d_t0)); *(volatile LAS u32*)(size_t)(204) = (u32)(c_ - d_t0); }
  const u64 ep = *lp<u64>(M_TAG) + 1;
  const Tag tag = {(u32)ep & 0x3ffu, (u32)(ep >> 10) & 0x3ffu};

  if (role == 9) {
    {
      const u32x4 gv = *lp<u32x4>(M_GW + 16 * lane), x0 = *lp<u32x4>(M_X + 16 * lane), x1 = *lp<u32x4>(M_X + 1024 + 16 * lane);
      float z0 = 0.f, z1 = 0.f;
#pragma unroll
      for (int t = 0; t < 4; ++t) {
        const float gl = bflo(gv[t]), gh = bfhi(gv[t]);
        z0 = dot2(cvtpk(bflo(x0[t]) * gl, bfhi(x0[t]) * gh), 0x3f803f80u, z0);
        z1 = dot2(cvtpk(bflo(x1[t]) * gl, bfhi(x1[t]) * gh), 0x3f803f80u, z1);
      }
      float z = swap32_add(z0, z1);
      z = swap16_add(z, z);
      z += dpp<ROR8>(z); z += dpp<ROR4>(z); z += dpp<ROR2>(z); z += dpp<ROR1>(z);
      const float gt = rbf(__builtin_amdgcn_rcpf(1.f + __expf(-rbf(z))));
      if ((lane & 31) == 0) { *lp<float>(M_G + (lane >> 5) * 8) = gt; *lp<float>(M_G + (lane >> 5) * 8 + 4) = rbf(1.f - gt); }
    }
    STAMP(1);
    lds_wait(C_LANDED, 28 + fo + 8 * FBLK, landed);
    const u32 fb = (43 * h) & ~15u, fe = fb + lane, fo2 = min(fb + 32 + lane, F1_ROW - 1);
    u64 fp[8];
#pragma unroll
    for (int i = 0; i < 8; ++i) fp[i] = ldg(ws + W_F1 + F1_ROW * i, (i & 2) ? fo2 : fe);
    lds_wait(C_P, 8);
    {
      const f32x4 a = *lp<f32x4>(M_WM), c = *lp<f32x4>(M_WM + 16), la = *lp<f32x4>(M_WL), lc = *lp<f32x4>(M_WL + 16);
      const float m = max3(max3(a[0], a[1], a[2]), max3(a[3], c[0], c[1]), max3(c[2], c[3], c[3]));
      const float L = ((la[0] + la[1]) + (la[2] + la[3])) + ((lc[0] + lc[1]) + (lc[2] + lc[3]));
      stg(ws + W_ST + ((h * 2 + b) * 4 + j) * 16, n16, gpack(m, L, tag));
    }
    STAMP(2);
    {
      u32 bad = 0;
#pragma unroll
      for (int i = 0; i < 8; ++i) bad |= gbad(fp[i], tag);
      if (__builtin_expect(__any(bad != 0), 0)) {
#pragma unroll 1
        for (int spin = 0; spin < SPIN_MAX; ++spin) {
          COUNT(14);
          bad = 0;
#pragma unroll
          for (int i = 0; i < 8; ++i) fp[i] = ldg(ws + W_F1 + F1_ROW * i, (i & 2) ? fo2 : fe);
#pragma unroll
          for (int i = 0; i < 8; ++i) bad |= gbad(fp[i], tag);
          if (!__any(bad != 0)) break;
        }
      }
      const u32 gp = cvtpk((gval0(fp[0]) + gval0(fp[1])) + (gval0(fp[2]) + gval0(fp[3])), (gval1(fp[0]) + gval1(fp[1])) + (gval1(fp[2]) + gval1(fp[3])));
      const u32 up = cvtpk((gval0(fp[4]) + gval0(fp[5])) + (gval0(fp[6]) + gval0(fp[7])), (gval1(fp[4]) + gval1(fp[5])) + (gval1(fp[6]) + gval1(fp[7])));
      const u32 hp = cvtpk(bflo(gp) * bflo(up), bfhi(gp) * bfhi(up));
      const u32 i = fe - 43 * h;
      if (i < 43) { *lp<u16>(M_H + 2 * i) = (u16)hp; *lp<u16>(M_H + 128 + 2 * i) = (u16)(hp >> 16); }
      lds_arrive(C_H);
    }
    STAMP(3);
    lds_wait(C_LANDED, 92 + fo + 8 * STBLK, landed);
    {
      const u64* src = ws + W_ST + h * 128;
      u64 s0, s1;
#pragma unroll 1
      for (int spin = 0; spin < SPIN_MAX; ++spin) {
        s0 = ldg(src, lane); s1 = ldg(src, 64 + lane);
        if (!__any((gbad(s0, tag) | gbad(s1, tag)) != 0)) break;
        COUNT(13);
      }
      const u64 sb = (lane & 8) ? s1 : s0;
      const float mj = gval0(sb);
      const float w = __builtin_amdgcn_exp2f((mj - xrow_max(mj)) * LOG2E);
      const float f = w * __builtin_amdgcn_rcpf(xrow_sum(w * gval1(sb)));
      if ((lane & 7) == 0) *lp<float>(M_F + 2 * (lane & 8) + 4 * g) = f;
      lds_arrive(C_F);
    }
    STAMP(4);
    WG_BARRIER("B1 helper");
    if (wgid == 68) {
      const u32 i0 = (lane >> 3) * 512 + ((lane >> 1) & 3) * 64 + (lane & 1) * 32;
#pragma unroll 1
      for (int spin = 0; spin < SPIN_MAX; ++spin) {
        u32 bad = 0;
#pragma unroll
        for (int i = 0; i < 4; ++i) bad |= gbad(ldg(ws + W_Q1 + 4096 * i, i0), tag);
        if (!__any(bad != 0)) { if (lane == 0) stg(ws + W_EPOCH, 0, ep); break; }
        __builtin_amdgcn_s_sleep(8);
      }
    }
    if (hh == 0 && s < 4) final_reduce(ws, out, xcd, s, lane, tag, G3FIRST STAMP_ARG);
    if (hh == 0 && s < 4) { auto T = [](int i) { return (u64)__builtin_amdgcn_readfirstlane(*(volatile LAS u32*)(size_t)(192 + 4 * i)); }; u64 now_; asm volatile("s_memrealtime %0\n s_waitcnt lgkmcnt(0)" : "=s"(now_)); u64 tgt_ = now_ + (0); u64 c_; int it_ = 0; do { asm volatile("s_memrealtime %0\n s_waitcnt lgkmcnt(0)" : "=s"(c_)); } while (c_ < tgt_ && ++it_ < 100000); }
    STAMP_OUT(9);
    return;
  }

  const int wave = role - 1, ct = wave & 3;
  const int bm = n16 & 1;
  STAMP(1);
  const u32 tile_a = 128 * (8 * g + q4) + 16 * ((2 * ct + (p4 >> 1)) ^ (((q4 >> 1) | ((g & 1) << 1)) << 1)) + 8 * (p4 & 1);
  if (wave < 4) {
    const u32 xa = M_X + 1024 * bm + 256 * rq + 16 * g;
    bf16x8 xf[4];
#pragma unroll
    for (int ks = 0; ks < 4; ++ks) xf[ks] = ld_frag(xa + 64 * ks);
    lds_wait(C_LANDED, 20, landed);
    f32x4 acc = {0.f, 0.f, 0.f, 0.f};
#pragma unroll
    for (int ks = 0; ks < 4; ++ks) acc = mma(xf[ks], ld_tr(LB_W + tile_a + 4096 * ks, LB_W + tile_a + 4096 * ks + 512), acc);
    {
      const float n0 = dpp<XOR1>(acc[0]), n1 = dpp<XOR1>(acc[1]);
      if (lane < 16) stg(ws + W_Q1 + h * 512 + rq * 64 + 32 * par + 8 * ct, 256 * bm + (n16 >> 1), gpack(bm ? n1 : acc[0], bm ? acc[1] : n0, tag));
    }
    if (wave == 0) { u64 c_; asm volatile("s_memrealtime %0\n s_waitcnt lgkmcnt(0)" : "=s"(c_)); asm volatile("" : "+s"(d_t0)); *(volatile LAS u32*)(size_t)(208) = (u32)(c_ - d_t0); }
    lds_arrive(C_QC);
    STAMP(2);
    __builtin_amdgcn_s_sleep(QSLEEP);
    const u64* src = ws + W_Q1 + h * 512 + b * 256 + 16 * wave;
    u64 g0;
#pragma unroll 1
    for (int spin = 0; spin < SPIN_MAX; ++spin) {
      g0 = ldg(src, 64 * g + n16);
      if (!__any(gbad(g0, tag) != 0)) break;
      COUNT(13);
    }
    const u32 qq = cvtpk(xrow_sum(gval0(g0)), xrow_sum(gval1(g0)));
    if (lane < 16) *lp<u32>(M_Q + 4 * (16 * wave + lane)) = qq;
    lds_arrive(C_Q);
  } else if (ffn) {
    const u32 xa = M_X + 1024 * bm + 512 * (s >> 2) + 32 * g, sel = (s & 2) ? 0x07060302u : 0x05040100u;
    u32x4 xf[4];
#pragma unroll
    for (int ks = 0; ks < 4; ++ks) {
      const u32x4 lo = *lp<u32x4>(xa + 128 * ks), hi = *lp<u32x4>(xa + 128 * ks + 16);
      xf[ks] = u32x4{__builtin_amdgcn_perm(lo[1], lo[0], sel), __builtin_amdgcn_perm(lo[3], lo[2], sel),
                     __builtin_amdgcn_perm(hi[1], hi[0], sel), __builtin_amdgcn_perm(hi[3], hi[2], sel)};
    }
    lds_wait(C_LANDED, 44, landed);
    f32x4 acc = {0.f, 0.f, 0.f, 0.f};
#pragma unroll
    for (int ks = 0; ks < 4; ++ks) acc = mma(__builtin_bit_cast(bf16x8, xf[ks]), ld_tr(LB_F + tile_a + 4096 * ks, LB_F + tile_a + 4096 * ks + 512), acc);
    if (lane < 16) stg(ws + W_F1 + ((s & 1) * 4 + (s & 2) + (s >> 2)) * F1_ROW + 64 * fg + 16 * ct, lane, gpack(acc[0], acc[1], tag));
    lds_arrive(C_FC);
    STAMP(2);
  }
  if (wave == 0) asm volatile("" : "+v"(pf0));
  STAMP(3);
  lds_wait(C_LANDED, 28 + 8 * wave + (wave >= 1 ? fo : 0u), landed);
  lds_wait(C_Q, 4);
  STAMP(4);
  float sc[8];
  {
    const u32 ka = LB_K + 256 * (32 * wave + n16) + 16 * (g ^ n16);
    f32x4 d0 = {0.f, 0.f, 0.f, 0.f}, d1 = {0.f, 0.f, 0.f, 0.f};
#pragma unroll
    for (int ks = 0; ks < 4; ++ks) {
      const bf16x8 qf = ld_frag(M_Q + 64 * ks + 16 * g);
      d0 = mma(ld_frag(ka ^ (64 * ks)), qf, d0);
      d1 = mma(ld_frag((ka ^ (64 * ks)) + 4096), qf, d1);
    }
#pragma unroll
    for (int i = 0; i < 4; i += 2) {
      const u32 a = cvtpk(d0[i], d0[i + 1]), c = cvtpk(d1[i], d1[i + 1]);
      const u32 a2 = cvtpk(bflo(a) * 0.08838834764831845f, bfhi(a) * 0.08838834764831845f);
      const u32 c2 = cvtpk(bflo(c) * 0.08838834764831845f, bfhi(c) * 0.08838834764831845f);
      sc[i] = bflo(a2); sc[i + 1] = bfhi(a2); sc[4 + i] = bflo(c2); sc[5 + i] = bfhi(c2);
    }
  }
  {
    float m = xrow_max(max3(max3(sc[0], sc[1], sc[2]), max3(sc[3], sc[4], sc[5]), max3(sc[6], sc[7], sc[7])));
    if (lane == 0) *lp<float>(M_WM + 4 * wave) = m;
    lds_arrive(C_M);
    lds_wait(C_M, 8);
    STAMP(5);
    const f32x4 a = *lp<f32x4>(M_WM), c = *lp<f32x4>(M_WM + 16);
    m = max3(max3(a[0], a[1], a[2]), max3(a[3], c[0], c[1]), max3(c[2], c[3], c[3]));
    const float mL = m * LOG2E;
    float p[8], l = 0.f;
#pragma unroll
    for (int i = 0; i < 8; ++i) { p[i] = __builtin_amdgcn_exp2f(fmaf(sc[i], LOG2E, -mL)); l += p[i]; }
    l = xrow_sum(l);
    const u32x4 pf = {cvtpk(p[0], p[1]), cvtpk(p[2], p[3]), cvtpk(p[4], p[5]), cvtpk(p[6], p[7])};
    if (n16 == 0) *lp<u32x4>(M_P + 64 * wave + 16 * g) = pf;
    if (lane == 0) *lp<float>(M_WL + 4 * wave) = l;
  }
  lds_arrive(C_P);
  lds_wait(C_P, 8);
  STAMP(6);
  f32x4 oacc = {0.f, 0.f, 0.f, 0.f};
  {
    const u32 va = LB_V + 256 * (4 * g + q4) + 16 * ((2 * wave + (p4 >> 1)) ^ ((4 * (g & 1) + q4) << 1)) + 8 * (p4 & 1);
    bf16x8 pfr[8];
#pragma unroll
    for (int t = 0; t < 8; ++t) pfr[t] = ld_frag(M_P + 64 * t + 16 * g);
    lds_wait(C_LANDED, 92 + fo + 8 * 5, landed);
#pragma unroll
    for (int t = 0; t < 6; ++t) oacc = mma(pfr[t], ld_tr(va + 8192 * t, va + 8192 * t + 4096), oacc);
    lds_wait(C_LANDED, 92 + fo + 8 * 6, landed);
    oacc = mma(pfr[6], ld_tr(va + 8192 * 6, va + 8192 * 6 + 4096), oacc);
    lds_wait(C_LANDED, 92 + fo + 8 * 7, landed);
    oacc = mma(pfr[7], ld_tr(va + 8192 * 7, va + 8192 * 7 + 4096), oacc);
  }
  {
    const float on = dpp<XOR1>(oacc[0]);
    if (lane < 16 && !(lane & 1)) stg(ws + W_O2 + ((h * 2 + b) * 4 + j) * 64 + 8 * wave, lane >> 1, gpack(oacc[0], on, tag));
  }
  STAMP(7);
  const u64* vs = ws + W_O2 + h * 512 + (wave & 1) * 256 + 16 * (wave >> 1);
  const u32 vi = 64 * p4 + (lane >> 2);
  lds_wait(C_ISS, 1);
  u64 gg = ldg(vs, vi);
  bf16x8 wof[4];
  float yb0 = 0.f, yb1 = 0.f;
  f32x4 gates;
  if (wave < 4) {
    lds_wait(C_H, 1);
    gates = *lp<f32x4>(M_G);
    lds_wait(C_LANDED, 154 + fo, landed);
    f32x4 acc = {0.f, 0.f, 0.f, 0.f};
#pragma unroll
    for (int ks = 0; ks < 2; ++ks) acc = mma(ld_frag(M_H + 128 * bm + 64 * ks + 16 * g), ld_tr(LB_D + tile_a + 4096 * ks, LB_D + tile_a + 4096 * ks + 512), acc);
    yb0 = gates[1] * acc[0]; yb1 = gates[3] * acc[1];
    lds_wait(C_LANDED, 170 + fo, landed);
#pragma unroll
    for (int ks = 0; ks < 4; ++ks) wof[ks] = ld_tr(LB_W + tile_a + 4096 * ks, LB_W + tile_a + 4096 * ks + 512);
  }
  lds_wait(C_F, 1);
  const float fq = *lp<float>(M_F + 16 * (wave & 1) + 4 * p4);
  STAMP(8);
  if (__builtin_expect(__any(gbad(gg, tag) != 0), 0)) {
#pragma unroll 1
    for (int spin = 0; spin < SPIN_MAX; ++spin) {
      COUNT(15);
      gg = ldg(vs, vi);
      if (!__any(gbad(gg, tag) != 0)) break;
      __builtin_amdgcn_s_sleep(G2SLEEP);
    }
  }
  STAMP(9);
  {
    float t0 = fq * gval0(gg), t1 = fq * gval1(gg);
    t0 += dpp<XOR1>(t0); t1 += dpp<XOR1>(t1); t0 += dpp<XOR2>(t0); t1 += dpp<XOR2>(t1);
    const u32 ob = cvtpk(t0, t1);
    if (p4 == 0) *lp<u32>(M_O + 256 * (wave & 1) + 64 * (wave >> 1) + lane) = ob;
  }
  WG_BARRIER("B1");
  STAMP(10);
  if (wave < 4) {
    f32x4 acc = {0.f, 0.f, 0.f, 0.f};
#pragma unroll
    for (int ks = 0; ks < 4; ++ks) acc = mma(ld_frag(M_O + 256 * bm + 64 * ks + 16 * g), wof[ks], acc);
    if (lane < 16) stg(ws + W_Y3 + ((s * 4 + ct) * 32 + h) * 16, lane, gpack(fmaf(gates[0], acc[0], yb0), fmaf(gates[2], acc[1], yb1), tag));
    STAMP(11);
  }
  STAMP_OUT(wave);
}
}

extern "C" void kernel_launch(void* const* d_in, const int* in_sizes, int n_in, void* d_out, int out_size, void* d_ws, size_t ws_size,
                              hipStream_t stream) {
  fused_decode<<<NWG, NTHR, 0, stream>>>((const u16*)d_in[0], (const u16*)d_in[1], (const u16*)d_in[2], (const u16*)d_in[3],
                                         (const u16*)d_in[4], (const u16*)d_in[6], (const u16*)d_in[7], (const u16*)d_in[8],
                                         (const u16*)d_in[9], (u16*)d_out, (u64*)d_ws);
}
```

```cpp
#include <hip/hip_runtime.h>
#include <stdint.h>

typedef unsigned short u16;
typedef unsigned int u32;
typedef unsigned long long u64;
typedef u32 u32x4 __attribute__((ext_vector_type(4)));
typedef short s16x4 __attribute__((ext_vector_type(4)));
typedef short s16x8 __attribute__((ext_vector_type(8)));
typedef float f32x2 __attribute__((ext_vector_type(2)));
typedef float f32x4 __attribute__((ext_vector_type(4)));
typedef __bf16 bf16x2 __attribute__((ext_vector_type(2)));
typedef __bf16 bf16x8 __attribute__((ext_vector_type(8)));
#define GAS __attribute__((address_space(1)))
#define LAS __attribute__((address_space(3)))
typedef GAS u64 gu64;

#define NO_TOUCH 1
namespace {
constexpr int NWG = 256, NTHR = 640;
constexpr int FF = 1376;
constexpr u32 W_EPOCH = 0;
constexpr u32 W_Q1 = 1024;
constexpr u32 F1_ROW = 1408;
constexpr u32 W_F1 = W_Q1 + 32 * 512;
constexpr u32 W_O2 = W_F1 + 8 * F1_ROW;
constexpr u32 W_ST = W_O2 + 32 * 512;
constexpr u32 W_Y3 = W_ST + 32 * 128;
constexpr u32 W_END = W_Y3 + 32 * 512;
constexpr size_t W_DBG = 262144;
constexpr u32 LB_M = 0;
constexpr u32 M_CNT = LB_M;
constexpr u32 M_TAG = M_CNT + 48;
constexpr u32 M_G = M_TAG + 16;
constexpr u32 M_WM = M_G + 16;
constexpr u32 M_WL = M_WM + 32;
constexpr u32 M_F = M_WL + 32;
constexpr u32 M_Q = 256;
constexpr u32 M_P = M_Q + 256;
constexpr u32 M_O = M_P + 512;
constexpr u32 M_H = M_O + 512;
constexpr u32 M_X = 2048;
constexpr u32 M_GW = M_X + 2048;
constexpr u32 LB_W = 8192;
constexpr u32 LB_D = LB_W + 16384;
constexpr u32 LB_K = LB_D + 8192;
constexpr u32 LB_V = LB_K + 65536;
constexpr u32 LB_F = LB_V + 49152;
constexpr u32 C_LANDED = M_CNT, C_F = M_CNT + 4, C_QC = M_CNT + 8, C_FC = M_CNT + 12, C_Q = M_CNT + 16, C_M = M_CNT + 20, C_P = M_CNT + 24, C_H = M_CNT + 28,
              C_ISS = M_CNT + 32;
constexpr u32 LDS_BYTES = 163840;
constexpr int SPIN_MAX = 1 << 15, LSPIN_MAX = 1 << 21;
constexpr int CODE_BYTES = 13824;
constexpr float LOG2E = 1.4426950408889634f;
#define QSLEEP 14
#define G2SLEEP 1
#define G3SLEEP 1
#define WIN 28
#define WIN2 28
#define KN1 8
#define G3DUAL 0
#define PACE 220
#define PACE_NF 100
#define TOUCH_F 3
#define TOUCH_STEP 350000
#define TOUCH_MOD 6
#define FBLK 5
#define STBLK 4
#define G3FIRST 2

__device__ __forceinline__ u32 cvtpk(float lo, float hi) {
  const f32x2 v = {lo, hi};
  u32 r = __builtin_bit_cast(u32, __builtin_convertvector(v, bf16x2));
  asm("" : "+v"(r));
  return r;
}
__device__ __forceinline__ float max2(float a, float b) { return __builtin_amdgcn_fmed3f(a, b, __builtin_inff()); }
__device__ __forceinline__ float max3(float a, float b, float c) { return max2(max2(a, b), c); }
__device__ __forceinline__ float dot2(u32 a, u32 b, float c) {
  return __builtin_amdgcn_fdot2_f32_bf16(__builtin_bit_cast(bf16x2, a), __builtin_bit_cast(bf16x2, b), c, false);
}
__device__ __forceinline__ float bflo(u32 p) { return __uint_as_float(p << 16); }
__device__ __forceinline__ float bfhi(u32 p) { return __uint_as_float(p & 0xffff0000u); }
__device__ __forceinline__ float rbf(float x) { return bflo(cvtpk(x, x)); }
template <int CTRL> __device__ __forceinline__ float dpp(float x) {
  return __builtin_bit_cast(float, __builtin_amdgcn_mov_dpp(__builtin_bit_cast(int, x), CTRL, 0xf, 0xf, true));
}
constexpr int XOR1 = 0xB1, XOR2 = 0x4E, ROR8 = 0x128, ROR4 = 0x124, ROR2 = 0x122, ROR1 = 0x121;
__device__ __forceinline__ float swap32_add(float a, float b) {
  auto t = __builtin_amdgcn_permlane32_swap(__float_as_uint(a), __float_as_uint(b), false, false);
  return __uint_as_float(t[0]) + __uint_as_float(t[1]);
}
__device__ __forceinline__ float swap16_add(float a, float b) {
  auto t = __builtin_amdgcn_permlane16_swap(__float_as_uint(a), __float_as_uint(b), false, false);
  return __uint_as_float(t[0]) + __uint_as_float(t[1]);
}
__device__ __forceinline__ float xrow_max(float x) {
  auto s = __builtin_amdgcn_permlane16_swap(__float_as_uint(x), __float_as_uint(x), false, false);
  x = max2(__uint_as_float(s[0]), __uint_as_float(s[1]));
  auto t = __builtin_amdgcn_permlane32_swap(__float_as_uint(x), __float_as_uint(x), false, false);
  return max2(__uint_as_float(t[0]), __uint_as_float(t[1]));
}
__device__ __forceinline__ float xrow_sum(float x) { x = swap16_add(x, x); return swap32_add(x, x); }
__device__ __forceinline__ u64 ldg(const u64* base, u32 i) { return __hip_atomic_load((const gu64*)((const GAS char*)base + 8u * i), __ATOMIC_RELAXED, __HIP_MEMORY_SCOPE_AGENT); }
__device__ __forceinline__ void stg(u64* base, u32 i, u64 v) { __hip_atomic_store((gu64*)((GAS char*)base + 8u * i), v, __ATOMIC_RELAXED, __HIP_MEMORY_SCOPE_AGENT); }
struct Tag { u32 a, b; };
__device__ __forceinline__ u64 gpack(float v0, float v1, Tag t) {
  const u32 lo = ((__float_as_uint(v0) + 0x200u) & 0xfffffc00u) | t.a, hi = ((__float_as_uint(v1) + 0x200u) & 0xfffffc00u) | t.b;
  return ((u64)hi << 32) | lo;
}
__device__ __forceinline__ float gval0(u64 g) { return __uint_as_float((u32)g & 0xfffffc00u); }
__device__ __forceinline__ float gval1(u64 g) { return __uint_as_float((u32)(g >> 32) & 0xfffffc00u); }
__device__ __forceinline__ u32 gbad(u64 g, Tag t) { return (((u32)g ^ t.a) | ((u32)(g >> 32) ^ t.b)) & 0x3ffu; }

template <typename T> __device__ __forceinline__ LAS T* lp(u32 a) { return (LAS T*)(size_t)a; }
__device__ __forceinline__ bf16x8 ld_frag(u32 a) { return __builtin_bit_cast(bf16x8, *lp<u32x4>(a)); }
__device__ __forceinline__ bf16x8 ld_tr(u32 a0, u32 a1) {
  const s16x4 lo = __builtin_amdgcn_ds_read_tr16_b64_v4i16(lp<s16x4>(a0)), hi = __builtin_amdgcn_ds_read_tr16_b64_v4i16(lp<s16x4>(a1));
  return __builtin_bit_cast(bf16x8, __builtin_shufflevector(lo, hi, 0, 1, 2, 3, 4, 5, 6, 7));
}
__device__ __forceinline__ f32x4 mma(bf16x8 a, bf16x8 b, f32x4 c) { return __builtin_amdgcn_mfma_f32_16x16x32_bf16(a, b, c, 0, 0, 0); }
__device__ __forceinline__ void lds_arrive(u32 c) {
  asm volatile("s_waitcnt lgkmcnt(0)\n\ts_mov_b64 exec, 1\n\tds_add_u32 %0, %1\n\ts_mov_b64 exec, -1" :: "v"(c), "v"(1u) : "memory");
}
__device__ __forceinline__ void lds_wait(u32 c, u32 n, u32& seen) {
  u32 v, cnt = LSPIN_MAX;
  asm volatile("s_cmp_ge_u32 %1, %4\n\ts_cbranch_scc1 2f\n"
               "1:\n\tds_read_b32 %0, %3\n\ts_waitcnt lgkmcnt(0)\n\tv_readfirstlane_b32 %1, %0\n\ts_cmp_ge_u32 %1, %4\n\ts_cbranch_scc1 2f\n\t"
               "s_sleep 1\n\ts_sub_u32 %2, %2, 1\n\ts_cbranch_scc0 1b\n2:"
               : "=&v"(v), "+&s"(seen), "+&s"(cnt) : "v"(c), "s"(n) : "memory", "scc");
}
__device__ __forceinline__ void lds_wait(u32 c, u32 n) { u32 seen = 0; lds_wait(c, n, seen); }
#define WG_BARRIER(id) asm volatile("s_waitcnt lgkmcnt(0)\n\ts_barrier ; " id ::: "memory")
#define POL "nt"
#define STR2(x) #x
#define STR(x) STR2(x)
#define DMA_PACE "2:\n\ts_memrealtime s[90:91]\n\ts_waitcnt lgkmcnt(0)\n\ts_lshl_b32 s90, s90, 4\n\ts_sub_u32 s90, s90, %[tn]\n\ts_cmp_lt_i32 s90, 0\n\ts_cbranch_scc1 2b\n\ts_add_u32 %[tn], %[tn], %[slot]\n\t"
#define DMA_THROTTLE0 "s_waitcnt vmcnt(%[win])\n\tds_write_b32 %[vz], %[prog]\n\t"
#define DMA_THROTTLE DMA_THROTTLE0 DMA_PACE
#define DMA_TILE_BODY(THR) \
  "s_mov_b32 m0, %[dst]\n\ts_nop 0\n1:\n\t" THR \
  "global_load_lds_dwordx4 %[v0], %[base] " POL "\n\ts_add_u32 m0, m0, 0x400\n\tv_add_u32 %[v0], %[step], %[v0]\n\t" \
  "global_load_lds_dwordx4 %[v1], %[base] " POL "\n\ts_add_u32 m0, m0, 0x400\n\tv_add_u32 %[v1], %[step], %[v1]\n\t" \
  "global_load_lds_dwordx4 %[v0], %[base] " POL "\n\ts_add_u32 m0, m0, 0x400\n\tv_add_u32 %[v0], %[step], %[v0]\n\t" \
  "global_load_lds_dwordx4 %[v1], %[base] " POL "\n\ts_add_u32 m0, m0, 0x400\n\tv_add_u32 %[v1], %[step], %[v1]\n\t" \
  "v_add_u32 %[prog], 4, %[prog]\n\ts_sub_u32 %[cnt], %[cnt], 1\n\ts_cbranch_scc0 1b"
template <int W>
__device__ __forceinline__ void dma_tile(u32 dst, const void* base, u32 v0, u32 v1, u32 step, u32 groups, u32& prog, u32& tn, u32 slot) {
  u32 cnt = groups - 1;
  if constexpr (W > 0)
    asm volatile(DMA_TILE_BODY(DMA_THROTTLE) : [v0] "+&v"(v0), [v1] "+&v"(v1), [prog] "+&v"(prog), [cnt] "+&s"(cnt), [tn] "+&s"(tn)
                 : [dst] "s"(dst), [base] "s"(base), [step] "s"(step), [vz] "v"(0u), [win] "n"(W), [slot] "s"(slot) : "memory", "scc", "s90", "s91");
  else
    asm volatile(DMA_TILE_BODY("") : [v0] "+&v"(v0), [v1] "+&v"(v1), [prog] "+&v"(prog), [cnt] "+&s"(cnt)
                 : [dst] "s"(dst), [base] "s"(base), [step] "s"(step) : "memory", "scc");
}
template <int W>
__device__ __forceinline__ void dma_two(u32 dst, const void* base, u32 v0, u32 v1, u32& prog) {
  asm volatile("s_mov_b32 m0, %[dst]\n\t" DMA_THROTTLE0 "global_load_lds_dwordx4 %[v0], %[base] " POL "\n\ts_add_u32 m0, m0, 0x400\n\tv_add_u32 %[prog], 2, %[prog]\n\t"
               "global_load_lds_dwordx4 %[v1], %[base] " POL ""
               : [prog] "+&v"(prog) : [dst] "s"(dst), [base] "s"(base), [v0] "v"(v0), [v1] "v"(v1), [vz] "v"(0u), [win] "n"(W) : "memory", "scc");
}
#define DMA_ROWS_BODY(THR) \
  "s_mov_b32 m0, %[dst]\n\ts_nop 0\n1:\n\t" THR \
  "global_load_lds_dwordx4 %[v0], %[base] " POL "\n\tglobal_load_lds_dwordx4 %[v1], %[base] offset:1024 " POL "\n\t" \
  "global_load_lds_dwordx4 %[v2], %[base] offset:2048 " POL "\n\tglobal_load_lds_dwordx4 %[v3], %[base] offset:3072 " POL "\n\t" \
  "s_add_u32 m0, m0, 0x1000\n\tv_add_u32 %[v0], 0x1000, %[v0]\n\tv_add_u32 %[v1], 0x1000, %[v1]\n\tv_add_u32 %[v2], 0x1000, %[v2]\n\tv_add_u32 %[v3], 0x1000, %[v3]\n\t" \
  "v_add_u32 %[prog], 4, %[prog]\n\ts_sub_u32 %[cnt], %[cnt], 1\n\ts_cbranch_scc0 1b"
template <int W>
__device__ __forceinline__ void dma_rows(u32 dst, const void* base, u32& v0, u32& v1, u32& v2, u32& v3, u32 groups, u32& prog, u32& tn, u32 slot) {
  u32 cnt = groups - 1;
  if constexpr (W > 0)
    asm volatile(DMA_ROWS_BODY(DMA_THROTTLE) : [v0] "+&v"(v0), [v1] "+&v"(v1), [v2] "+&v"(v2), [v3] "+&v"(v3), [prog] "+&v"(prog), [cnt] "+&s"(cnt), [tn] "+&s"(tn)
                 : [dst] "s"(dst), [base] "s"(base), [vz] "v"(0u), [win] "n"(W), [slot] "s"(slot) : "memory", "scc", "s90", "s91");
  else
    asm volatile(DMA_ROWS_BODY("") : [v0] "+&v"(v0), [v1] "+&v"(v1), [v2] "+&v"(v2), [v3] "+&v"(v3), [prog] "+&v"(prog), [cnt] "+&s"(cnt)
                 : [dst] "s"(dst), [base] "s"(base) : "memory", "scc");
}

#define STAMP(i)
#define LSTAMP(i)
#define STAMP_DECL
#define STAMP_OUT(slot)
#define COUNT(i)
#define STAMP_PAR
#define STAMP_ARG

__device__ __forceinline__ void final_reduce(const u64* ws, u16* out, int c, int qd, int lane, Tag tag, int first STAMP_PAR) {
  const u64* src = ws + W_Y3 + (c * 4 + qd) * 512;
  u64 gg[8];
  for (int i = 0; i < first; ++i) __builtin_amdgcn_s_sleep(1);
#pragma unroll
  for (int i = 0; i < 8; ++i) gg[i] = ldg(src, 64 * i + lane);
#pragma unroll 1
  for (int spin = 0; spin < SPIN_MAX; ++spin) {
    bool again = false;
#pragma unroll
    for (int i = 0; i < 8; ++i)
      if (__any(gbad(gg[i], tag) != 0)) { gg[i] = ldg(src, 64 * i + lane); again = true; }
    if (!again) break;
    COUNT(15);
  }
  const float v0 = ((gval0(gg[0]) + gval0(gg[1])) + (gval0(gg[2]) + gval0(gg[3]))) + ((gval0(gg[4]) + gval0(gg[5])) + (gval0(gg[6]) + gval0(gg[7])));
  const float v1 = ((gval1(gg[0]) + gval1(gg[1])) + (gval1(gg[2]) + gval1(gg[3]))) + ((gval1(gg[4]) + gval1(gg[5])) + (gval1(gg[6]) + gval1(gg[7])));
  float v = swap16_add(v0, v1);
  v = swap32_add(v, v);
  const u32 o2 = cvtpk(v, dpp<XOR1>(v));
  if (lane < 32 && !(lane & 1)) *(u32*)(out + (lane >> 4) * 512 + 64 * c + 16 * qd + (lane & 15)) = o2;
  LSTAMP(12);
}

__global__ void __launch_bounds__(NTHR, 3)
fused_decode(const u16* __restrict__ x, const u16* __restrict__ Kc, const u16* __restrict__ Vc, const u16* __restrict__ wq,
             const u16* __restrict__ wo, const u16* __restrict__ wg, const u16* __restrict__ wu, const u16* __restrict__ wd,
             const u16* __restrict__ gw, u16* __restrict__ out, u64* __restrict__ ws) {
  __shared__ __attribute__((aligned(1024))) unsigned char lds_all[LDS_BYTES];
  asm volatile("" :: "v"((LAS unsigned char*)lds_all));
  STAMP_DECL;
  LSTAMP(0);
  asm volatile("" :: "s"(x), "s"(Kc), "s"(Vc), "s"(wq), "s"(wo), "s"(wg), "s"(wu), "s"(wd), "s"(gw), "s"(out), "s"(ws));
  const int tid = threadIdx.x, lane = tid & 63;
  const int wid = __builtin_amdgcn_readfirstlane(tid >> 6);
  const int wgid = blockIdx.x, xcd = wgid & 7, idx = wgid >> 3;
  const int hh = idx >> 3, s = idx & 7, h = xcd * 4 + hh;
  const int rq = s >> 1, par = s & 1;
  const int b = s & 1, j = s >> 1;
  const u32 r8 = lane >> 3, r4 = lane >> 4;
  const u32 sw0 = 16u * ((lane & 7) ^ (r8 & 2));
  const size_t kvoff = ((size_t)(b * 32 + h) * 4096 + 3072 + j * 256) * 256;
  u32 k0 = r4 * 256 + 16 * ((lane & 15) ^ r4), k1 = k0 ^ 64, k2 = k0 ^ 128, k3 = k0 ^ 192;
  u32 prog = 28 - WIN;
  if (wid == 0) {
    STAMP(6);
    __builtin_amdgcn_s_setprio(2);
    if (lane < 12) *lp<u32>(M_CNT + 4 * lane) = 0;
    asm volatile("s_mov_b64 exec, 1\n\ts_mov_b32 m0, %0\n\ts_nop 0\n\tglobal_load_lds_dwordx4 %1, %2\n\ts_mov_b64 exec, -1\n\t"
                 "s_mov_b32 m0, %3\n\ts_nop 0\n\tglobal_load_lds_dwordx4 %4, %5\n\tglobal_load_lds_dwordx4 %4, %5 offset:1024\n\t"
                 "s_mov_b32 m0, %6\n\ts_nop 0\n\tglobal_load_lds_dwordx4 %4, %7"
                 :: "s"(M_TAG), "v"(0u), "s"(ws + W_EPOCH), "s"(M_X), "v"(16u * lane), "s"(x), "s"(M_GW), "s"(gw) : "memory");
    STAMP(7);
    u32 dummy = 0, dummy2 = 0;
    dma_tile<0>(LB_W, (const char*)wq + ((size_t)rq << 20) + 256 * h + 128 * par, r8 * 8192 + sw0, r8 * 8192 + (sw0 ^ 64) + 65536, 131072, 4, dummy, dummy2, 0);
    STAMP(8);
    dma_rows<0>(LB_K, (const char*)Kc + kvoff, k0, k1, k2, k3, 2, dummy, dummy2, 0);
  }
  u32 pf0;
  {
    u64 pc;
    asm volatile("s_getpc_b64 %0" : "=s"(pc));
    const u32 off = min((idx * 4 + (lane & 3)) * 128u, (u32)CODE_BYTES - 128u);
    pc &= ~127ull;
    if (tid >= 64 && tid < 68) asm volatile("global_load_dword %0, %1, %2" : "=v"(pf0) : "v"(off), "s"(pc) : "memory");
  }
  WG_BARRIER("B0");
  const int fgr = (int)((0x46573210u >> (4 * xcd)) & 15u) * 4 + hh;
  const bool ffn = fgr < 22;
  const int fg = ffn ? fgr : 0;
  const u32 fo = ffn ? 16u : 0u;
  int role = wid;
  asm volatile("" : "+s"(role));
  if (role == 0) {
    STAMP(1);
    const u32 sw1 = sw0 ^ 64;
    u32 tn = 0;
    const u32 slot = ffn ? PACE : PACE * PACE_NF / 100;
    { u64 now; asm volatile("s_memrealtime %0\n\ts_waitcnt lgkmcnt(0)" : "=s"(now)); tn = (u32)now << 4; }
    if (ffn)
      dma_tile<WIN>(LB_F, (const char*)((s & 1) ? wu : wg) + (size_t)(256 * (s >> 2) + ((s >> 1) & 1)) * (2 * FF) + 128 * fg - 64 * ((s >> 1) & 1),
                     r8 * (4 * FF) + sw0, r8 * (4 * FF) + sw1 + 8 * 4 * FF, 16 * 4 * FF, 4, prog, tn, slot);
    dma_rows<WIN>(LB_K + 8192, (const char*)Kc + kvoff, k0, k1, k2, k3, KN1, prog, tn, slot);
    prog -= WIN2 - WIN;
    dma_rows<WIN2>(LB_K + 8192 + 4096 * KN1, (const char*)Kc + kvoff, k0, k1, k2, k3, 14 - KN1, prog, tn, slot);
    STAMP(2);
    {
      u32 v0 = r4 * 256 + 16 * ((lane & 15) ^ (r4 << 1)), v1 = v0 ^ 128, v2 = v0, v3 = v1;
      dma_rows<WIN2>(LB_V, (const char*)Vc + kvoff, v0, v1, v2, v3, 12, prog, tn, slot);
      if (ffn) lds_wait(C_FC, 4);
      dma_rows<WIN2>(LB_V + 49152, (const char*)Vc + kvoff, v0, v1, v2, v3, 4, prog, tn, slot);
    }
    STAMP(3);
    {
      const char* base = (const char*)(wd + (size_t)43 * h * 512 + 64 * s);
      dma_tile<WIN2>(LB_D, base, r8 * 1024 + sw0, r8 * 1024 + sw1 + 8192, 16384, 1, prog, tn, slot);
      const u32 rmax = FF - 1 - 43 * h;
      dma_two<WIN2>(LB_D + 4096, base, min(32 + r8, rmax) * 1024 + sw0, min(40 + r8, rmax) * 1024 + sw1, prog);
    }
    lds_wait(C_QC, 4);
    dma_tile<WIN2>(LB_W, (const char*)(wo + (size_t)128 * h * 512 + 64 * s), r8 * 1024 + sw0, r8 * 1024 + sw1 + 8192, 16384, 4, prog, tn, slot);
    *(volatile LAS u32*)(size_t)C_ISS = 1;
    STAMP(4);
#define DRAIN(k) "s_waitcnt vmcnt(" #k ")\n\tv_add_u32 %1, " STR(WIN2) " - " #k ", %0\n\tds_write_b32 %2, %1\n\t"
    u32 t;
    asm volatile(DRAIN(28) DRAIN(24) DRAIN(20) DRAIN(16) DRAIN(12) DRAIN(8) DRAIN(4) DRAIN(0) "s_waitcnt lgkmcnt(0)" : "+v"(prog), "=&v"(t) : "v"(0u) : "memory");
    LSTAMP(5);
    STAMP_OUT(8);
    return;
  }
  const int g = lane >> 4, n16 = lane & 15, q4 = n16 >> 2, p4 = lane & 3;
  u32 landed = 0;
  if (role == 9) {
    const u32x4 z4 = {0u, 0u, 0u, 0u};
    *lp<u32x4>(LB_D + 6144 + 16 * lane) = z4; *lp<u32x4>(LB_D + 7168 + 16 * lane) = z4;
    if (lane < 16) *lp<u32x4>(M_H + 16 * lane) = z4;
  }
  lds_wait(C_LANDED, 4, landed);
  const u64 ep = *lp<u64>(M_TAG) + 1;
  const Tag tag = {(u32)ep & 0x3ffu, (u32)(ep >> 10) & 0x3ffu};

  if (role == 9) {
    {
      const u32x4 gv = *lp<u32x4>(M_GW + 16 * lane), x0 = *lp<u32x4>(M_X + 16 * lane), x1 = *lp<u32x4>(M_X + 1024 + 16 * lane);
      float z0 = 0.f, z1 = 0.f;
#pragma unroll
      for (int t = 0; t < 4; ++t) {
        const float gl = bflo(gv[t]), gh = bfhi(gv[t]);
        z0 = dot2(cvtpk(bflo(x0[t]) * gl, bfhi(x0[t]) * gh), 0x3f803f80u, z0);
        z1 = dot2(cvtpk(bflo(x1[t]) * gl, bfhi(x1[t]) * gh), 0x3f803f80u, z1);
      }
      float z = swap32_add(z0, z1);
      z = swap16_add(z, z);
      z += dpp<ROR8>(z); z += dpp<ROR4>(z); z += dpp<ROR2>(z); z += dpp<ROR1>(z);
      const float gt = rbf(__builtin_amdgcn_rcpf(1.f + __expf(-rbf(z))));
      if ((lane & 31) == 0) { *lp<float>(M_G + (lane >> 5) * 8) = gt; *lp<float>(M_G + (lane >> 5) * 8 + 4) = rbf(1.f - gt); }
    }
    STAMP(1);
    lds_wait(C_LANDED, 28 + fo + 8 * FBLK, landed);
    const u32 fb = (43 * h) & ~15u, fe = fb + lane, fo2 = min(fb + 32 + lane, F1_ROW - 1);
    u64 fp[8];
#pragma unroll
    for (int i = 0; i < 8; ++i) fp[i] = ldg(ws + W_F1 + F1_ROW * i, (i & 2) ? fo2 : fe);
    lds_wait(C_P, 8);
    {
      const f32x4 a = *lp<f32x4>(M_WM), c = *lp<f32x4>(M_WM + 16), la = *lp<f32x4>(M_WL), lc = *lp<f32x4>(M_WL + 16);
      const float m = max3(max3(a[0], a[1], a[2]), max3(a[3], c[0], c[1]), max3(c[2], c[3], c[3]));
      const float L = ((la[0] + la[1]) + (la[2] + la[3])) + ((lc[0] + lc[1]) + (lc[2] + lc[3]));
      stg(ws + W_ST + ((h * 2 + b) * 4 + j) * 16, n16, gpack(m, L, tag));
    }
    STAMP(2);
    {
      u32 bad = 0;
#pragma unroll
      for (int i = 0; i < 8; ++i) bad |= gbad(fp[i], tag);
      if (__builtin_expect(__any(bad != 0), 0)) {
#pragma unroll 1
        for (int spin = 0; spin < SPIN_MAX; ++spin) {
          COUNT(14);
          bad = 0;
#pragma unroll
          for (int i = 0; i < 8; ++i) fp[i] = ldg(ws + W_F1 + F1_ROW * i, (i & 2) ? fo2 : fe);
#pragma unroll
          for (int i = 0; i < 8; ++i) bad |= gbad(fp[i], tag);
          if (!__any(bad != 0)) break;
        }
      }
      const u32 gp = cvtpk((gval0(fp[0]) + gval0(fp[1])) + (gval0(fp[2]) + gval0(fp[3])), (gval1(fp[0]) + gval1(fp[1])) + (gval1(fp[2]) + gval1(fp[3])));
      const u32 up = cvtpk((gval0(fp[4]) + gval0(fp[5])) + (gval0(fp[6]) + gval0(fp[7])), (gval1(fp[4]) + gval1(fp[5])) + (gval1(fp[6]) + gval1(fp[7])));
      const u32 hp = cvtpk(bflo(gp) * bflo(up), bfhi(gp) * bfhi(up));
      const u32 i = fe - 43 * h;
      if (i < 43) { *lp<u16>(M_H + 2 * i) = (u16)hp; *lp<u16>(M_H + 128 + 2 * i) = (u16)(hp >> 16); }
      lds_arrive(C_H);
    }
    STAMP(3);
    lds_wait(C_LANDED, 92 + fo + 8 * STBLK, landed);
    {
      const u64* src = ws + W_ST + h * 128;
      u64 s0, s1;
#pragma unroll 1
      for (int spin = 0; spin < SPIN_MAX; ++spin) {
        s0 = ldg(src, lane); s1 = ldg(src, 64 + lane);
        if (!__any((gbad(s0, tag) | gbad(s1, tag)) != 0)) break;
        COUNT(13);
      }
      const u64 sb = (lane & 8) ? s1 : s0;
      const float mj = gval0(sb);
      const float w = __builtin_amdgcn_exp2f((mj - xrow_max(mj)) * LOG2E);
      const float f = w * __builtin_amdgcn_rcpf(xrow_sum(w * gval1(sb)));
      if ((lane & 7) == 0) *lp<float>(M_F + 2 * (lane & 8) + 4 * g) = f;
      lds_arrive(C_F);
    }
    STAMP(4);
    WG_BARRIER("B1 helper");
    if (wgid == 68) {
      const u32 i0 = (lane >> 3) * 512 + ((lane >> 1) & 3) * 64 + (lane & 1) * 32;
#pragma unroll 1
      for (int spin = 0; spin < SPIN_MAX; ++spin) {
        u32 bad = 0;
#pragma unroll
        for (int i = 0; i < 4; ++i) bad |= gbad(ldg(ws + W_Q1 + 4096 * i, i0), tag);
        if (!__any(bad != 0)) { if (lane == 0) stg(ws + W_EPOCH, 0, ep); break; }
        __builtin_amdgcn_s_sleep(8);
      }
    }
    if (hh == 0 && s < 4) final_reduce(ws, out, xcd, s, lane, tag, G3FIRST STAMP_ARG);
    STAMP_OUT(9);
    return;
  }

  const int wave = role - 1, ct = wave & 3;
  const int bm = n16 & 1;
  STAMP(1);
  const u32 tile_a = 128 * (8 * g + q4) + 16 * ((2 * ct + (p4 >> 1)) ^ (((q4 >> 1) | ((g & 1) << 1)) << 1)) + 8 * (p4 & 1);
  if (wave < 4) {
    const u32 xa = M_X + 1024 * bm + 256 * rq + 16 * g;
    bf16x8 xf[4];
#pragma unroll
    for (int ks = 0; ks < 4; ++ks) xf[ks] = ld_frag(xa + 64 * ks);
    lds_wait(C_LANDED, 20, landed);
    f32x4 acc = {0.f, 0.f, 0.f, 0.f};
#pragma unroll
    for (int ks = 0; ks < 4; ++ks) acc = mma(xf[ks], ld_tr(LB_W + tile_a + 4096 * ks, LB_W + tile_a + 4096 * ks + 512), acc);
    {
      const float n0 = dpp<XOR1>(acc[0]), n1 = dpp<XOR1>(acc[1]);
      if (lane < 16) stg(ws + W_Q1 + h * 512 + rq * 64 + 32 * par + 8 * ct, 256 * bm + (n16 >> 1), gpack(bm ? n1 : acc[0], bm ? acc[1] : n0, tag));
    }
    lds_arrive(C_QC);
    STAMP(2);
    __builtin_amdgcn_s_sleep(QSLEEP);
    const u64* src = ws + W_Q1 + h * 512 + b * 256 + 16 * wave;
    u64 g0;
#pragma unroll 1
    for (int spin = 0; spin < SPIN_MAX; ++spin) {
      g0 = ldg(src, 64 * g + n16);
      if (!__any(gbad(g0, tag) != 0)) break;
      COUNT(13);
    }
    const u32 qq = cvtpk(xrow_sum(gval0(g0)), xrow_sum(gval1(g0)));
    if (lane < 16) *lp<u32>(M_Q + 4 * (16 * wave + lane)) = qq;
    lds_arrive(C_Q);
  } else if (ffn) {
    const u32 xa = M_X + 1024 * bm + 512 * (s >> 2) + 32 * g, sel = (s & 2) ? 0x07060302u : 0x05040100u;
    u32x4 xf[4];
#pragma unroll
    for (int ks = 0; ks < 4; ++ks) {
      const u32x4 lo = *lp<u32x4>(xa + 128 * ks), hi = *lp<u32x4>(xa + 128 * ks + 16);
      xf[ks] = u32x4{__builtin_amdgcn_perm(lo[1], lo[0], sel), __builtin_amdgcn_perm(lo[3], lo[2], sel),
                     __builtin_amdgcn_perm(hi[1], hi[0], sel), __builtin_amdgcn_perm(hi[3], hi[2], sel)};
    }
    lds_wait(C_LANDED, 44, landed);
    f32x4 acc = {0.f, 0.f, 0.f, 0.f};
#pragma unroll
    for (int ks = 0; ks < 4; ++ks) acc = mma(__builtin_bit_cast(bf16x8, xf[ks]), ld_tr(LB_F + tile_a + 4096 * ks, LB_F + tile_a + 4096 * ks + 512), acc);
    if (lane < 16) stg(ws + W_F1 + ((s & 1) * 4 + (s & 2) + (s >> 2)) * F1_ROW + 64 * fg + 16 * ct, lane, gpack(acc[0], acc[1], tag));
    lds_arrive(C_FC);
    STAMP(2);
  }
  if (wave == 0) asm volatile("" : "+v"(pf0));
  STAMP(3);
  lds_wait(C_LANDED, 28 + 8 * wave + (wave >= 1 ? fo : 0u), landed);
  lds_wait(C_Q, 4);
  STAMP(4);
  float sc[8];
  {
    const u32 ka = LB_K + 256 * (32 * wave + n16) + 16 * (g ^ n16);
    f32x4 d0 = {0.f, 0.f, 0.f, 0.f}, d1 = {0.f, 0.f, 0.f, 0.f};
#pragma unroll
    for (int ks = 0; ks < 4; ++ks) {
      const bf16x8 qf = ld_frag(M_Q + 64 * ks + 16 * g);
      d0 = mma(ld_frag(ka ^ (64 * ks)), qf, d0);
      d1 = mma(ld_frag((ka ^ (64 * ks)) + 4096), qf, d1);
    }
#pragma unroll
    for (int i = 0; i < 4; i += 2) {
      const u32 a = cvtpk(d0[i], d0[i + 1]), c = cvtpk(d1[i], d1[i + 1]);
      const u32 a2 = cvtpk(bflo(a) * 0.08838834764831845f, bfhi(a) * 0.08838834764831845f);
      const u32 c2 = cvtpk(bflo(c) * 0.08838834764831845f, bfhi(c) * 0.08838834764831845f);
      sc[i] = bflo(a2); sc[i + 1] = bfhi(a2); sc[4 + i] = bflo(c2); sc[5 + i] = bfhi(c2);
    }
  }
  {
    float m = xrow_max(max3(max3(sc[0], sc[1], sc[2]), max3(sc[3], sc[4], sc[5]), max3(sc[6], sc[7], sc[7])));
    if (lane == 0) *lp<float>(M_WM + 4 * wave) = m;
    lds_arrive(C_M);
    lds_wait(C_M, 8);
    STAMP(5);
    const f32x4 a = *lp<f32x4>(M_WM), c = *lp<f32x4>(M_WM + 16);
    m = max3(max3(a[0], a[1], a[2]), max3(a[3], c[0], c[1]), max3(c[2], c[3], c[3]));
    const float mL = m * LOG2E;
    float p[8], l = 0.f;
#pragma unroll
    for (int i = 0; i < 8; ++i) { p[i] = __builtin_amdgcn_exp2f(fmaf(sc[i], LOG2E, -mL)); l += p[i]; }
    l = xrow_sum(l);
    const u32x4 pf = {cvtpk(p[0], p[1]), cvtpk(p[2], p[3]), cvtpk(p[4], p[5]), cvtpk(p[6], p[7])};
    if (n16 == 0) *lp<u32x4>(M_P + 64 * wave + 16 * g) = pf;
    if (lane == 0) *lp<float>(M_WL + 4 * wave) = l;
  }
  lds_arrive(C_P);
  lds_wait(C_P, 8);
  STAMP(6);
  f32x4 oacc = {0.f, 0.f, 0.f, 0.f};
  {
    const u32 va = LB_V + 256 * (4 * g + q4) + 16 * ((2 * wave + (p4 >> 1)) ^ ((4 * (g & 1) + q4) << 1)) + 8 * (p4 & 1);
    bf16x8 pfr[8];
#pragma unroll
    for (int t = 0; t < 8; ++t) pfr[t] = ld_frag(M_P + 64 * t + 16 * g);
    lds_wait(C_LANDED, 92 + fo + 8 * 5, landed);
#pragma unroll
    for (int t = 0; t < 6; ++t) oacc = mma(pfr[t], ld_tr(va + 8192 * t, va + 8192 * t + 4096), oacc);
    lds_wait(C_LANDED, 92 + fo + 8 * 6, landed);
    oacc = mma(pfr[6], ld_tr(va + 8192 * 6, va + 8192 * 6 + 4096), oacc);
    lds_wait(C_LANDED, 92 + fo + 8 * 7, landed);
    oacc = mma(pfr[7], ld_tr(va + 8192 * 7, va + 8192 * 7 + 4096), oacc);
  }
  {
    const float on = dpp<XOR1>(oacc[0]);
    if (lane < 16 && !(lane & 1)) stg(ws + W_O2 + ((h * 2 + b) * 4 + j) * 64 + 8 * wave, lane >> 1, gpack(oacc[0], on, tag));
  }
  STAMP(7);
  const u64* vs = ws + W_O2 + h * 512 + (wave & 1) * 256 + 16 * (wave >> 1);
  const u32 vi = 64 * p4 + (lane >> 2);
  lds_wait(C_ISS, 1);
  u64 gg = ldg(vs, vi);
  bf16x8 wof[4];
  float yb0 = 0.f, yb1 = 0.f;
  f32x4 gates;
  if (wave < 4) {
    lds_wait(C_H, 1);
    gates = *lp<f32x4>(M_G);
    lds_wait(C_LANDED, 154 + fo, landed);
    f32x4 acc = {0.f, 0.f, 0.f, 0.f};
#pragma unroll
    for (int ks = 0; ks < 2; ++ks) acc = mma(ld_frag(M_H + 128 * bm + 64 * ks + 16 * g), ld_tr(LB_D + tile_a + 4096 * ks, LB_D + tile_a + 4096 * ks + 512), acc);
    yb0 = gates[1] * acc[0]; yb1 = gates[3] * acc[1];
    lds_wait(C_LANDED, 170 + fo, landed);
#pragma unroll
    for (int ks = 0; ks < 4; ++ks) wof[ks] = ld_tr(LB_W + tile_a + 4096 * ks, LB_W + tile_a + 4096 * ks + 512);
  }
  lds_wait(C_F, 1);
  const float fq = *lp<float>(M_F + 16 * (wave & 1) + 4 * p4);
  STAMP(8);
  if (__builtin_expect(__any(gbad(gg, tag) != 0), 0)) {
#pragma unroll 1
    for (int spin = 0; spin < SPIN_MAX; ++spin) {
      COUNT(15);
      gg = ldg(vs, vi);
      if (!__any(gbad(gg, tag) != 0)) break;
      __builtin_amdgcn_s_sleep(G2SLEEP);
    }
  }
  STAMP(9);
  {
    float t0 = fq * gval0(gg), t1 = fq * gval1(gg);
    t0 += dpp<XOR1>(t0); t1 += dpp<XOR1>(t1); t0 += dpp<XOR2>(t0); t1 += dpp<XOR2>(t1);
    const u32 ob = cvtpk(t0, t1);
    if (p4 == 0) *lp<u32>(M_O + 256 * (wave & 1) + 64 * (wave >> 1) + lane) = ob;
  }
  WG_BARRIER("B1");
  STAMP(10);
  if (wave < 4) {
    f32x4 acc = {0.f, 0.f, 0.f, 0.f};
#pragma unroll
    for (int ks = 0; ks < 4; ++ks) acc = mma(ld_frag(M_O + 256 * bm + 64 * ks + 16 * g), wof[ks], acc);
    if (lane < 16) stg(ws + W_Y3 + ((s * 4 + ct) * 32 + h) * 16, lane, gpack(fmaf(gates[0], acc[0], yb0), fmaf(gates[2], acc[1], yb1), tag));
    STAMP(11);
  }
  STAMP_OUT(wave);
}
}

extern "C" void kernel_launch(void* const* d_in, const int* in_sizes, int n_in, void* d_out, int out_size, void* d_ws, size_t ws_size,
                              hipStream_t stream) {
  fused_decode<<<NWG, NTHR, 0, stream>>>((const u16*)d_in[0], (const u16*)d_in[1], (const u16*)d_in[2], (const u16*)d_in[3],
                                         (const u16*)d_in[4], (const u16*)d_in[6], (const u16*)d_in[7], (const u16*)d_in[8],
                                         (const u16*)d_in[9], (u16*)d_out, (u64*)d_ws);
}
```

```cpp
#include <hip/hip_runtime.h>
#include <stdint.h>

typedef unsigned short u16;
typedef unsigned int u32;
typedef unsigned long long u64;
typedef u32 u32x4 __attribute__((ext_vector_type(4)));
typedef short s16x4 __attribute__((ext_vector_type(4)));
typedef short s16x8 __attribute__((ext_vector_type(8)));
typedef float f32x2 __attribute__((ext_vector_type(2)));
typedef float f32x4 __attribute__((ext_vector_type(4)));
typedef __bf16 bf16x2 __attribute__((ext_vector_type(2)));
typedef __bf16 bf16x8 __attribute__((ext_vector_type(8)));
#define GAS __attribute__((address_space(1)))
#define LAS __attribute__((address_space(3)))
typedef GAS u64 gu64;

namespace {
constexpr int NWG = 256, NTHR = 640;
constexpr int FF = 1376;
constexpr u32 W_EPOCH = 0;
constexpr u32 W_Q1 = 1024;
constexpr u32 F1_ROW = 1408;
constexpr u32 W_F1 = W_Q1 + 32 * 512;
constexpr u32 W_O2 = W_F1 + 8 * F1_ROW;
constexpr u32 W_ST = W_O2 + 32 * 512;
constexpr u32 W_Y3 = W_ST + 32 * 128;
constexpr u32 W_END = W_Y3 + 32 * 512;
constexpr u32 LB_M = 0;
constexpr u32 M_CNT = LB_M;
constexpr u32 M_TAG = M_CNT + 48;
constexpr u32 M_G = M_TAG + 16;
constexpr u32 M_WM = M_G + 16;
constexpr u32 M_WL = M_WM + 32;
constexpr u32 M_F = M_WL + 32;
constexpr u32 M_Q = 256;
constexpr u32 M_P = M_Q + 256;
constexpr u32 M_O = M_P + 512;
constexpr u32 M_H = M_O + 512;
constexpr u32 M_X = 2048;
constexpr u32 M_GW = M_X + 2048;
constexpr u32 LB_W = 8192;
constexpr u32 LB_D = LB_W + 16384;
constexpr u32 LB_K = LB_D + 8192;
constexpr u32 LB_V = LB_K + 65536;
constexpr u32 LB_F = LB_V + 49152;
constexpr u32 C_LANDED = M_CNT, C_F = M_CNT + 4, C_QC = M_CNT + 8, C_FC = M_CNT + 12, C_Q = M_CNT + 16, C_M = M_CNT + 20, C_P = M_CNT + 24, C_H = M_CNT + 28,
              C_ISS = M_CNT + 32;
constexpr u32 LDS_BYTES = 163840;
constexpr int SPIN_MAX = 1 << 15, LSPIN_MAX = 1 << 21;
constexpr int CODE_BYTES = 15872;
constexpr float LOG2E = 1.4426950408889634f;
#define WIN 28
constexpr u32 PACE = 220;
constexpr int QSLEEP = 22;
constexpr int FBLK = 5, STBLK = 4;

__device__ __forceinline__ u32 cvtpk(float lo, float hi) {
  const f32x2 v = {lo, hi};
  u32 r = __builtin_bit_cast(u32, __builtin_convertvector(v, bf16x2));
  asm("" : "+v"(r));
  return r;
}
__device__ __forceinline__ float max2(float a, float b) { return __builtin_amdgcn_fmed3f(a, b, __builtin_inff()); }
__device__ __forceinline__ float max3(float a, float b, float c) { return max2(max2(a, b), c); }
__device__ __forceinline__ float dot2(u32 a, u32 b, float c) {
  return __builtin_amdgcn_fdot2_f32_bf16(__builtin_bit_cast(bf16x2, a), __builtin_bit_cast(bf16x2, b), c, false);
}
__device__ __forceinline__ float bflo(u32 p) { return __uint_as_float(p << 16); }
__device__ __forceinline__ float bfhi(u32 p) { return __uint_as_float(p & 0xffff0000u); }
__device__ __forceinline__ float rbf(float x) { return bflo(cvtpk(x, x)); }
template <int CTRL> __device__ __forceinline__ float dpp(float x) {
  return __builtin_bit_cast(float, __builtin_amdgcn_mov_dpp(__builtin_bit_cast(int, x), CTRL, 0xf, 0xf, true));
}
constexpr int XOR1 = 0xB1, XOR2 = 0x4E, ROR8 = 0x128, ROR4 = 0x124, ROR2 = 0x122, ROR1 = 0x121;
__device__ __forceinline__ float swap32_add(float a, float b) {
  auto t = __builtin_amdgcn_permlane32_swap(__float_as_uint(a), __float_as_uint(b), false, false);
  return __uint_as_float(t[0]) + __uint_as_float(t[1]);
}
__device__ __forceinline__ float swap16_add(float a, float b) {
  auto t = __builtin_amdgcn_permlane16_swap(__float_as_uint(a), __float_as_uint(b), false, false);
  return __uint_as_float(t[0]) + __uint_as_float(t[1]);
}
__device__ __forceinline__ float xrow_max(float x) {
  auto s = __builtin_amdgcn_permlane16_swap(__float_as_uint(x), __float_as_uint(x), false, false);
  x = max2(__uint_as_float(s[0]), __uint_as_float(s[1]));
  auto t = __builtin_amdgcn_permlane32_swap(__float_as_uint(x), __float_as_uint(x), false, false);
  return max2(__uint_as_float(t[0]), __uint_as_float(t[1]));
}
__device__ __forceinline__ float xrow_sum(float x) { x = swap16_add(x, x); return swap32_add(x, x); }
__device__ __forceinline__ u64 ldg(const u64* base, u32 i) { return __hip_atomic_load((const gu64*)((const GAS char*)base + 8u * i), __ATOMIC_RELAXED, __HIP_MEMORY_SCOPE_AGENT); }
__device__ __forceinline__ void stg(u64* base, u32 i, u64 v) { __hip_atomic_store((gu64*)((GAS char*)base + 8u * i), v, __ATOMIC_RELAXED, __HIP_MEMORY_SCOPE_AGENT); }
struct Tag { u32 a, b; };
__device__ __forceinline__ u64 gpack(float v0, float v1, Tag t) {
  const u32 lo = ((__float_as_uint(v0) + 0x200u) & 0xfffffc00u) | t.a, hi = ((__float_as_uint(v1) + 0x200u) & 0xfffffc00u) | t.b;
  return ((u64)hi << 32) | lo;
}
__device__ __forceinline__ float gval0(u64 g) { return __uint_as_float((u32)g & 0xfffffc00u); }
__device__ __forceinline__ float gval1(u64 g) { return __uint_as_float((u32)(g >> 32) & 0xfffffc00u); }
__device__ __forceinline__ u32 gbad(u64 g, Tag t) { return (((u32)g ^ t.a) | ((u32)(g >> 32) ^ t.b)) & 0x3ffu; }

template <typename T> __device__ __forceinline__ LAS T* lp(u32 a) { return (LAS T*)(size_t)a; }
__device__ __forceinline__ bf16x8 ld_frag(u32 a) { return __builtin_bit_cast(bf16x8, *lp<u32x4>(a)); }
__device__ __forceinline__ bf16x8 ld_tr(u32 a0, u32 a1) {
  const s16x4 lo = __builtin_amdgcn_ds_read_tr16_b64_v4i16(lp<s16x4>(a0)), hi = __builtin_amdgcn_ds_read_tr16_b64_v4i16(lp<s16x4>(a1));
  return __builtin_bit_cast(bf16x8, __builtin_shufflevector(lo, hi, 0, 1, 2, 3, 4, 5, 6, 7));
}
__device__ __forceinline__ f32x4 mma(bf16x8 a, bf16x8 b, f32x4 c) { return __builtin_amdgcn_mfma_f32_16x16x32_bf16(a, b, c, 0, 0, 0); }
__device__ __forceinline__ void lds_arrive(u32 c) {
  asm volatile("s_waitcnt lgkmcnt(0)\n\ts_mov_b64 exec, 1\n\tds_add_u32 %0, %1\n\ts_mov_b64 exec, -1" :: "v"(c), "v"(1u) : "memory");
}
__device__ __forceinline__ void lds_wait(u32 c, u32 n, u32& seen) {
  u32 v, cnt = LSPIN_MAX;
  asm volatile("s_cmp_ge_u32 %1, %4\n\ts_cbranch_scc1 2f\n"
               "1:\n\tds_read_b32 %0, %3\n\ts_waitcnt lgkmcnt(0)\n\tv_readfirstlane_b32 %1, %0\n\ts_cmp_ge_u32 %1, %4\n\ts_cbranch_scc1 2f\n\t"
               "s_sleep 1\n\ts_sub_u32 %2, %2, 1\n\ts_cbranch_scc0 1b\n2:"
               : "=&v"(v), "+&s"(seen), "+&s"(cnt) : "v"(c), "s"(n) : "memory", "scc");
}
__device__ __forceinline__ void lds_wait(u32 c, u32 n) { u32 seen = 0; lds_wait(c, n, seen); }
__device__ __forceinline__ void lds_wait_hot(u32 c, u32 n, u32& seen) {
  u32 v, cnt = LSPIN_MAX;
  asm volatile("s_cmp_ge_u32 %1, %4\n\ts_cbranch_scc1 2f\n"
               "1:\n\tds_read_b32 %0, %3\n\ts_waitcnt lgkmcnt(0)\n\tv_readfirstlane_b32 %1, %0\n\ts_cmp_ge_u32 %1, %4\n\ts_cbranch_scc1 2f\n\t"
               "s_sub_u32 %2, %2, 1\n\ts_cbranch_scc0 1b\n2:"
               : "=&v"(v), "+&s"(seen), "+&s"(cnt) : "v"(c), "s"(n) : "memory", "scc");
}
#define WG_BARRIER(id) asm volatile("s_waitcnt lgkmcnt(0)\n\ts_barrier ; " id ::: "memory")
#define STR2(x) #x
#define STR(x) STR2(x)
#define DMA_PACE "2:\n\ts_memrealtime s[72:73]\n\ts_waitcnt lgkmcnt(0)\n\ts_lshl_b32 s72, s72, 4\n\ts_sub_u32 s72, s72, %[tn]\n\ts_cmp_lt_i32 s72, 0\n\ts_cbranch_scc1 2b\n\ts_add_u32 %[tn], %[tn], %[slot]\n\t"
#define DMA_THROTTLE0 "s_waitcnt vmcnt(%[win])\n\tds_write_b32 %[vz], %[prog]\n\t"
#define DMA_THROTTLE DMA_THROTTLE0 DMA_PACE
#define DMA_TILE_BODY(THR) \
  "s_mov_b32 m0, %[dst]\n\ts_nop 0\n1:\n\t" THR \
  "global_load_lds_dwordx4 %[v0], %[base] nt\n\ts_add_u32 m0, m0, 0x400\n\tv_add_u32 %[v0], %[step], %[v0]\n\t" \
  "global_load_lds_dwordx4 %[v1], %[base] nt\n\ts_add_u32 m0, m0, 0x400\n\tv_add_u32 %[v1], %[step], %[v1]\n\t" \
  "global_load_lds_dwordx4 %[v0], %[base] nt\n\ts_add_u32 m0, m0, 0x400\n\tv_add_u32 %[v0], %[step], %[v0]\n\t" \
  "global_load_lds_dwordx4 %[v1], %[base] nt\n\ts_add_u32 m0, m0, 0x400\n\tv_add_u32 %[v1], %[step], %[v1]\n\t" \
  "v_add_u32 %[prog], 4, %[prog]\n\ts_sub_u32 %[cnt], %[cnt], 1\n\ts_cbranch_scc0 1b"
template <int W>
__device__ __forceinline__ void dma_tile(u32 dst, const void* base, u32 v0, u32 v1, u32 step, u32 groups, u32& prog, u32& tn, u32 slot) {
  u32 cnt = groups - 1;
  if constexpr (W > 0)
    asm volatile(DMA_TILE_BODY(DMA_THROTTLE) : [v0] "+&v"(v0), [v1] "+&v"(v1), [prog] "+&v"(prog), [cnt] "+&s"(cnt), [tn] "+&s"(tn)
                 : [dst] "s"(dst), [base] "s"(base), [step] "s"(step), [vz] "v"(0u), [win] "n"(W), [slot] "s"(slot) : "memory", "scc", "s72", "s73");
  else
    asm volatile(DMA_TILE_BODY("") : [v0] "+&v"(v0), [v1] "+&v"(v1), [prog] "+&v"(prog), [cnt] "+&s"(cnt)
                 : [dst] "s"(dst), [base] "s"(base), [step] "s"(step) : "memory", "scc");
}
template <int W>
__device__ __forceinline__ void dma_two(u32 dst, const void* base, u32 v0, u32 v1, u32& prog) {
  asm volatile("s_mov_b32 m0, %[dst]\n\t" DMA_THROTTLE0 "global_load_lds_dwordx4 %[v0], %[base] nt\n\ts_add_u32 m0, m0, 0x400\n\tv_add_u32 %[prog], 2, %[prog]\n\t"
               "global_load_lds_dwordx4 %[v1], %[base] nt"
               : [prog] "+&v"(prog) : [dst] "s"(dst), [base] "s"(base), [v0] "v"(v0), [v1] "v"(v1), [vz] "v"(0u), [win] "n"(W) : "memory", "scc");
}
#define DMA_ROWS_BODY(THR) \
  "s_mov_b32 m0, %[dst]\n\ts_nop 0\n1:\n\t" THR \
  "global_load_lds_dwordx4 %[v0], %[base] nt\n\tglobal_load_lds_dwordx4 %[v1], %[base] offset:1024 nt\n\t" \
  "global_load_lds_dwordx4 %[v2], %[base] offset:2048 nt\n\tglobal_load_lds_dwordx4 %[v3], %[base] offset:3072 nt\n\t" \
  "s_add_u32 m0, m0, 0x1000\n\tv_add_u32 %[v0], 0x1000, %[v0]\n\tv_add_u32 %[v1], 0x1000, %[v1]\n\tv_add_u32 %[v2], 0x1000, %[v2]\n\tv_add_u32 %[v3], 0x1000, %[v3]\n\t" \
  "v_add_u32 %[prog], 4, %[prog]\n\ts_sub_u32 %[cnt], %[cnt], 1\n\ts_cbranch_scc0 1b"
template <int W>
__device__ __forceinline__ void dma_rows(u32 dst, const void* base, u32& v0, u32& v1, u32& v2, u32& v3, u32 groups, u32& prog, u32& tn, u32 slot) {
  u32 cnt = groups - 1;
  if constexpr (W > 0)
    asm volatile(DMA_ROWS_BODY(DMA_THROTTLE) : [v0] "+&v"(v0), [v1] "+&v"(v1), [v2] "+&v"(v2), [v3] "+&v"(v3), [prog] "+&v"(prog), [cnt] "+&s"(cnt), [tn] "+&s"(tn)
                 : [dst] "s"(dst), [base] "s"(base), [vz] "v"(0u), [win] "n"(W), [slot] "s"(slot) : "memory", "scc", "s72", "s73");
  else
    asm volatile(DMA_ROWS_BODY("") : [v0] "+&v"(v0), [v1] "+&v"(v1), [v2] "+&v"(v2), [v3] "+&v"(v3), [prog] "+&v"(prog), [cnt] "+&s"(cnt)
                 : [dst] "s"(dst), [base] "s"(base) : "memory", "scc");
}

__device__ __forceinline__ void final_reduce(const u64* ws, u16* out, int c, int qd, int lane, Tag tag) {
  const u64* src = ws + W_Y3 + (c * 4 + qd) * 512;
  u64 gg[8];
  __builtin_amdgcn_s_sleep(4);
#pragma unroll
  for (int i = 0; i < 8; ++i) gg[i] = ldg(src, 64 * i + lane);
#pragma unroll 1
  for (int spin = 0; spin < SPIN_MAX; ++spin) {
    u32 bad = 0;
#pragma unroll
    for (int i = 0; i < 8; ++i) bad |= gbad(gg[i], tag);
    if (!__any(bad != 0)) break;
#pragma unroll
    for (int i = 0; i < 8; ++i) gg[i] = ldg(src, 64 * i + lane);
  }
  const float v0 = ((gval0(gg[0]) + gval0(gg[1])) + (gval0(gg[2]) + gval0(gg[3]))) + ((gval0(gg[4]) + gval0(gg[5])) + (gval0(gg[6]) + gval0(gg[7])));
  const float v1 = ((gval1(gg[0]) + gval1(gg[1])) + (gval1(gg[2]) + gval1(gg[3]))) + ((gval1(gg[4]) + gval1(gg[5])) + (gval1(gg[6]) + gval1(gg[7])));
  float v = swap16_add(v0, v1);
  v = swap32_add(v, v);
  const u32 o2 = cvtpk(v, dpp<XOR1>(v));
  if (lane < 32 && !(lane & 1)) *(u32*)(out + (lane >> 4) * 512 + 64 * c + 16 * qd + (lane & 15)) = o2;
}

__global__ void __launch_bounds__(NTHR, 3)
fused_decode(const u16* __restrict__ x, const u16* __restrict__ Kc, const u16* __restrict__ Vc, const u16* __restrict__ wq,
             const u16* __restrict__ wo, const u16* __restrict__ wg, const u16* __restrict__ wu, const u16* __restrict__ wd,
             const u16* __restrict__ gw, u16* __restrict__ out, u64* __restrict__ ws) {
  __shared__ __attribute__((aligned(1024))) unsigned char lds_all[LDS_BYTES];
  asm volatile("" :: "v"((LAS unsigned char*)lds_all));
  asm volatile("" :: "s"(x), "s"(Kc), "s"(Vc), "s"(wq), "s"(wo), "s"(wg), "s"(wu), "s"(wd), "s"(gw), "s"(out), "s"(ws));
  const int tid = threadIdx.x, lane = tid & 63;
  const int wid = __builtin_amdgcn_readfirstlane(tid >> 6);
  const int wgid = blockIdx.x, xcd = wgid & 7, idx = wgid >> 3;
  const int hh = idx >> 3, s = idx & 7, h = xcd * 4 + hh;
  const int rq = s >> 1, par = s & 1;
  const int b = s & 1, j = s >> 1;
  const u32 r8 = lane >> 3, r4 = lane >> 4;
  const u32 sw0 = 16u * ((lane & 7) ^ (r8 & 2));
  const size_t kvoff = ((size_t)(b * 32 + h) * 4096 + 3072 + j * 256) * 256;
  u32 k0 = r4 * 256 + 16 * ((lane & 15) ^ r4), k1 = k0 ^ 64, k2 = k0 ^ 128, k3 = k0 ^ 192;
  u32 prog = 28 - WIN;
  if (wid == 0) {
    __builtin_amdgcn_s_setprio(2);
    if (lane < 12) *lp<u32>(M_CNT + 4 * lane) = 0;
    asm volatile("s_mov_b64 exec, 1\n\ts_mov_b32 m0, %0\n\ts_nop 0\n\tglobal_load_lds_dwordx4 %1, %2 sc1\n\ts_mov_b64 exec, -1\n\t"
                 "s_mov_b32 m0, %3\n\ts_nop 0\n\tglobal_load_lds_dwordx4 %4, %5\n\tglobal_load_lds_dwordx4 %4, %5 offset:1024\n\t"
                 "s_mov_b32 m0, %6\n\ts_nop 0\n\tglobal_load_lds_dwordx4 %4, %7"
                 :: "s"(M_TAG), "v"(0u), "s"(ws + W_EPOCH), "s"(M_X), "v"(16u * lane), "s"(x), "s"(M_GW), "s"(gw) : "memory");
    u32 dummy = 0, dummy2 = 0;
    dma_tile<0>(LB_W, (const char*)wq + ((size_t)rq << 20) + 256 * h + 128 * par, r8 * 8192 + sw0, r8 * 8192 + (sw0 ^ 64) + 65536, 131072, 4, dummy, dummy2, 0);
    dma_rows<0>(LB_K, (const char*)Kc + kvoff, k0, k1, k2, k3, 2, dummy, dummy2, 0);
  }
  u32 pf0;
  {
    u64 pc;
    asm volatile("s_getpc_b64 %0" : "=s"(pc));
    const u32 off = min((idx * 4 + (lane & 3)) * 128u, (u32)CODE_BYTES - 128u);
    pc &= ~127ull;
    if (tid >= 64 && tid < 68) asm volatile("global_load_dword %0, %1, %2" : "=v"(pf0) : "v"(off), "s"(pc) : "memory");
  }
  WG_BARRIER("B0");
  const int fgr = (int)((0x46573210u >> (4 * xcd)) & 15u) * 4 + hh;
  const bool ffn = fgr < 22;
  const int fg = ffn ? fgr : 0;
  const u32 fo = ffn ? 16u : 0u;
  int role = wid;
  asm volatile("" : "+s"(role));
  if (role == 0) {
    const u32 sw1 = sw0 ^ 64;
    u32 tn = 0;
    const u32 slot = PACE;
    { u64 now; asm volatile("s_memrealtime %0\n\ts_waitcnt lgkmcnt(0)" : "=s"(now)); tn = (u32)now << 4; }
    if (ffn)
      dma_tile<WIN>(LB_F, (const char*)((s & 1) ? wu : wg) + (size_t)(256 * (s >> 2) + ((s >> 1) & 1)) * (2 * FF) + 128 * fg - 64 * ((s >> 1) & 1),
                     r8 * (4 * FF) + sw0, r8 * (4 * FF) + sw1 + 8 * 4 * FF, 16 * 4 * FF, 4, prog, tn, slot);
    dma_rows<WIN>(LB_K + 8192, (const char*)Kc + kvoff, k0, k1, k2, k3, 14, prog, tn, slot);
    {
      u32 v0 = r4 * 256 + 16 * ((lane & 15) ^ (r4 << 1)), v1 = v0 ^ 128, v2 = v0, v3 = v1;
      dma_rows<WIN>(LB_V, (const char*)Vc + kvoff, v0, v1, v2, v3, 12, prog, tn, slot);
      if (ffn) lds_wait(C_FC, 4);
      dma_rows<WIN>(LB_V + 49152, (const char*)Vc + kvoff, v0, v1, v2, v3, 4, prog, tn, slot);
    }
    {
      const char* base = (const char*)(wd + (size_t)43 * h * 512 + 64 * s);
      dma_tile<WIN>(LB_D, base, r8 * 1024 + sw0, r8 * 1024 + sw1 + 8192, 16384, 1, prog, tn, slot);
      const u32 rmax = FF - 1 - 43 * h;
      dma_two<WIN>(LB_D + 4096, base, min(32 + r8, rmax) * 1024 + sw0, min(40 + r8, rmax) * 1024 + sw1, prog);
    }
    lds_wait(C_QC, 4);
    dma_tile<WIN>(LB_W, (const char*)(wo + (size_t)128 * h * 512 + 64 * s), r8 * 1024 + sw0, r8 * 1024 + sw1 + 8192, 16384, 4, prog, tn, slot);
    *(volatile LAS u32*)(size_t)C_ISS = 1;
#define DRAIN(k) "s_waitcnt vmcnt(" #k ")\n\tv_add_u32 %1, " STR(WIN) " - " #k ", %0\n\tds_write_b32 %2, %1\n\t"
    u32 t;
    asm volatile(DRAIN(28) DRAIN(24) DRAIN(22) DRAIN(20) DRAIN(16) DRAIN(12) DRAIN(8) DRAIN(4) DRAIN(0) "s_waitcnt lgkmcnt(0)" : "+v"(prog), "=&v"(t) : "v"(0u) : "memory");
    return;
  }
  const int g = lane >> 4, n16 = lane & 15, q4 = n16 >> 2, p4 = lane & 3;
  u32 landed = 0;
  if (role == 9) {
    const u32x4 z4 = {0u, 0u, 0u, 0u};
    *lp<u32x4>(LB_D + 6144 + 16 * lane) = z4; *lp<u32x4>(LB_D + 7168 + 16 * lane) = z4;
    if (lane < 16) *lp<u32x4>(M_H + 16 * lane) = z4;
  }
  lds_wait(C_LANDED, 4, landed);
  const u64 ep = *lp<u64>(M_TAG) + 1;
  const Tag tag = {(u32)ep & 0x3ffu, (u32)(ep >> 10) & 0x3ffu};

  if (role == 9) {
    {
      const u32x4 gv = *lp<u32x4>(M_GW + 16 * lane), x0 = *lp<u32x4>(M_X + 16 * lane), x1 = *lp<u32x4>(M_X + 1024 + 16 * lane);
      float z0 = 0.f, z1 = 0.f;
#pragma unroll
      for (int t = 0; t < 4; ++t) {
        const float gl = bflo(gv[t]), gh = bfhi(gv[t]);
        z0 = dot2(cvtpk(bflo(x0[t]) * gl, bfhi(x0[t]) * gh), 0x3f803f80u, z0);
        z1 = dot2(cvtpk(bflo(x1[t]) * gl, bfhi(x1[t]) * gh), 0x3f803f80u, z1);
      }
      float z = swap32_add(z0, z1);
      z = swap16_add(z, z);
      z += dpp<ROR8>(z); z += dpp<ROR4>(z); z += dpp<ROR2>(z); z += dpp<ROR1>(z);
      const float gt = rbf(__builtin_amdgcn_rcpf(1.f + __expf(-rbf(z))));
      if ((lane & 31) == 0) { *lp<float>(M_G + (lane >> 5) * 8) = gt; *lp<float>(M_G + (lane >> 5) * 8 + 4) = rbf(1.f - gt); }
    }
    lds_wait(C_LANDED, 28 + fo + 8 * FBLK, landed);
    const u32 fb = (43 * h) & ~15u, fe = fb + lane, fo2 = min(fb + 32 + lane, F1_ROW - 1);
    u64 fp[8];
#pragma unroll
    for (int i = 0; i < 8; ++i) fp[i] = ldg(ws + W_F1 + F1_ROW * i, (i & 2) ? fo2 : fe);
    lds_wait(C_P, 8);
    {
      const f32x4 a = *lp<f32x4>(M_WM), c = *lp<f32x4>(M_WM + 16), la = *lp<f32x4>(M_WL), lc = *lp<f32x4>(M_WL + 16);
      const float m = max3(max3(a[0], a[1], a[2]), max3(a[3], c[0], c[1]), max3(c[2], c[3], c[3]));
      const float L = ((la[0] + la[1]) + (la[2] + la[3])) + ((lc[0] + lc[1]) + (lc[2] + lc[3]));
      stg(ws + W_ST + ((h * 2 + b) * 4 + j) * 16, n16, gpack(m, L, tag));
    }
    {
      u32 bad = 0;
#pragma unroll
      for (int i = 0; i < 8; ++i) bad |= gbad(fp[i], tag);
      if (__builtin_expect(__any(bad != 0), 0)) {
#pragma unroll 1
        for (int spin = 0; spin < SPIN_MAX; ++spin) {
          bad = 0;
#pragma unroll
          for (int i = 0; i < 8; ++i) fp[i] = ldg(ws + W_F1 + F1_ROW * i, (i & 2) ? fo2 : fe);
#pragma unroll
          for (int i = 0; i < 8; ++i) bad |= gbad(fp[i], tag);
          if (!__any(bad != 0)) break;
        }
      }
      const u32 gp = cvtpk((gval0(fp[0]) + gval0(fp[1])) + (gval0(fp[2]) + gval0(fp[3])), (gval1(fp[0]) + gval1(fp[1])) + (gval1(fp[2]) + gval1(fp[3])));
      const u32 up = cvtpk((gval0(fp[4]) + gval0(fp[5])) + (gval0(fp[6]) + gval0(fp[7])), (gval1(fp[4]) + gval1(fp[5])) + (gval1(fp[6]) + gval1(fp[7])));
      const u32 hp = cvtpk(bflo(gp) * bflo(up), bfhi(gp) * bfhi(up));
      const u32 i = fe - 43 * h;
      if (i < 43) { *lp<u16>(M_H + 2 * i) = (u16)hp; *lp<u16>(M_H + 128 + 2 * i) = (u16)(hp >> 16); }
      lds_arrive(C_H);
    }
    lds_wait(C_LANDED, 92 + fo + 8 * STBLK, landed);
    {
      const u64* src = ws + W_ST + h * 128;
      u64 s0, s1;
#pragma unroll 1
      for (int spin = 0; spin < SPIN_MAX; ++spin) {
        s0 = ldg(src, lane); s1 = ldg(src, 64 + lane);
        if (!__any((gbad(s0, tag) | gbad(s1, tag)) != 0)) break;
      }
      const u64 sb = (lane & 8) ? s1 : s0;
      const float mj = gval0(sb);
      const float w = __builtin_amdgcn_exp2f((mj - xrow_max(mj)) * LOG2E);
      const float f = w * __builtin_amdgcn_rcpf(xrow_sum(w * gval1(sb)));
      if ((lane & 7) == 0) *lp<float>(M_F + 2 * (lane & 8) + 4 * g) = f;
      lds_arrive(C_F);
    }
    WG_BARRIER("B1 helper");
    if (wgid == 68) {
      const u32 i0 = (lane >> 3) * 512 + ((lane >> 1) & 3) * 64 + (lane & 1) * 32;
#pragma unroll 1
      for (int spin = 0; spin < SPIN_MAX; ++spin) {
        u32 bad = 0;
#pragma unroll
        for (int i = 0; i < 4; ++i) bad |= gbad(ldg(ws + W_Q1 + 4096 * i, i0), tag);
        if (!__any(bad != 0)) { if (lane == 0) stg(ws + W_EPOCH, 0, ep); break; }
        __builtin_amdgcn_s_sleep(8);
      }
    }
    if (hh == 0 && s < 4) final_reduce(ws, out, xcd, s, lane, tag);
    return;
  }

  const int wave = role - 1, ct = wave & 3;
  const int bm = n16 & 1;
  const u32 tile_a = 128 * (8 * g + q4) + 16 * ((2 * ct + (p4 >> 1)) ^ (((q4 >> 1) | ((g & 1) << 1)) << 1)) + 8 * (p4 & 1);
  if (wave < 4) {
    const u32 xa = M_X + 1024 * bm + 256 * rq + 16 * g;
    bf16x8 xf[4];
#pragma unroll
    for (int ks = 0; ks < 4; ++ks) xf[ks] = ld_frag(xa + 64 * ks);
    lds_wait(C_LANDED, 20, landed);
    f32x4 acc = {0.f, 0.f, 0.f, 0.f};
#pragma unroll
    for (int ks = 0; ks < 4; ++ks) acc = mma(xf[ks], ld_tr(LB_W + tile_a + 4096 * ks, LB_W + tile_a + 4096 * ks + 512), acc);
    {
      const float n0 = dpp<XOR1>(acc[0]), n1 = dpp<XOR1>(acc[1]);
      if (lane < 16) stg(ws + W_Q1 + h * 512 + rq * 64 + 32 * par + 8 * ct, 256 * bm + (n16 >> 1), gpack(bm ? n1 : acc[0], bm ? acc[1] : n0, tag));
    }
    lds_arrive(C_QC);
    __builtin_amdgcn_s_sleep(QSLEEP);
    const u64* src = ws + W_Q1 + h * 512 + b * 256 + 16 * wave;
    u64 g0;
#pragma unroll 1
    for (int spin = 0; spin < SPIN_MAX; ++spin) {
      g0 = ldg(src, 64 * g + n16);
      if (!__any(gbad(g0, tag) != 0)) break;
    }
    const u32 qq = cvtpk(xrow_sum(gval0(g0)), xrow_sum(gval1(g0)));
    if (lane < 16) *lp<u32>(M_Q + 4 * (16 * wave + lane)) = qq;
    lds_arrive(C_Q);
  } else if (ffn) {
    const u32 xa = M_X + 1024 * bm + 512 * (s >> 2) + 32 * g, sel = (s & 2) ? 0x07060302u : 0x05040100u;
    u32x4 xf[4];
#pragma unroll
    for (int ks = 0; ks < 4; ++ks) {
      const u32x4 lo = *lp<u32x4>(xa + 128 * ks), hi = *lp<u32x4>(xa + 128 * ks + 16);
      xf[ks] = u32x4{__builtin_amdgcn_perm(lo[1], lo[0], sel), __builtin_amdgcn_perm(lo[3], lo[2], sel),
                     __builtin_amdgcn_perm(hi[1], hi[0], sel), __builtin_amdgcn_perm(hi[3], hi[2], sel)};
    }
    lds_wait(C_LANDED, 44, landed);
    f32x4 acc = {0.f, 0.f, 0.f, 0.f};
#pragma unroll
    for (int ks = 0; ks < 4; ++ks) acc = mma(__builtin_bit_cast(bf16x8, xf[ks]), ld_tr(LB_F + tile_a + 4096 * ks, LB_F + tile_a + 4096 * ks + 512), acc);
    if (lane < 16) stg(ws + W_F1 + ((s & 1) * 4 + (s & 2) + (s >> 2)) * F1_ROW + 64 * fg + 16 * ct, lane, gpack(acc[0], acc[1], tag));
    lds_arrive(C_FC);
  }
  if (wave == 0) asm volatile("" : "+v"(pf0));
  lds_wait(C_LANDED, 28 + 8 * wave + (wave >= 1 ? fo : 0u), landed);
  lds_wait(C_Q, 4);
  float sc[8];
  {
    const u32 ka = LB_K + 256 * (32 * wave + n16) + 16 * (g ^ n16);
    f32x4 d0 = {0.f, 0.f, 0.f, 0.f}, d1 = {0.f, 0.f, 0.f, 0.f};
#pragma unroll
    for (int ks = 0; ks < 4; ++ks) {
      const bf16x8 qf = ld_frag(M_Q + 64 * ks + 16 * g);
      d0 = mma(ld_frag(ka ^ (64 * ks)), qf, d0);
      d1 = mma(ld_frag((ka ^ (64 * ks)) + 4096), qf, d1);
    }
#pragma unroll
    for (int i = 0; i < 4; i += 2) {
      const u32 a = cvtpk(d0[i], d0[i + 1]), c = cvtpk(d1[i], d1[i + 1]);
      const u32 a2 = cvtpk(bflo(a) * 0.08838834764831845f, bfhi(a) * 0.08838834764831845f);
      const u32 c2 = cvtpk(bflo(c) * 0.08838834764831845f, bfhi(c) * 0.08838834764831845f);
      sc[i] = bflo(a2); sc[i + 1] = bfhi(a2); sc[4 + i] = bflo(c2); sc[5 + i] = bfhi(c2);
    }
  }
  {
    float m = xrow_max(max3(max3(sc[0], sc[1], sc[2]), max3(sc[3], sc[4], sc[5]), max3(sc[6], sc[7], sc[7])));
    if (lane == 0) *lp<float>(M_WM + 4 * wave) = m;
    lds_arrive(C_M);
    lds_wait(C_M, 8);
    const f32x4 a = *lp<f32x4>(M_WM), c = *lp<f32x4>(M_WM + 16);
    m = max3(max3(a[0], a[1], a[2]), max3(a[3], c[0], c[1]), max3(c[2], c[3], c[3]));
    float p[8], l = 0.f;
#pragma unroll
    for (int i = 0; i < 8; ++i) { p[i] = __builtin_amdgcn_exp2f((sc[i] - m) * LOG2E); l += p[i]; }
    l = xrow_sum(l);
    const u32x4 pf = {cvtpk(p[0], p[1]), cvtpk(p[2], p[3]), cvtpk(p[4], p[5]), cvtpk(p[6], p[7])};
    if (n16 == 0) *lp<u32x4>(M_P + 64 * wave + 16 * g) = pf;
    if (lane == 0) *lp<float>(M_WL + 4 * wave) = l;
  }
  lds_arrive(C_P);
  lds_wait(C_P, 8);
  f32x4 oacc = {0.f, 0.f, 0.f, 0.f};
  {
    const u32 va = LB_V + 256 * (4 * g + q4) + 16 * ((2 * wave + (p4 >> 1)) ^ ((4 * (g & 1) + q4) << 1)) + 8 * (p4 & 1);
    bf16x8 pfr[8];
#pragma unroll
    for (int t = 0; t < 8; ++t) pfr[t] = ld_frag(M_P + 64 * t + 16 * g);
    lds_wait(C_LANDED, 92 + fo + 8 * 5, landed);
#pragma unroll
    for (int t = 0; t < 6; ++t) oacc = mma(pfr[t], ld_tr(va + 8192 * t, va + 8192 * t + 4096), oacc);
    lds_wait(C_LANDED, 92 + fo + 8 * 6, landed);
    oacc = mma(pfr[6], ld_tr(va + 8192 * 6, va + 8192 * 6 + 4096), oacc);
    lds_wait_hot(C_LANDED, 92 + fo + 8 * 7, landed);
    oacc = mma(pfr[7], ld_tr(va + 8192 * 7, va + 8192 * 7 + 4096), oacc);
  }
  {
    const float on = dpp<XOR1>(oacc[0]);
    if (lane < 16 && !(lane & 1)) stg(ws + W_O2 + ((h * 2 + b) * 4 + j) * 64 + 8 * wave, lane >> 1, gpack(oacc[0], on, tag));
  }
  const u64* vs = ws + W_O2 + h * 512 + (wave & 1) * 256 + 16 * (wave >> 1);
  const u32 vi = 64 * p4 + (lane >> 2);
  lds_wait(C_ISS, 1);
  u64 gg = ldg(vs, vi);
  bf16x8 wof[4];
  float yb0 = 0.f, yb1 = 0.f;
  f32x4 gates;
  if (wave < 4) {
    lds_wait(C_H, 1);
    gates = *lp<f32x4>(M_G);
    lds_wait(C_LANDED, 154 + fo, landed);
    f32x4 acc = {0.f, 0.f, 0.f, 0.f};
#pragma unroll
    for (int ks = 0; ks < 2; ++ks) acc = mma(ld_frag(M_H + 128 * bm + 64 * ks + 16 * g), ld_tr(LB_D + tile_a + 4096 * ks, LB_D + tile_a + 4096 * ks + 512), acc);
    yb0 = gates[1] * acc[0]; yb1 = gates[3] * acc[1];
    lds_wait_hot(C_LANDED, 170 + fo, landed);
#pragma unroll
    for (int ks = 0; ks < 4; ++ks) wof[ks] = ld_tr(LB_W + tile_a + 4096 * ks, LB_W + tile_a + 4096 * ks + 512);
  }
  lds_wait(C_F, 1);
  const float fq = *lp<float>(M_F + 16 * (wave & 1) + 4 * p4);
  if (__builtin_expect(__any(gbad(gg, tag) != 0), 0)) {
#pragma unroll 1
    for (int spin = 0; spin < SPIN_MAX; ++spin) {
      gg = ldg(vs, vi);
      if (!__any(gbad(gg, tag) != 0)) break;
      __builtin_amdgcn_s_sleep(1);
    }
  }
  {
    float t0 = fq * gval0(gg), t1 = fq * gval1(gg);
    t0 += dpp<XOR1>(t0); t1 += dpp<XOR1>(t1); t0 += dpp<XOR2>(t0); t1 += dpp<XOR2>(t1);
    const u32 ob = cvtpk(t0, t1);
    if (p4 == 0) *lp<u32>(M_O + 256 * (wave & 1) + 64 * (wave >> 1) + lane) = ob;
  }
  WG_BARRIER("B1");
  if (wave < 4) {
    f32x4 acc = {0.f, 0.f, 0.f, 0.f};
#pragma unroll
    for (int ks = 0; ks < 4; ++ks) acc = mma(ld_frag(M_O + 256 * bm + 64 * ks + 16 * g), wof[ks], acc);
    if (lane < 16) stg(ws + W_Y3 + ((s * 4 + ct) * 32 + h) * 16, lane, gpack(fmaf(gates[0], acc[0], yb0), fmaf(gates[2], acc[1], yb1), tag));
  }
}
}

extern "C" void kernel_launch(void* const* d_in, const int* in_sizes, int n_in, void* d_out, int out_size, void* d_ws, size_t ws_size,
                              hipStream_t stream) {
  fused_decode<<<NWG, NTHR, 0, stream>>>((const u16*)d_in[0], (const u16*)d_in[1], (const u16*)d_in[2], (const u16*)d_in[3],
                                         (const u16*)d_in[4], (const u16*)d_in[6], (const u16*)d_in[7], (const u16*)d_in[8],
                                         (const u16*)d_in[9], (u16*)d_out, (u64*)d_ws);
}
```

```cpp
#include <hip/hip_runtime.h>
#include <stdint.h>

typedef unsigned short u16;
typedef unsigned int u32;
typedef unsigned long long u64;
typedef u32 u32x4 __attribute__((ext_vector_type(4)));
typedef short s16x4 __attribute__((ext_vector_type(4)));
typedef short s16x8 __attribute__((ext_vector_type(8)));
typedef float f32x2 __attribute__((ext_vector_type(2)));
typedef float f32x4 __attribute__((ext_vector_type(4)));
typedef __bf16 bf16x2 __attribute__((ext_vector_type(2)));
typedef __bf16 bf16x8 __attribute__((ext_vector_type(8)));
#define GAS __attribute__((address_space(1)))
#define LAS __attribute__((address_space(3)))
typedef GAS u64 gu64;

namespace {
constexpr int NWG = 256, NTHR = 640;
constexpr int FF = 1376;
constexpr u32 W_EPOCH = 0;
constexpr u32 W_Q1 = 1024;
constexpr u32 F1_ROW = 1408;
constexpr u32 W_F1 = W_Q1 + 32 * 512;
constexpr u32 W_O2 = W_F1 + 8 * F1_ROW;
constexpr u32 W_ST = W_O2 + 32 * 512;
constexpr u32 W_Y3 = W_ST + 32 * 128;
constexpr u32 W_END = W_Y3 + 32 * 512;
constexpr u32 LB_M = 0;
constexpr u32 M_CNT = LB_M;
constexpr u32 M_TAG = M_CNT + 48;
constexpr u32 M_G = M_TAG + 16;
constexpr u32 M_WM = M_G + 16;
constexpr u32 M_WL = M_WM + 32;
constexpr u32 M_F = M_WL + 32;
constexpr u32 M_Q = 256;
constexpr u32 M_P = M_Q + 256;
constexpr u32 M_O = M_P + 512;
constexpr u32 M_H = M_O + 512;
constexpr u32 M_X = 2048;
constexpr u32 M_GW = M_X + 2048;
constexpr u32 LB_W = 8192;
constexpr u32 LB_D = LB_W + 16384;
constexpr u32 LB_K = LB_D + 8192;
constexpr u32 LB_V = LB_K + 65536;
constexpr u32 LB_F = LB_V + 49152;
constexpr u32 C_LANDED = M_CNT, C_F = M_CNT + 4, C_QC = M_CNT + 8, C_FC = M_CNT + 12, C_Q = M_CNT + 16, C_M = M_CNT + 20, C_P = M_CNT + 24, C_H = M_CNT + 28,
              C_ISS = M_CNT + 32;
constexpr u32 LDS_BYTES = 163840;
constexpr int SPIN_MAX = 1 << 15, LSPIN_MAX = 1 << 21;
constexpr int CODE_BYTES = 15872;
constexpr float LOG2E = 1.4426950408889634f;
#define WIN 28
constexpr u32 PACE = 220;
constexpr int QSLEEP = 22;
constexpr int FBLK = 5, STBLK = 4;

__device__ __forceinline__ u32 cvtpk(float lo, float hi) {
  const f32x2 v = {lo, hi};
  u32 r = __builtin_bit_cast(u32, __builtin_convertvector(v, bf16x2));
  asm("" : "+v"(r));
  return r;
}
__device__ __forceinline__ float max2(float a, float b) { return __builtin_amdgcn_fmed3f(a, b, __builtin_inff()); }
__device__ __forceinline__ float max3(float a, float b, float c) { return max2(max2(a, b), c); }
__device__ __forceinline__ float dot2(u32 a, u32 b, float c) {
  return __builtin_amdgcn_fdot2_f32_bf16(__builtin_bit_cast(bf16x2, a), __builtin_bit_cast(bf16x2, b), c, false);
}
__device__ __forceinline__ float bflo(u32 p) { return __uint_as_float(p << 16); }
__device__ __forceinline__ float bfhi(u32 p) { return __uint_as_float(p & 0xffff0000u); }
__device__ __forceinline__ float rbf(float x) { return bflo(cvtpk(x, x)); }
template <int CTRL> __device__ __forceinline__ float dpp(float x) {
  return __builtin_bit_cast(float, __builtin_amdgcn_mov_dpp(__builtin_bit_cast(int, x), CTRL, 0xf, 0xf, true));
}
constexpr int XOR1 = 0xB1, XOR2 = 0x4E, ROR8 = 0x128, ROR4 = 0x124, ROR2 = 0x122, ROR1 = 0x121;
__device__ __forceinline__ float swap32_add(float a, float b) {
  auto t = __builtin_amdgcn_permlane32_swap(__float_as_uint(a), __float_as_uint(b), false, false);
  return __uint_as_float(t[0]) + __uint_as_float(t[1]);
}
__device__ __forceinline__ float swap16_add(float a, float b) {
  auto t = __builtin_amdgcn_permlane16_swap(__float_as_uint(a), __float_as_uint(b), false, false);
  return __uint_as_float(t[0]) + __uint_as_float(t[1]);
}
__device__ __forceinline__ float xrow_max(float x) {
  auto s = __builtin_amdgcn_permlane16_swap(__float_as_uint(x), __float_as_uint(x), false, false);
  x = max2(__uint_as_float(s[0]), __uint_as_float(s[1]));
  auto t = __builtin_amdgcn_permlane32_swap(__float_as_uint(x), __float_as_uint(x), false, false);
  return max2(__uint_as_float(t[0]), __uint_as_float(t[1]));
}
__device__ __forceinline__ float xrow_sum(float x) { x = swap16_add(x, x); return swap32_add(x, x); }
__device__ __forceinline__ u64 ldg(const u64* base, u32 i) { return __hip_atomic_load((const gu64*)((const GAS char*)base + 8u * i), __ATOMIC_RELAXED, __HIP_MEMORY_SCOPE_AGENT); }
__device__ __forceinline__ void stg(u64* base, u32 i, u64 v) { __hip_atomic_store((gu64*)((GAS char*)base + 8u * i), v, __ATOMIC_RELAXED, __HIP_MEMORY_SCOPE_AGENT); }
struct Tag { u32 a, b; };
__device__ __forceinline__ u64 gpack(float v0, float v1, Tag t) {
  const u32 lo = ((__float_as_uint(v0) + 0x200u) & 0xfffffc00u) | t.a, hi = ((__float_as_uint(v1) + 0x200u) & 0xfffffc00u) | t.b;
  return ((u64)hi << 32) | lo;
}
__device__ __forceinline__ float gval0(u64 g) { return __uint_as_float((u32)g & 0xfffffc00u); }
__device__ __forceinline__ float gval1(u64 g) { return __uint_as_float((u32)(g >> 32) & 0xfffffc00u); }
__device__ __forceinline__ u32 gbad(u64 g, Tag t) { return (((u32)g ^ t.a) | ((u32)(g >> 32) ^ t.b)) & 0x3ffu; }

template <typename T> __device__ __forceinline__ LAS T* lp(u32 a) { return (LAS T*)(size_t)a; }
__device__ __forceinline__ bf16x8 ld_frag(u32 a) { return __builtin_bit_cast(bf16x8, *lp<u32x4>(a)); }
__device__ __forceinline__ bf16x8 ld_tr(u32 a0, u32 a1) {
  const s16x4 lo = __builtin_amdgcn_ds_read_tr16_b64_v4i16(lp<s16x4>(a0)), hi = __builtin_amdgcn_ds_read_tr16_b64_v4i16(lp<s16x4>(a1));
  return __builtin_bit_cast(bf16x8, __builtin_shufflevector(lo, hi, 0, 1, 2, 3, 4, 5, 6, 7));
}
__device__ __forceinline__ f32x4 mma(bf16x8 a, bf16x8 b, f32x4 c) { return __builtin_amdgcn_mfma_f32_16x16x32_bf16(a, b, c, 0, 0, 0); }
__device__ __forceinline__ void lds_arrive(u32 c) {
  asm volatile("s_waitcnt lgkmcnt(0)\n\ts_mov_b64 exec, 1\n\tds_add_u32 %0, %1\n\ts_mov_b64 exec, -1" :: "v"(c), "v"(1u) : "memory");
}
__device__ __forceinline__ void lds_wait(u32 c, u32 n, u32& seen) {
  u32 v, cnt = LSPIN_MAX;
  asm volatile("s_cmp_ge_u32 %1, %4\n\ts_cbranch_scc1 2f\n"
               "1:\n\tds_read_b32 %0, %3\n\ts_waitcnt lgkmcnt(0)\n\tv_readfirstlane_b32 %1, %0\n\ts_cmp_ge_u32 %1, %4\n\ts_cbranch_scc1 2f\n\t"
               "s_sleep 1\n\ts_sub_u32 %2, %2, 1\n\ts_cbranch_scc0 1b\n2:"
               : "=&v"(v), "+&s"(seen), "+&s"(cnt) : "v"(c), "s"(n) : "memory", "scc");
}
__device__ __forceinline__ void lds_wait(u32 c, u32 n) { u32 seen = 0; lds_wait(c, n, seen); }
__device__ __forceinline__ void lds_wait_hot(u32 c, u32 n, u32& seen) {
  u32 v, cnt = LSPIN_MAX;
  asm volatile("s_cmp_ge_u32 %1, %4\n\ts_cbranch_scc1 2f\n"
               "1:\n\tds_read_b32 %0, %3\n\ts_waitcnt lgkmcnt(0)\n\tv_readfirstlane_b32 %1, %0\n\ts_cmp_ge_u32 %1, %4\n\ts_cbranch_scc1 2f\n\t"
               "s_sub_u32 %2, %2, 1\n\ts_cbranch_scc0 1b\n2:"
               : "=&v"(v), "+&s"(seen), "+&s"(cnt) : "v"(c), "s"(n) : "memory", "scc");
}
#define WG_BARRIER(id) asm volatile("s_waitcnt lgkmcnt(0)\n\ts_barrier ; " id ::: "memory")
#define STR2(x) #x
#define STR(x) STR2(x)
#define DMA_PACE "2:\n\ts_memrealtime s[72:73]\n\ts_waitcnt lgkmcnt(0)\n\ts_lshl_b32 s72, s72, 4\n\ts_sub_u32 s72, s72, %[tn]\n\ts_cmp_lt_i32 s72, 0\n\ts_cbranch_scc1 2b\n\ts_add_u32 %[tn], %[tn], %[slot]\n\t"
#define DMA_THROTTLE0 "s_waitcnt vmcnt(%[win])\n\tds_write_b32 %[vz], %[prog]\n\t"
#define DMA_THROTTLE DMA_THROTTLE0 DMA_PACE
#define DMA_TILE_BODY(THR) \
  "s_mov_b32 m0, %[dst]\n\ts_nop 0\n1:\n\t" THR \
  "global_load_lds_dwordx4 %[v0], %[base] nt\n\ts_add_u32 m0, m0, 0x400\n\tv_add_u32 %[v0], %[step], %[v0]\n\t" \
  "global_load_lds_dwordx4 %[v1], %[base] nt\n\ts_add_u32 m0, m0, 0x400\n\tv_add_u32 %[v1], %[step], %[v1]\n\t" \
  "global_load_lds_dwordx4 %[v0], %[base] nt\n\ts_add_u32 m0, m0, 0x400\n\tv_add_u32 %[v0], %[step], %[v0]\n\t" \
  "global_load_lds_dwordx4 %[v1], %[base] nt\n\ts_add_u32 m0, m0, 0x400\n\tv_add_u32 %[v1], %[step], %[v1]\n\t" \
  "v_add_u32 %[prog], 4, %[prog]\n\ts_sub_u32 %[cnt], %[cnt], 1\n\ts_cbranch_scc0 1b"
template <int W>
__device__ __forceinline__ void dma_tile(u32 dst, const void* base, u32 v0, u32 v1, u32 step, u32 groups, u32& prog, u32& tn, u32 slot) {
  u32 cnt = groups - 1;
  if constexpr (W > 0)
    asm volatile(DMA_TILE_BODY(DMA_THROTTLE) : [v0] "+&v"(v0), [v1] "+&v"(v1), [prog] "+&v"(prog), [cnt] "+&s"(cnt), [tn] "+&s"(tn)
                 : [dst] "s"(dst), [base] "s"(base), [step] "s"(step), [vz] "v"(0u), [win] "n"(W), [slot] "s"(slot) : "memory", "scc", "s72", "s73");
  else
    asm volatile(DMA_TILE_BODY("") : [v0] "+&v"(v0), [v1] "+&v"(v1), [prog] "+&v"(prog), [cnt] "+&s"(cnt)
                 : [dst] "s"(dst), [base] "s"(base), [step] "s"(step) : "memory", "scc");
}
template <int W>
__device__ __forceinline__ void dma_two(u32 dst, const void* base, u32 v0, u32 v1, u32& prog) {
  asm volatile("s_mov_b32 m0, %[dst]\n\t" DMA_THROTTLE0 "global_load_lds_dwordx4 %[v0], %[base] nt\n\ts_add_u32 m0, m0, 0x400\n\tv_add_u32 %[prog], 2, %[prog]\n\t"
               "global_load_lds_dwordx4 %[v1], %[base] nt"
               : [prog] "+&v"(prog) : [dst] "s"(dst), [base] "s"(base), [v0] "v"(v0), [v1] "v"(v1), [vz] "v"(0u), [win] "n"(W) : "memory", "scc");
}
#define DMA_ROWS_BODY(THR) \
  "s_mov_b32 m0, %[dst]\n\ts_nop 0\n1:\n\t" THR \
  "global_load_lds_dwordx4 %[v0], %[base] nt\n\tglobal_load_lds_dwordx4 %[v1], %[base] offset:1024 nt\n\t" \
  "global_load_lds_dwordx4 %[v2], %[base] offset:2048 nt\n\tglobal_load_lds_dwordx4 %[v3], %[base] offset:3072 nt\n\t" \
  "s_add_u32 m0, m0, 0x1000\n\tv_add_u32 %[v0], 0x1000, %[v0]\n\tv_add_u32 %[v1], 0x1000, %[v1]\n\tv_add_u32 %[v2], 0x1000, %[v2]\n\tv_add_u32 %[v3], 0x1000, %[v3]\n\t" \
  "v_add_u32 %[prog], 4, %[prog]\n\ts_sub_u32 %[cnt], %[cnt], 1\n\ts_cbranch_scc0 1b"
template <int W>
__device__ __forceinline__ void dma_rows(u32 dst, const void* base, u32& v0, u32& v1, u32& v2, u32& v3, u32 groups, u32& prog, u32& tn, u32 slot) {
  u32 cnt = groups - 1;
  if constexpr (W > 0)
    asm volatile(DMA_ROWS_BODY(DMA_THROTTLE) : [v0] "+&v"(v0), [v1] "+&v"(v1), [v2] "+&v"(v2), [v3] "+&v"(v3), [prog] "+&v"(prog), [cnt] "+&s"(cnt), [tn] "+&s"(tn)
                 : [dst] "s"(dst), [base] "s"(base), [vz] "v"(0u), [win] "n"(W), [slot] "s"(slot) : "memory", "scc", "s72", "s73");
  else
    asm volatile(DMA_ROWS_BODY("") : [v0] "+&v"(v0), [v1] "+&v"(v1), [v2] "+&v"(v2), [v3] "+&v"(v3), [prog] "+&v"(prog), [cnt] "+&s"(cnt)
                 : [dst] "s"(dst), [base] "s"(base) : "memory", "scc");
}

__device__ __forceinline__ void final_reduce(const u64* ws, u16* out, int c, int qd, int lane, Tag tag) {
  const u64* src = ws + W_Y3 + (c * 4 + qd) * 512;
  u64 gg[8];
  __builtin_amdgcn_s_sleep(4);
#pragma unroll
  for (int i = 0; i < 8; ++i) gg[i] = ldg(src, 64 * i + lane);
#pragma unroll 1
  for (int spin = 0; spin < SPIN_MAX; ++spin) {
    u32 bad = 0;
#pragma unroll
    for (int i = 0; i < 8; ++i) bad |= gbad(gg[i], tag);
    if (!__any(bad != 0)) break;
#pragma unroll
    for (int i = 0; i < 8; ++i) gg[i] = ldg(src, 64 * i + lane);
  }
  const float v0 = ((gval0(gg[0]) + gval0(gg[1])) + (gval0(gg[2]) + gval0(gg[3]))) + ((gval0(gg[4]) + gval0(gg[5])) + (gval0(gg[6]) + gval0(gg[7])));
  const float v1 = ((gval1(gg[0]) + gval1(gg[1])) + (gval1(gg[2]) + gval1(gg[3]))) + ((gval1(gg[4]) + gval1(gg[5])) + (gval1(gg[6]) + gval1(gg[7])));
  float v = swap16_add(v0, v1);
  v = swap32_add(v, v);
  const u32 o2 = cvtpk(v, dpp<XOR1>(v));
  if (lane < 32 && !(lane & 1)) *(u32*)(out + (lane >> 4) * 512 + 64 * c + 16 * qd + (lane & 15)) = o2;
}

__global__ void __launch_bounds__(NTHR, 3)
fused_decode(const u16* __restrict__ x, const u16* __restrict__ Kc, const u16* __restrict__ Vc, const u16* __restrict__ wq,
             const u16* __restrict__ wo, const u16* __restrict__ wg, const u16* __restrict__ wu, const u16* __restrict__ wd,
             const u16* __restrict__ gw, u16* __restrict__ out, u64* __restrict__ ws) {
  __shared__ __attribute__((aligned(1024))) unsigned char lds_all[LDS_BYTES];
  asm volatile("" :: "v"((LAS unsigned char*)lds_all));
  asm volatile("" :: "s"(x), "s"(Kc), "s"(Vc), "s"(wq), "s"(wo), "s"(wg), "s"(wu), "s"(wd), "s"(gw), "s"(out), "s"(ws));
  const int tid = threadIdx.x, lane = tid & 63;
  const int wid = __builtin_amdgcn_readfirstlane(tid >> 6);
  const int wgid = blockIdx.x, xcd = wgid & 7, idx = wgid >> 3;
  const int hh = idx >> 3, s = idx & 7, h = xcd * 4 + hh;
  const int rq = s >> 1, par = s & 1;
  const int b = s & 1, j = s >> 1;
  const u32 r8 = lane >> 3, r4 = lane >> 4;
  const u32 sw0 = 16u * ((lane & 7) ^ (r8 & 2));
  const size_t kvoff = ((size_t)(b * 32 + h) * 4096 + 3072 + j * 256) * 256;
  u32 k0 = r4 * 256 + 16 * ((lane & 15) ^ r4), k1 = k0 ^ 64, k2 = k0 ^ 128, k3 = k0 ^ 192;
  u32 prog = 28 - WIN;
  if (wid == 0) {
    __builtin_amdgcn_s_setprio(2);
    if (lane < 12) *lp<u32>(M_CNT + 4 * lane) = 0;
    asm volatile("s_mov_b64 exec, 1\n\ts_mov_b32 m0, %0\n\ts_nop 0\n\tglobal_load_lds_dwordx4 %1, %2 sc1\n\ts_mov_b64 exec, -1\n\t"
                 "s_mov_b32 m0, %3\n\ts_nop 0\n\tglobal_load_lds_dwordx4 %4, %5\n\tglobal_load_lds_dwordx4 %4, %5 offset:1024\n\t"
                 "s_mov_b32 m0, %6\n\ts_nop 0\n\tglobal_load_lds_dwordx4 %4, %7"
                 :: "s"(M_TAG), "v"(0u), "s"(ws + W_EPOCH), "s"(M_X), "v"(16u * lane), "s"(x), "s"(M_GW), "s"(gw) : "memory");
    u32 dummy = 0, dummy2 = 0;
    dma_tile<0>(LB_W, (const char*)wq + ((size_t)rq << 20) + 256 * h + 128 * par, r8 * 8192 + sw0, r8 * 8192 + (sw0 ^ 64) + 65536, 131072, 4, dummy, dummy2, 0);
    dma_rows<0>(LB_K, (const char*)Kc + kvoff, k0, k1, k2, k3, 2, dummy, dummy2, 0);
  }
  u32 tv = 0;
  if (wid == 9) {
    const char* p = lane == 0 ? (const char*)Vc + kvoff : lane == 1 ? (const char*)(wo + (size_t)128 * h * 512) : lane == 2 ? (const char*)(wd + (size_t)43 * h * 512)
                  : lane == 3 ? (const char*)wg : lane == 4 ? (const char*)wu : (const char*)Vc + kvoff + 65535;
    if (lane < 6) asm volatile("global_load_ubyte %0, %1, off nt" : "=v"(tv) : "v"(p) : "memory");
  }
  u32 pf0;
  {
    u64 pc;
    asm volatile("s_getpc_b64 %0" : "=s"(pc));
    const u32 off = min((idx * 4 + (lane & 3)) * 128u, (u32)CODE_BYTES - 128u);
    pc &= ~127ull;
    if (tid >= 64 && tid < 68) asm volatile("global_load_dword %0, %1, %2" : "=v"(pf0) : "v"(off), "s"(pc) : "memory");
  }
  WG_BARRIER("B0");
  const int fgr = (int)((0x46573210u >> (4 * xcd)) & 15u) * 4 + hh;
  const bool ffn = fgr < 22;
  const int fg = ffn ? fgr : 0;
  const u32 fo = ffn ? 16u : 0u;
  int role = wid;
  asm volatile("" : "+s"(role));
  if (role == 0) {
    const u32 sw1 = sw0 ^ 64;
    u32 tn = 0;
    const u32 slot = PACE;
    { u64 now; asm volatile("s_memrealtime %0\n\ts_waitcnt lgkmcnt(0)" : "=s"(now)); tn = (u32)now << 4; }
    if (ffn)
      dma_tile<WIN>(LB_F, (const char*)((s & 1) ? wu : wg) + (size_t)(256 * (s >> 2) + ((s >> 1) & 1)) * (2 * FF) + 128 * fg - 64 * ((s >> 1) & 1),
                     r8 * (4 * FF) + sw0, r8 * (4 * FF) + sw1 + 8 * 4 * FF, 16 * 4 * FF, 4, prog, tn, slot);
    dma_rows<WIN>(LB_K + 8192, (const char*)Kc + kvoff, k0, k1, k2, k3, 14, prog, tn, slot);
    {
      u32 v0 = r4 * 256 + 16 * ((lane & 15) ^ (r4 << 1)), v1 = v0 ^ 128, v2 = v0, v3 = v1;
      dma_rows<WIN>(LB_V, (const char*)Vc + kvoff, v0, v1, v2, v3, 12, prog, tn, slot);
      if (ffn) lds_wait(C_FC, 4);
      dma_rows<WIN>(LB_V + 49152, (const char*)Vc + kvoff, v0, v1, v2, v3, 4, prog, tn, slot);
    }
    {
      const char* base = (const char*)(wd + (size_t)43 * h * 512 + 64 * s);
      dma_tile<WIN>(LB_D, base, r8 * 1024 + sw0, r8 * 1024 + sw1 + 8192, 16384, 1, prog, tn, slot);
      const u32 rmax = FF - 1 - 43 * h;
      dma_two<WIN>(LB_D + 4096, base, min(32 + r8, rmax) * 1024 + sw0, min(40 + r8, rmax) * 1024 + sw1, prog);
    }
    lds_wait(C_QC, 4);
    dma_tile<WIN>(LB_W, (const char*)(wo + (size_t)128 * h * 512 + 64 * s), r8 * 1024 + sw0, r8 * 1024 + sw1 + 8192, 16384, 4, prog, tn, slot);
    *(volatile LAS u32*)(size_t)C_ISS = 1;
#define DRAIN(k) "s_waitcnt vmcnt(" #k ")\n\tv_add_u32 %1, " STR(WIN) " - " #k ", %0\n\tds_write_b32 %2, %1\n\t"
    u32 t;
    asm volatile(DRAIN(28) DRAIN(24) DRAIN(22) DRAIN(20) DRAIN(16) DRAIN(12) DRAIN(8) DRAIN(4) DRAIN(0) "s_waitcnt lgkmcnt(0)" : "+v"(prog), "=&v"(t) : "v"(0u) : "memory");
    return;
  }
  const int g = lane >> 4, n16 = lane & 15, q4 = n16 >> 2, p4 = lane & 3;
  u32 landed = 0;
  if (role == 9) {
    const u32x4 z4 = {0u, 0u, 0u, 0u};
    *lp<u32x4>(LB_D + 6144 + 16 * lane) = z4; *lp<u32x4>(LB_D + 7168 + 16 * lane) = z4;
    if (lane < 16) *lp<u32x4>(M_H + 16 * lane) = z4;
  }
  lds_wait(C_LANDED, 4, landed);
  const u64 ep = *lp<u64>(M_TAG) + 1;
  const Tag tag = {(u32)ep & 0x3ffu, (u32)(ep >> 10) & 0x3ffu};

  if (role == 9) {
    asm volatile("s_waitcnt vmcnt(0)" : "+v"(tv) :: "memory");
    {
      const u32x4 gv = *lp<u32x4>(M_GW + 16 * lane), x0 = *lp<u32x4>(M_X + 16 * lane), x1 = *lp<u32x4>(M_X + 1024 + 16 * lane);
      float z0 = 0.f, z1 = 0.f;
#pragma unroll
      for (int t = 0; t < 4; ++t) {
        const float gl = bflo(gv[t]), gh = bfhi(gv[t]);
        z0 = dot2(cvtpk(bflo(x0[t]) * gl, bfhi(x0[t]) * gh), 0x3f803f80u, z0);
        z1 = dot2(cvtpk(bflo(x1[t]) * gl, bfhi(x1[t]) * gh), 0x3f803f80u, z1);
      }
      float z = swap32_add(z0, z1);
      z = swap16_add(z, z);
      z += dpp<ROR8>(z); z += dpp<ROR4>(z); z += dpp<ROR2>(z); z += dpp<ROR1>(z);
      const float gt = rbf(__builtin_amdgcn_rcpf(1.f + __expf(-rbf(z))));
      if ((lane & 31) == 0) { *lp<float>(M_G + (lane >> 5) * 8) = gt; *lp<float>(M_G + (lane >> 5) * 8 + 4) = rbf(1.f - gt); }
    }
    lds_wait(C_LANDED, 28 + fo + 8 * FBLK, landed);
    const u32 fb = (43 * h) & ~15u, fe = fb + lane, fo2 = min(fb + 32 + lane, F1_ROW - 1);
    u64 fp[8];
#pragma unroll
    for (int i = 0; i < 8; ++i) fp[i] = ldg(ws + W_F1 + F1_ROW * i, (i & 2) ? fo2 : fe);
    lds_wait(C_P, 8);
    {
      const f32x4 a = *lp<f32x4>(M_WM), c = *lp<f32x4>(M_WM + 16), la = *lp<f32x4>(M_WL), lc = *lp<f32x4>(M_WL + 16);
      const float m = max3(max3(a[0], a[1], a[2]), max3(a[3], c[0], c[1]), max3(c[2], c[3], c[3]));
      const float L = ((la[0] + la[1]) + (la[2] + la[3])) + ((lc[0] + lc[1]) + (lc[2] + lc[3]));
      stg(ws + W_ST + ((h * 2 + b) * 4 + j) * 16, n16, gpack(m, L, tag));
    }
    {
      u32 bad = 0;
#pragma unroll
      for (int i = 0; i < 8; ++i) bad |= gbad(fp[i], tag);
      if (__builtin_expect(__any(bad != 0), 0)) {
#pragma unroll 1
        for (int spin = 0; spin < SPIN_MAX; ++spin) {
          bad = 0;
#pragma unroll
          for (int i = 0; i < 8; ++i) fp[i] = ldg(ws + W_F1 + F1_ROW * i, (i & 2) ? fo2 : fe);
#pragma unroll
          for (int i = 0; i < 8; ++i) bad |= gbad(fp[i], tag);
          if (!__any(bad != 0)) break;
        }
      }
      const u32 gp = cvtpk((gval0(fp[0]) + gval0(fp[1])) + (gval0(fp[2]) + gval0(fp[3])), (gval1(fp[0]) + gval1(fp[1])) + (gval1(fp[2]) + gval1(fp[3])));
      const u32 up = cvtpk((gval0(fp[4]) + gval0(fp[5])) + (gval0(fp[6]) + gval0(fp[7])), (gval1(fp[4]) + gval1(fp[5])) + (gval1(fp[6]) + gval1(fp[7])));
      const u32 hp = cvtpk(bflo(gp) * bflo(up), bfhi(gp) * bfhi(up));
      const u32 i = fe - 43 * h;
      if (i < 43) { *lp<u16>(M_H + 2 * i) = (u16)hp; *lp<u16>(M_H + 128 + 2 * i) = (u16)(hp >> 16); }
      lds_arrive(C_H);
    }
    lds_wait(C_LANDED, 92 + fo + 8 * STBLK, landed);
    {
      const u64* src = ws + W_ST + h * 128;
      u64 s0, s1;
#pragma unroll 1
      for (int spin = 0; spin < SPIN_MAX; ++spin) {
        s0 = ldg(src, lane); s1 = ldg(src, 64 + lane);
        if (!__any((gbad(s0, tag) | gbad(s1, tag)) != 0)) break;
      }
      const u64 sb = (lane & 8) ? s1 : s0;
      const float mj = gval0(sb);
      const float w = __builtin_amdgcn_exp2f((mj - xrow_max(mj)) * LOG2E);
      const float f = w * __builtin_amdgcn_rcpf(xrow_sum(w * gval1(sb)));
      if ((lane & 7) == 0) *lp<float>(M_F + 2 * (lane & 8) + 4 * g) = f;
      lds_arrive(C_F);
    }
    WG_BARRIER("B1 helper");
    if (wgid == 68) {
      const u32 i0 = (lane >> 3) * 512 + ((lane >> 1) & 3) * 64 + (lane & 1) * 32;
#pragma unroll 1
      for (int spin = 0; spin < SPIN_MAX; ++spin) {
        u32 bad = 0;
#pragma unroll
        for (int i = 0; i < 4; ++i) bad |= gbad(ldg(ws + W_Q1 + 4096 * i, i0), tag);
        if (!__any(bad != 0)) { if (lane == 0) stg(ws + W_EPOCH, 0, ep); break; }
        __builtin_amdgcn_s_sleep(8);
      }
    }
    if (hh == 0 && s < 4) final_reduce(ws, out, xcd, s, lane, tag);
    else if (lane == 0) {
      u32 tv2;
      asm volatile("global_load_ubyte %0, %1, off\n\ts_waitcnt vmcnt(0)" : "=v"(tv2) : "v"(out + 64 * xcd) : "memory");
    }
    return;
  }

  const int wave = role - 1, ct = wave & 3;
  const int bm = n16 & 1;
  const u32 tile_a = 128 * (8 * g + q4) + 16 * ((2 * ct + (p4 >> 1)) ^ (((q4 >> 1) | ((g & 1) << 1)) << 1)) + 8 * (p4 & 1);
  if (wave < 4) {
    const u32 xa = M_X + 1024 * bm + 256 * rq + 16 * g;
    bf16x8 xf[4];
#pragma unroll
    for (int ks = 0; ks < 4; ++ks) xf[ks] = ld_frag(xa + 64 * ks);
    lds_wait(C_LANDED, 20, landed);
    f32x4 acc = {0.f, 0.f, 0.f, 0.f};
#pragma unroll
    for (int ks = 0; ks < 4; ++ks) acc = mma(xf[ks], ld_tr(LB_W + tile_a + 4096 * ks, LB_W + tile_a + 4096 * ks + 512), acc);
    {
      const float n0 = dpp<XOR1>(acc[0]), n1 = dpp<XOR1>(acc[1]);
      if (lane < 16) stg(ws + W_Q1 + h * 512 + rq * 64 + 32 * par + 8 * ct, 256 * bm + (n16 >> 1), gpack(bm ? n1 : acc[0], bm ? acc[1] : n0, tag));
    }
    lds_arrive(C_QC);
    __builtin_amdgcn_s_sleep(QSLEEP);
    const u64* src = ws + W_Q1 + h * 512 + b * 256 + 16 * wave;
    u64 g0;
#pragma unroll 1
    for (int spin = 0; spin < SPIN_MAX; ++spin) {
      g0 = ldg(src, 64 * g + n16);
      if (!__any(gbad(g0, tag) != 0)) break;
    }
    const u32 qq = cvtpk(xrow_sum(gval0(g0)), xrow_sum(gval1(g0)));
    if (lane < 16) *lp<u32>(M_Q + 4 * (16 * wave + lane)) = qq;
    lds_arrive(C_Q);
  } else if (ffn) {
    const u32 xa = M_X + 1024 * bm + 512 * (s >> 2) + 32 * g, sel = (s & 2) ? 0x07060302u : 0x05040100u;
    u32x4 xf[4];
#pragma unroll
    for (int ks = 0; ks < 4; ++ks) {
      const u32x4 lo = *lp<u32x4>(xa + 128 * ks), hi = *lp<u32x4>(xa + 128 * ks + 16);
      xf[ks] = u32x4{__builtin_amdgcn_perm(lo[1], lo[0], sel), __builtin_amdgcn_perm(lo[3], lo[2], sel),
                     __builtin_amdgcn_perm(hi[1], hi[0], sel), __builtin_amdgcn_perm(hi[3], hi[2], sel)};
    }
    lds_wait(C_LANDED, 44, landed);
    f32x4 acc = {0.f, 0.f, 0.f, 0.f};
#pragma unroll
    for (int ks = 0; ks < 4; ++ks) acc = mma(__builtin_bit_cast(bf16x8, xf[ks]), ld_tr(LB_F + tile_a + 4096 * ks, LB_F + tile_a + 4096 * ks + 512), acc);
    if (lane < 16) stg(ws + W_F1 + ((s & 1) * 4 + (s & 2) + (s >> 2)) * F1_ROW + 64 * fg + 16 * ct, lane, gpack(acc[0], acc[1], tag));
    lds_arrive(C_FC);
  }
  if (wave == 0) asm volatile("" : "+v"(pf0));
  lds_wait(C_LANDED, 28 + 8 * wave + (wave >= 1 ? fo : 0u), landed);
  lds_wait(C_Q, 4);
  float sc[8];
  {
    const u32 ka = LB_K + 256 * (32 * wave + n16) + 16 * (g ^ n16);
    f32x4 d0 = {0.f, 0.f, 0.f, 0.f}, d1 = {0.f, 0.f, 0.f, 0.f};
#pragma unroll
    for (int ks = 0; ks < 4; ++ks) {
      const bf16x8 qf = ld_frag(M_Q + 64 * ks + 16 * g);
      d0 = mma(ld_frag(ka ^ (64 * ks)), qf, d0);
      d1 = mma(ld_frag((ka ^ (64 * ks)) + 4096), qf, d1);
    }
#pragma unroll
    for (int i = 0; i < 4; i += 2) {
      const u32 a = cvtpk(d0[i], d0[i + 1]), c = cvtpk(d1[i], d1[i + 1]);
      const u32 a2 = cvtpk(bflo(a) * 0.08838834764831845f, bfhi(a) * 0.08838834764831845f);
      const u32 c2 = cvtpk(bflo(c) * 0.08838834764831845f, bfhi(c) * 0.08838834764831845f);
      sc[i] = bflo(a2); sc[i + 1] = bfhi(a2); sc[4 + i] = bflo(c2); sc[5 + i] = bfhi(c2);
    }
  }
  {
    float m = xrow_max(max3(max3(sc[0], sc[1], sc[2]), max3(sc[3], sc[4], sc[5]), max3(sc[6], sc[7], sc[7])));
    if (lane == 0) *lp<float>(M_WM + 4 * wave) = m;
    lds_arrive(C_M);
    lds_wait(C_M, 8);
    const f32x4 a = *lp<f32x4>(M_WM), c = *lp<f32x4>(M_WM + 16);
    m = max3(max3(a[0], a[1], a[2]), max3(a[3], c[0], c[1]), max3(c[2], c[3], c[3]));
    float p[8], l = 0.f;
#pragma unroll
    for (int i = 0; i < 8; ++i) { p[i] = __builtin_amdgcn_exp2f((sc[i] - m) * LOG2E); l += p[i]; }
    l = xrow_sum(l);
    const u32x4 pf = {cvtpk(p[0], p[1]), cvtpk(p[2], p[3]), cvtpk(p[4], p[5]), cvtpk(p[6], p[7])};
    if (n16 == 0) *lp<u32x4>(M_P + 64 * wave + 16 * g) = pf;
    if (lane == 0) *lp<float>(M_WL + 4 * wave) = l;
  }
  lds_arrive(C_P);
  lds_wait(C_P, 8);
  f32x4 oacc = {0.f, 0.f, 0.f, 0.f};
  {
    const u32 va = LB_V + 256 * (4 * g + q4) + 16 * ((2 * wave + (p4 >> 1)) ^ ((4 * (g & 1) + q4) << 1)) + 8 * (p4 & 1);
    bf16x8 pfr[8];
#pragma unroll
    for (int t = 0; t < 8; ++t) pfr[t] = ld_frag(M_P + 64 * t + 16 * g);
    lds_wait(C_LANDED, 92 + fo + 8 * 5, landed);
#pragma unroll
    for (int t = 0; t < 6; ++t) oacc = mma(pfr[t], ld_tr(va + 8192 * t, va + 8192 * t + 4096), oacc);
    lds_wait(C_LANDED, 92 + fo + 8 * 6, landed);
    oacc = mma(pfr[6], ld_tr(va + 8192 * 6, va + 8192 * 6 + 4096), oacc);
    lds_wait_hot(C_LANDED, 92 + fo + 8 * 7, landed);
    oacc = mma(pfr[7], ld_tr(va + 8192 * 7, va + 8192 * 7 + 4096), oacc);
  }
  {
    const float on = dpp<XOR1>(oacc[0]);
    if (lane < 16 && !(lane & 1)) stg(ws + W_O2 + ((h * 2 + b) * 4 + j) * 64 + 8 * wave, lane >> 1, gpack(oacc[0], on, tag));
  }
  const u64* vs = ws + W_O2 + h * 512 + (wave & 1) * 256 + 16 * (wave >> 1);
  const u32 vi = 64 * p4 + (lane >> 2);
  lds_wait(C_ISS, 1);
  u64 gg = ldg(vs, vi);
  bf16x8 wof[4];
  float yb0 = 0.f, yb1 = 0.f;
  f32x4 gates;
  if (wave < 4) {
    lds_wait(C_H, 1);
    gates = *lp<f32x4>(M_G);
    lds_wait(C_LANDED, 154 + fo, landed);
    f32x4 acc = {0.f, 0.f, 0.f, 0.f};
#pragma unroll
    for (int ks = 0; ks < 2; ++ks) acc = mma(ld_frag(M_H + 128 * bm + 64 * ks + 16 * g), ld_tr(LB_D + tile_a + 4096 * ks, LB_D + tile_a + 4096 * ks + 512), acc);
    yb0 = gates[1] * acc[0]; yb1 = gates[3] * acc[1];
    lds_wait_hot(C_LANDED, 170 + fo, landed);
#pragma unroll
    for (int ks = 0; ks < 4; ++ks) wof[ks] = ld_tr(LB_W + tile_a + 4096 * ks, LB_W + tile_a + 4096 * ks + 512);
  }
  lds_wait(C_F, 1);
  const float fq = *lp<float>(M_F + 16 * (wave & 1) + 4 * p4);
  if (__builtin_expect(__any(gbad(gg, tag) != 0), 0)) {
#pragma unroll 1
    for (int spin = 0; spin < SPIN_MAX; ++spin) {
      gg = ldg(vs, vi);
      if (!__any(gbad(gg, tag) != 0)) break;
      __builtin_amdgcn_s_sleep(1);
    }
  }
  {
    float t0 = fq * gval0(gg), t1 = fq * gval1(gg);
    t0 += dpp<XOR1>(t0); t1 += dpp<XOR1>(t1); t0 += dpp<XOR2>(t0); t1 += dpp<XOR2>(t1);
    const u32 ob = cvtpk(t0, t1);
    if (p4 == 0) *lp<u32>(M_O + 256 * (wave & 1) + 64 * (wave >> 1) + lane) = ob;
  }
  WG_BARRIER("B1");
  if (wave < 4) {
    f32x4 acc = {0.f, 0.f, 0.f, 0.f};
#pragma unroll
    for (int ks = 0; ks < 4; ++ks) acc = mma(ld_frag(M_O + 256 * bm + 64 * ks + 16 * g), wof[ks], acc);
    if (lane < 16) stg(ws + W_Y3 + ((s * 4 + ct) * 32 + h) * 16, lane, gpack(fmaf(gates[0], acc[0], yb0), fmaf(gates[2], acc[1], yb1), tag));
  }
}
}

extern "C" void kernel_launch(void* const* d_in, const int* in_sizes, int n_in, void* d_out, int out_size, void* d_ws, size_t ws_size,
                              hipStream_t stream) {
  fused_decode<<<NWG, NTHR, 0, stream>>>((const u16*)d_in[0], (const u16*)d_in[1], (const u16*)d_in[2], (const u16*)d_in[3],
                                         (const u16*)d_in[4], (const u16*)d_in[6], (const u16*)d_in[7], (const u16*)d_in[8],
                                         (const u16*)d_in[9], (u16*)d_out, (u64*)d_ws);
}
```

```cpp
#include <hip/hip_runtime.h>
#include <stdint.h>

typedef unsigned short u16;
typedef unsigned int u32;
typedef unsigned long long u64;
typedef u32 u32x4 __attribute__((ext_vector_type(4)));
typedef short s16x4 __attribute__((ext_vector_type(4)));
typedef short s16x8 __attribute__((ext_vector_type(8)));
typedef float f32x2 __attribute__((ext_vector_type(2)));
typedef float f32x4 __attribute__((ext_vector_type(4)));
typedef __bf16 bf16x2 __attribute__((ext_vector_type(2)));
typedef __bf16 bf16x8 __attribute__((ext_vector_type(8)));
#define GAS __attribute__((address_space(1)))
#define LAS __attribute__((address_space(3)))
typedef GAS u64 gu64;

namespace {
constexpr int NWG = 256, NTHR = 640;
constexpr int FF = 1376;
constexpr u32 W_EPOCH = 0;
constexpr u32 W_Q1 = 1024;
constexpr u32 F1_ROW = 1408;
constexpr u32 W_F1 = W_Q1 + 32 * 512;
constexpr u32 W_O2 = W_F1 + 8 * F1_ROW;
constexpr u32 W_ST = W_O2 + 32 * 512;
constexpr u32 W_Y3 = W_ST + 32 * 128;
constexpr u32 W_END = W_Y3 + 32 * 512;
constexpr u32 LB_M = 0;
constexpr u32 M_CNT = LB_M;
constexpr u32 M_TAG = M_CNT + 48;
constexpr u32 M_G = M_TAG + 16;
constexpr u32 M_WM = M_G + 16;
constexpr u32 M_WL = M_WM + 32;
constexpr u32 M_F = M_WL + 32;
constexpr u32 M_Q = 256;
constexpr u32 M_P = M_Q + 256;
constexpr u32 M_O = M_P + 512;
constexpr u32 M_H = M_O + 512;
constexpr u32 M_X = 2048;
constexpr u32 M_GW = M_X + 2048;
constexpr u32 LB_W = 8192;
constexpr u32 LB_D = LB_W + 16384;
constexpr u32 LB_K = LB_D + 8192;
constexpr u32 LB_V = LB_K + 65536;
constexpr u32 LB_F = LB_V + 49152;
constexpr u32 C_LANDED = M_CNT, C_F = M_CNT + 4, C_QC = M_CNT + 8, C_FC = M_CNT + 12, C_Q = M_CNT + 16, C_M = M_CNT + 20, C_P = M_CNT + 24, C_H = M_CNT + 28,
              C_ISS = M_CNT + 32;
constexpr u32 LDS_BYTES = 163840;
constexpr int SPIN_MAX = 1 << 15, LSPIN_MAX = 1 << 21;
constexpr int CODE_BYTES = 15872;
constexpr float LOG2E = 1.4426950408889634f;
#define WIN 28
constexpr u32 PACE = 220;
constexpr int QSLEEP = 22;
constexpr int FBLK = 5, STBLK = 4;

__device__ __forceinline__ u32 cvtpk(float lo, float hi) {
  const f32x2 v = {lo, hi};
  u32 r = __builtin_bit_cast(u32, __builtin_convertvector(v, bf16x2));
  asm("" : "+v"(r));
  return r;
}
__device__ __forceinline__ float max2(float a, float b) { return __builtin_amdgcn_fmed3f(a, b, __builtin_inff()); }
__device__ __forceinline__ float max3(float a, float b, float c) { return max2(max2(a, b), c); }
__device__ __forceinline__ float dot2(u32 a, u32 b, float c) {
  return __builtin_amdgcn_fdot2_f32_bf16(__builtin_bit_cast(bf16x2, a), __builtin_bit_cast(bf16x2, b), c, false);
}
__device__ __forceinline__ float bflo(u32 p) { return __uint_as_float(p << 16); }
__device__ __forceinline__ float bfhi(u32 p) { return __uint_as_float(p & 0xffff0000u); }
__device__ __forceinline__ float rbf(float x) { return bflo(cvtpk(x, x)); }
template <int CTRL> __device__ __forceinline__ float dpp(float x) {
  return __builtin_bit_cast(float, __builtin_amdgcn_mov_dpp(__builtin_bit_cast(int, x), CTRL, 0xf, 0xf, true));
}
constexpr int XOR1 = 0xB1, XOR2 = 0x4E, ROR8 = 0x128, ROR4 = 0x124, ROR2 = 0x122, ROR1 = 0x121;
__device__ __forceinline__ float swap32_add(float a, float b) {
  auto t = __builtin_amdgcn_permlane32_swap(__float_as_uint(a), __float_as_uint(b), false, false);
  return __uint_as_float(t[0]) + __uint_as_float(t[1]);
}
__device__ __forceinline__ float swap16_add(float a, float b) {
  auto t = __builtin_amdgcn_permlane16_swap(__float_as_uint(a), __float_as_uint(b), false, false);
  return __uint_as_float(t[0]) + __uint_as_float(t[1]);
}
__device__ __forceinline__ float xrow_max(float x) {
  auto s = __builtin_amdgcn_permlane16_swap(__float_as_uint(x), __float_as_uint(x), false, false);
  x = max2(__uint_as_float(s[0]), __uint_as_float(s[1]));
  auto t = __builtin_amdgcn_permlane32_swap(__float_as_uint(x), __float_as_uint(x), false, false);
  return max2(__uint_as_float(t[0]), __uint_as_float(t[1]));
}
__device__ __forceinline__ float xrow_sum(float x) { x = swap16_add(x, x); return swap32_add(x, x); }
__device__ __forceinline__ u64 ldg(const u64* base, u32 i) { return __hip_atomic_load((const gu64*)((const GAS char*)base + 8u * i), __ATOMIC_RELAXED, __HIP_MEMORY_SCOPE_AGENT); }
__device__ __forceinline__ void stg(u64* base, u32 i, u64 v) { __hip_atomic_store((gu64*)((GAS char*)base + 8u * i), v, __ATOMIC_RELAXED, __HIP_MEMORY_SCOPE_AGENT); }
struct Tag { u32 a, b; };
__device__ __forceinline__ u64 gpack(float v0, float v1, Tag t) {
  const u32 lo = ((__float_as_uint(v0) + 0x200u) & 0xfffffc00u) | t.a, hi = ((__float_as_uint(v1) + 0x200u) & 0xfffffc00u) | t.b;
  return ((u64)hi << 32) | lo;
}
__device__ __forceinline__ float gval0(u64 g) { return __uint_as_float((u32)g & 0xfffffc00u); }
__device__ __forceinline__ float gval1(u64 g) { return __uint_as_float((u32)(g >> 32) & 0xfffffc00u); }
__device__ __forceinline__ u32 gbad(u64 g, Tag t) { return (((u32)g ^ t.a) | ((u32)(g >> 32) ^ t.b)) & 0x3ffu; }

template <typename T> __device__ __forceinline__ LAS T* lp(u32 a) { return (LAS T*)(size_t)a; }
__device__ __forceinline__ bf16x8 ld_frag(u32 a) { return __builtin_bit_cast(bf16x8, *lp<u32x4>(a)); }
__device__ __forceinline__ bf16x8 ld_tr(u32 a0, u32 a1) {
  const s16x4 lo = __builtin_amdgcn_ds_read_tr16_b64_v4i16(lp<s16x4>(a0)), hi = __builtin_amdgcn_ds_read_tr16_b64_v4i16(lp<s16x4>(a1));
  return __builtin_bit_cast(bf16x8, __builtin_shufflevector(lo, hi, 0, 1, 2, 3, 4, 5, 6, 7));
}
__device__ __forceinline__ f32x4 mma(bf16x8 a, bf16x8 b, f32x4 c) { return __builtin_amdgcn_mfma_f32_16x16x32_bf16(a, b, c, 0, 0, 0); }
__device__ __forceinline__ void lds_arrive(u32 c) {
  asm volatile("s_waitcnt lgkmcnt(0)\n\ts_mov_b64 exec, 1\n\tds_add_u32 %0, %1\n\ts_mov_b64 exec, -1" :: "v"(c), "v"(1u) : "memory");
}
__device__ __forceinline__ void lds_wait(u32 c, u32 n, u32& seen) {
  u32 v, cnt = LSPIN_MAX;
  asm volatile("s_cmp_ge_u32 %1, %4\n\ts_cbranch_scc1 2f\n"
               "1:\n\tds_read_b32 %0, %3\n\ts_waitcnt lgkmcnt(0)\n\tv_readfirstlane_b32 %1, %0\n\ts_cmp_ge_u32 %1, %4\n\ts_cbranch_scc1 2f\n\t"
               "s_sleep 1\n\ts_sub_u32 %2, %2, 1\n\ts_cbranch_scc0 1b\n2:"
               : "=&v"(v), "+&s"(seen), "+&s"(cnt) : "v"(c), "s"(n) : "memory", "scc");
}
__device__ __forceinline__ void lds_wait(u32 c, u32 n) { u32 seen = 0; lds_wait(c, n, seen); }
__device__ __forceinline__ void lds_wait_hot(u32 c, u32 n, u32& seen) {
  u32 v, cnt = LSPIN_MAX;
  asm volatile("s_cmp_ge_u32 %1, %4\n\ts_cbranch_scc1 2f\n"
               "1:\n\tds_read_b32 %0, %3\n\ts_waitcnt lgkmcnt(0)\n\tv_readfirstlane_b32 %1, %0\n\ts_cmp_ge_u32 %1, %4\n\ts_cbranch_scc1 2f\n\t"
               "s_sub_u32 %2, %2, 1\n\ts_cbranch_scc0 1b\n2:"
               : "=&v"(v), "+&s"(seen), "+&s"(cnt) : "v"(c), "s"(n) : "memory", "scc");
}
#define WG_BARRIER(id) asm volatile("s_waitcnt lgkmcnt(0)\n\ts_barrier ; " id ::: "memory")
#define STR2(x) #x
#define STR(x) STR2(x)
#define DMA_PACE "2:\n\ts_memrealtime s[72:73]\n\ts_waitcnt lgkmcnt(0)\n\ts_lshl_b32 s72, s72, 4\n\ts_sub_u32 s72, s72, %[tn]\n\ts_cmp_lt_i32 s72, 0\n\ts_cbranch_scc1 2b\n\ts_add_u32 %[tn], %[tn], %[slot]\n\t"
#define DMA_THROTTLE0 "s_waitcnt vmcnt(%[win])\n\tds_write_b32 %[vz], %[prog]\n\t"
#define DMA_THROTTLE DMA_THROTTLE0 DMA_PACE
#define DMA_TILE_BODY(THR) \
  "s_mov_b32 m0, %[dst]\n\ts_nop 0\n1:\n\t" THR \
  "global_load_lds_dwordx4 %[v0], %[base] nt\n\ts_add_u32 m0, m0, 0x400\n\tv_add_u32 %[v0], %[step], %[v0]\n\t" \
  "global_load_lds_dwordx4 %[v1], %[base] nt\n\ts_add_u32 m0, m0, 0x400\n\tv_add_u32 %[v1], %[step], %[v1]\n\t" \
  "global_load_lds_dwordx4 %[v0], %[base] nt\n\ts_add_u32 m0, m0, 0x400\n\tv_add_u32 %[v0], %[step], %[v0]\n\t" \
  "global_load_lds_dwordx4 %[v1], %[base] nt\n\ts_add_u32 m0, m0, 0x400\n\tv_add_u32 %[v1], %[step], %[v1]\n\t" \
  "v_add_u32 %[prog], 4, %[prog]\n\ts_sub_u32 %[cnt], %[cnt], 1\n\ts_cbranch_scc0 1b"
template <int W>
__device__ __forceinline__ void dma_tile(u32 dst, const void* base, u32 v0, u32 v1, u32 step, u32 groups, u32& prog, u32& tn, u32 slot) {
  u32 cnt = groups - 1;
  if constexpr (W > 0)
    asm volatile(DMA_TILE_BODY(DMA_THROTTLE) : [v0] "+&v"(v0), [v1] "+&v"(v1), [prog] "+&v"(prog), [cnt] "+&s"(cnt), [tn] "+&s"(tn)
                 : [dst] "s"(dst), [base] "s"(base), [step] "s"(step), [vz] "v"(0u), [win] "n"(W), [slot] "s"(slot) : "memory", "scc", "s72", "s73");
  else
    asm volatile(DMA_TILE_BODY("") : [v0] "+&v"(v0), [v1] "+&v"(v1), [prog] "+&v"(prog), [cnt] "+&s"(cnt)
                 : [dst] "s"(dst), [base] "s"(base), [step] "s"(step) : "memory", "scc");
}
template <int W>
__device__ __forceinline__ void dma_two(u32 dst, const void* base, u32 v0, u32 v1, u32& prog) {
  asm volatile("s_mov_b32 m0, %[dst]\n\t" DMA_THROTTLE0 "global_load_lds_dwordx4 %[v0], %[base] nt\n\ts_add_u32 m0, m0, 0x400\n\tv_add_u32 %[prog], 2, %[prog]\n\t"
               "global_load_lds_dwordx4 %[v1], %[base] nt"
               : [prog] "+&v"(prog) : [dst] "s"(dst), [base] "s"(base), [v0] "v"(v0), [v1] "v"(v1), [vz] "v"(0u), [win] "n"(W) : "memory", "scc");
}
#define DMA_ROWS_BODY(THR) \
  "s_mov_b32 m0, %[dst]\n\ts_nop 0\n1:\n\t" THR \
  "global_load_lds_dwordx4 %[v0], %[base] nt\n\tglobal_load_lds_dwordx4 %[v1], %[base] offset:1024 nt\n\t" \
  "global_load_lds_dwordx4 %[v2], %[base] offset:2048 nt\n\tglobal_load_lds_dwordx4 %[v3], %[base] offset:3072 nt\n\t" \
  "s_add_u32 m0, m0, 0x1000\n\tv_add_u32 %[v0], 0x1000, %[v0]\n\tv_add_u32 %[v1], 0x1000, %[v1]\n\tv_add_u32 %[v2], 0x1000, %[v2]\n\tv_add_u32 %[v3], 0x1000, %[v3]\n\t" \
  "v_add_u32 %[prog], 4, %[prog]\n\ts_sub_u32 %[cnt], %[cnt], 1\n\ts_cbranch_scc0 1b"
template <int W>
__device__ __forceinline__ void dma_rows(u32 dst, const void* base, u32& v0, u32& v1, u32& v2, u32& v3, u32 groups, u32& prog, u32& tn, u32 slot) {
  u32 cnt = groups - 1;
  if constexpr (W > 0)
    asm volatile(DMA_ROWS_BODY(DMA_THROTTLE) : [v0] "+&v"(v0), [v1] "+&v"(v1), [v2] "+&v"(v2), [v3] "+&v"(v3), [prog] "+&v"(prog), [cnt] "+&s"(cnt), [tn] "+&s"(tn)
                 : [dst] "s"(dst), [base] "s"(base), [vz] "v"(0u), [win] "n"(W), [slot] "s"(slot) : "memory", "scc", "s72", "s73");
  else
    asm volatile(DMA_ROWS_BODY("") : [v0] "+&v"(v0), [v1] "+&v"(v1), [v2] "+&v"(v2), [v3] "+&v"(v3), [prog] "+&v"(prog), [cnt] "+&s"(cnt)
                 : [dst] "s"(dst), [base] "s"(base) : "memory", "scc");
}

__device__ __forceinline__ void final_reduce(const u64* ws, u16* out, int c, int qd, int lane, Tag tag) {
  const u64* src = ws + W_Y3 + (c * 4 + qd) * 512;
  u64 gg[8];
  __builtin_amdgcn_s_sleep(4);
#pragma unroll
  for (int i = 0; i < 8; ++i) gg[i] = ldg(src, 64 * i + lane);
#pragma unroll 1
  for (int spin = 0; spin < SPIN_MAX; ++spin) {
    u32 bad = 0;
#pragma unroll
    for (int i = 0; i < 8; ++i) bad |= gbad(gg[i], tag);
    if (!__any(bad != 0)) break;
#pragma unroll
    for (int i = 0; i < 8; ++i) gg[i] = ldg(src, 64 * i + lane);
  }
  const float v0 = ((gval0(gg[0]) + gval0(gg[1])) + (gval0(gg[2]) + gval0(gg[3]))) + ((gval0(gg[4]) + gval0(gg[5])) + (gval0(gg[6]) + gval0(gg[7])));
  const float v1 = ((gval1(gg[0]) + gval1(gg[1])) + (gval1(gg[2]) + gval1(gg[3]))) + ((gval1(gg[4]) + gval1(gg[5])) + (gval1(gg[6]) + gval1(gg[7])));
  float v = swap16_add(v0, v1);
  v = swap32_add(v, v);
  const u32 o2 = cvtpk(v, dpp<XOR1>(v));
  if (lane < 32 && !(lane & 1)) *(u32*)(out + (lane >> 4) * 512 + 64 * c + 16 * qd + (lane & 15)) = o2;
}

__global__ void __launch_bounds__(NTHR, 3)
fused_decode(const u16* __restrict__ x, const u16* __restrict__ Kc, const u16* __restrict__ Vc, const u16* __restrict__ wq,
             const u16* __restrict__ wo, const u16* __restrict__ wg, const u16* __restrict__ wu, const u16* __restrict__ wd,
             const u16* __restrict__ gw, u16* __restrict__ out, u64* __restrict__ ws) {
  __shared__ __attribute__((aligned(1024))) unsigned char lds_all[LDS_BYTES];
  asm volatile("" :: "v"((LAS unsigned char*)lds_all));
  asm volatile("" :: "s"(x), "s"(Kc), "s"(Vc), "s"(wq), "s"(wo), "s"(wg), "s"(wu), "s"(wd), "s"(gw), "s"(out), "s"(ws));
  const int tid = threadIdx.x, lane = tid & 63;
  const int wid = __builtin_amdgcn_readfirstlane(tid >> 6);
  const int wgid = blockIdx.x, xcd = wgid & 7, idx = wgid >> 3;
  const int hh = idx >> 3, s = idx & 7, h = xcd * 4 + hh;
  const int rq = s >> 1, par = s & 1;
  const int b = s & 1, j = s >> 1;
  const u32 r8 = lane >> 3, r4 = lane >> 4;
  const u32 sw0 = 16u * ((lane & 7) ^ (r8 & 2));
  const size_t kvoff = ((size_t)(b * 32 + h) * 4096 + 3072 + j * 256) * 256;
  u32 k0 = r4 * 256 + 16 * ((lane & 15) ^ r4), k1 = k0 ^ 64, k2 = k0 ^ 128, k3 = k0 ^ 192;
  u32 prog = 28 - WIN;
  if (wid == 0) {
    __builtin_amdgcn_s_setprio(2);
    if (lane < 12) *lp<u32>(M_CNT + 4 * lane) = 0;
    asm volatile("s_mov_b64 exec, 1\n\ts_mov_b32 m0, %0\n\ts_nop 0\n\tglobal_load_lds_dwordx4 %1, %2 sc1\n\ts_mov_b64 exec, -1\n\t"
                 "s_mov_b32 m0, %3\n\ts_nop 0\n\tglobal_load_lds_dwordx4 %4, %5\n\tglobal_load_lds_dwordx4 %4, %5 offset:1024\n\t"
                 "s_mov_b32 m0, %6\n\ts_nop 0\n\tglobal_load_lds_dwordx4 %4, %7"
                 :: "s"(M_TAG), "v"(0u), "s"(ws + W_EPOCH), "s"(M_X), "v"(16u * lane), "s"(x), "s"(M_GW), "s"(gw) : "memory");
    u32 dummy = 0, dummy2 = 0;
    dma_tile<0>(LB_W, (const char*)wq + ((size_t)rq << 20) + 256 * h + 128 * par, r8 * 8192 + sw0, r8 * 8192 + (sw0 ^ 64) + 65536, 131072, 4, dummy, dummy2, 0);
    dma_rows<0>(LB_K, (const char*)Kc + kvoff, k0, k1, k2, k3, 2, dummy, dummy2, 0);
  }
  u32 pf0;
  {
    u64 pc;
    asm volatile("s_getpc_b64 %0" : "=s"(pc));
    const u32 off = min((idx * 4 + (lane & 3)) * 128u, (u32)CODE_BYTES - 128u);
    pc &= ~127ull;
    if (tid >= 64 && tid < 68) asm volatile("global_load_dword %0, %1, %2" : "=v"(pf0) : "v"(off), "s"(pc) : "memory");
  }
  WG_BARRIER("B0");
  const int fgr = hh * 8 + xcd;
  const bool ffn = fgr < 22;
  const int fg = ffn ? fgr : 0;
  const u32 fo = ffn ? 16u : 0u;
  int role = wid;
  asm volatile("" : "+s"(role));
  if (role == 0) {
    const u32 sw1 = sw0 ^ 64;
    u32 tn = 0;
    const u32 slot = PACE;
    { u64 now; asm volatile("s_memrealtime %0\n\ts_waitcnt lgkmcnt(0)" : "=s"(now)); tn = (u32)now << 4; }
    if (ffn)
      dma_tile<WIN>(LB_F, (const char*)((s & 1) ? wu : wg) + (size_t)(256 * (s >> 2) + ((s >> 1) & 1)) * (2 * FF) + 128 * fg - 64 * ((s >> 1) & 1),
                     r8 * (4 * FF) + sw0, r8 * (4 * FF) + sw1 + 8 * 4 * FF, 16 * 4 * FF, 4, prog, tn, slot);
    dma_rows<WIN>(LB_K + 8192, (const char*)Kc + kvoff, k0, k1, k2, k3, 14, prog, tn, slot);
    {
      u32 v0 = r4 * 256 + 16 * ((lane & 15) ^ (r4 << 1)), v1 = v0 ^ 128, v2 = v0, v3 = v1;
      dma_rows<WIN>(LB_V, (const char*)Vc + kvoff, v0, v1, v2, v3, 12, prog, tn, slot);
      if (ffn) lds_wait(C_FC, 4);
      dma_rows<WIN>(LB_V + 49152, (const char*)Vc + kvoff, v0, v1, v2, v3, 4, prog, tn, slot);
    }
    {
      const char* base = (const char*)(wd + (size_t)43 * h * 512 + 64 * s);
      dma_tile<WIN>(LB_D, base, r8 * 1024 + sw0, r8 * 1024 + sw1 + 8192, 16384, 1, prog, tn, slot);
      const u32 rmax = FF - 1 - 43 * h;
      dma_two<WIN>(LB_D + 4096, base, min(32 + r8, rmax) * 1024 + sw0, min(40 + r8, rmax) * 1024 + sw1, prog);
    }
    lds_wait(C_QC, 4);
    dma_tile<WIN>(LB_W, (const char*)(wo + (size_t)128 * h * 512 + 64 * s), r8 * 1024 + sw0, r8 * 1024 + sw1 + 8192, 16384, 4, prog, tn, slot);
    *(volatile LAS u32*)(size_t)C_ISS = 1;
#define DRAIN(k) "s_waitcnt vmcnt(" #k ")\n\tv_add_u32 %1, " STR(WIN) " - " #k ", %0\n\tds_write_b32 %2, %1\n\t"
    u32 t;
    asm volatile(DRAIN(28) DRAIN(24) DRAIN(22) DRAIN(20) DRAIN(16) DRAIN(12) DRAIN(8) DRAIN(4) DRAIN(0) "s_waitcnt lgkmcnt(0)" : "+v"(prog), "=&v"(t) : "v"(0u) : "memory");
    return;
  }
  const int g = lane >> 4, n16 = lane & 15, q4 = n16 >> 2, p4 = lane & 3;
  u32 landed = 0;
  if (role == 9) {
    const u32x4 z4 = {0u, 0u, 0u, 0u};
    *lp<u32x4>(LB_D + 6144 + 16 * lane) = z4; *lp<u32x4>(LB_D + 7168 + 16 * lane) = z4;
    if (lane < 16) *lp<u32x4>(M_H + 16 * lane) = z4;
  }
  lds_wait(C_LANDED, 4, landed);
  const u64 ep = *lp<u64>(M_TAG) + 1;
  const Tag tag = {(u32)ep & 0x3ffu, (u32)(ep >> 10) & 0x3ffu};

  if (role == 9) {
    {
      const u32x4 gv = *lp<u32x4>(M_GW + 16 * lane), x0 = *lp<u32x4>(M_X + 16 * lane), x1 = *lp<u32x4>(M_X + 1024 + 16 * lane);
      float z0 = 0.f, z1 = 0.f;
#pragma unroll
      for (int t = 0; t < 4; ++t) {
        const float gl = bflo(gv[t]), gh = bfhi(gv[t]);
        z0 = dot2(cvtpk(bflo(x0[t]) * gl, bfhi(x0[t]) * gh), 0x3f803f80u, z0);
        z1 = dot2(cvtpk(bflo(x1[t]) * gl, bfhi(x1[t]) * gh), 0x3f803f80u, z1);
      }
      float z = swap32_add(z0, z1);
      z = swap16_add(z, z);
      z += dpp<ROR8>(z); z += dpp<ROR4>(z); z += dpp<ROR2>(z); z += dpp<ROR1>(z);
      const float gt = rbf(__builtin_amdgcn_rcpf(1.f + __expf(-rbf(z))));
      if ((lane & 31) == 0) { *lp<float>(M_G + (lane >> 5) * 8) = gt; *lp<float>(M_G + (lane >> 5) * 8 + 4) = rbf(1.f - gt); }
    }
    lds_wait(C_LANDED, 28 + fo + 8 * FBLK, landed);
    const u32 fb = (43 * h) & ~15u, fe = fb + lane, fo2 = min(fb + 32 + lane, F1_ROW - 1);
    u64 fp[8];
#pragma unroll
    for (int i = 0; i < 8; ++i) fp[i] = ldg(ws + W_F1 + F1_ROW * i, (i & 2) ? fo2 : fe);
    lds_wait(C_P, 8);
    {
      const f32x4 a = *lp<f32x4>(M_WM), c = *lp<f32x4>(M_WM + 16), la = *lp<f32x4>(M_WL), lc = *lp<f32x4>(M_WL + 16);
      const float m = max3(max3(a[0], a[1], a[2]), max3(a[3], c[0], c[1]), max3(c[2], c[3], c[3]));
      const float L = ((la[0] + la[1]) + (la[2] + la[3])) + ((lc[0] + lc[1]) + (lc[2] + lc[3]));
      stg(ws + W_ST + ((h * 2 + b) * 4 + j) * 16, n16, gpack(m, L, tag));
    }
    {
      u32 bad = 0;
#pragma unroll
      for (int i = 0; i < 8; ++i) bad |= gbad(fp[i], tag);
      if (__builtin_expect(__any(bad != 0), 0)) {
#pragma unroll 1
        for (int spin = 0; spin < SPIN_MAX; ++spin) {
          bad = 0;
#pragma unroll
          for (int i = 0; i < 8; ++i) fp[i] = ldg(ws + W_F1 + F1_ROW * i, (i & 2) ? fo2 : fe);
#pragma unroll
          for (int i = 0; i < 8; ++i) bad |= gbad(fp[i], tag);
          if (!__any(bad != 0)) break;
        }
      }
      const u32 gp = cvtpk((gval0(fp[0]) + gval0(fp[1])) + (gval0(fp[2]) + gval0(fp[3])), (gval1(fp[0]) + gval1(fp[1])) + (gval1(fp[2]) + gval1(fp[3])));
      const u32 up = cvtpk((gval0(fp[4]) + gval0(fp[5])) + (gval0(fp[6]) + gval0(fp[7])), (gval1(fp[4]) + gval1(fp[5])) + (gval1(fp[6]) + gval1(fp[7])));
      const u32 hp = cvtpk(bflo(gp) * bflo(up), bfhi(gp) * bfhi(up));
      const u32 i = fe - 43 * h;
      if (i < 43) { *lp<u16>(M_H + 2 * i) = (u16)hp; *lp<u16>(M_H + 128 + 2 * i) = (u16)(hp >> 16); }
      lds_arrive(C_H);
    }
    lds_wait(C_LANDED, 92 + fo + 8 * STBLK, landed);
    {
      const u64* src = ws + W_ST + h * 128;
      u64 s0, s1;
#pragma unroll 1
      for (int spin = 0; spin < SPIN_MAX; ++spin) {
        s0 = ldg(src, lane); s1 = ldg(src, 64 + lane);
        if (!__any((gbad(s0, tag) | gbad(s1, tag)) != 0)) break;
      }
      const u64 sb = (lane & 8) ? s1 : s0;
      const float mj = gval0(sb);
      const float w = __builtin_amdgcn_exp2f((mj - xrow_max(mj)) * LOG2E);
      const float f = w * __builtin_amdgcn_rcpf(xrow_sum(w * gval1(sb)));
      if ((lane & 7) == 0) *lp<float>(M_F + 2 * (lane & 8) + 4 * g) = f;
      lds_arrive(C_F);
    }
    WG_BARRIER("B1 helper");
    if (wgid == 68) {
      const u32 i0 = (lane >> 3) * 512 + ((lane >> 1) & 3) * 64 + (lane & 1) * 32;
#pragma unroll 1
      for (int spin = 0; spin < SPIN_MAX; ++spin) {
        u32 bad = 0;
#pragma unroll
        for (int i = 0; i < 4; ++i) bad |= gbad(ldg(ws + W_Q1 + 4096 * i, i0), tag);
        if (!__any(bad != 0)) { if (lane == 0) stg(ws + W_EPOCH, 0, ep); break; }
        __builtin_amdgcn_s_sleep(8);
      }
    }
    if (hh == 0 && s < 4) final_reduce(ws, out, xcd, s, lane, tag);
    return;
  }

  const int wave = role - 1, ct = wave & 3;
  const int bm = n16 & 1;
  const u32 tile_a = 128 * (8 * g + q4) + 16 * ((2 * ct + (p4 >> 1)) ^ (((q4 >> 1) | ((g & 1) << 1)) << 1)) + 8 * (p4 & 1);
  if (wave < 4) {
    const u32 xa = M_X + 1024 * bm + 256 * rq + 16 * g;
    bf16x8 xf[4];
#pragma unroll
    for (int ks = 0; ks < 4; ++ks) xf[ks] = ld_frag(xa + 64 * ks);
    lds_wait(C_LANDED, 20, landed);
    f32x4 acc = {0.f, 0.f, 0.f, 0.f};
#pragma unroll
    for (int ks = 0; ks < 4; ++ks) acc = mma(xf[ks], ld_tr(LB_W + tile_a + 4096 * ks, LB_W + tile_a + 4096 * ks + 512), acc);
    {
      const float n0 = dpp<XOR1>(acc[0]), n1 = dpp<XOR1>(acc[1]);
      if (lane < 16) stg(ws + W_Q1 + h * 512 + rq * 64 + 32 * par + 8 * ct, 256 * bm + (n16 >> 1), gpack(bm ? n1 : acc[0], bm ? acc[1] : n0, tag));
    }
    lds_arrive(C_QC);
    __builtin_amdgcn_s_sleep(QSLEEP);
    const u64* src = ws + W_Q1 + h * 512 + b * 256 + 16 * wave;
    u64 g0;
#pragma unroll 1
    for (int spin = 0; spin < SPIN_MAX; ++spin) {
      g0 = ldg(src, 64 * g + n16);
      if (!__any(gbad(g0, tag) != 0)) break;
    }
    const u32 qq = cvtpk(xrow_sum(gval0(g0)), xrow_sum(gval1(g0)));
    if (lane < 16) *lp<u32>(M_Q + 4 * (16 * wave + lane)) = qq;
    lds_arrive(C_Q);
  } else if (ffn) {
    const u32 xa = M_X + 1024 * bm + 512 * (s >> 2) + 32 * g, sel = (s & 2) ? 0x07060302u : 0x05040100u;
    u32x4 xf[4];
#pragma unroll
    for (int ks = 0; ks < 4; ++ks) {
      const u32x4 lo = *lp<u32x4>(xa + 128 * ks), hi = *lp<u32x4>(xa + 128 * ks + 16);
      xf[ks] = u32x4{__builtin_amdgcn_perm(lo[1], lo[0], sel), __builtin_amdgcn_perm(lo[3], lo[2], sel),
                     __builtin_amdgcn_perm(hi[1], hi[0], sel), __builtin_amdgcn_perm(hi[3], hi[2], sel)};
    }
    lds_wait(C_LANDED, 44, landed);
    f32x4 acc = {0.f, 0.f, 0.f, 0.f};
#pragma unroll
    for (int ks = 0; ks < 4; ++ks) acc = mma(__builtin_bit_cast(bf16x8, xf[ks]), ld_tr(LB_F + tile_a + 4096 * ks, LB_F + tile_a + 4096 * ks + 512), acc);
    if (lane < 16) stg(ws + W_F1 + ((s & 1) * 4 + (s & 2) + (s >> 2)) * F1_ROW + 64 * fg + 16 * ct, lane, gpack(acc[0], acc[1], tag));
    lds_arrive(C_FC);
  }
  if (wave == 0) asm volatile("" : "+v"(pf0));
  lds_wait(C_LANDED, 28 + 8 * wave + (wave >= 1 ? fo : 0u), landed);
  lds_wait(C_Q, 4);
  float sc[8];
  {
    const u32 ka = LB_K + 256 * (32 * wave + n16) + 16 * (g ^ n16);
    f32x4 d0 = {0.f, 0.f, 0.f, 0.f}, d1 = {0.f, 0.f, 0.f, 0.f};
#pragma unroll
    for (int ks = 0; ks < 4; ++ks) {
      const bf16x8 qf = ld_frag(M_Q + 64 * ks + 16 * g);
      d0 = mma(ld_frag(ka ^ (64 * ks)), qf, d0);
      d1 = mma(ld_frag((ka ^ (64 * ks)) + 4096), qf, d1);
    }
#pragma unroll
    for (int i = 0; i < 4; i += 2) {
      const u32 a = cvtpk(d0[i], d0[i + 1]), c = cvtpk(d1[i], d1[i + 1]);
      const u32 a2 = cvtpk(bflo(a) * 0.08838834764831845f, bfhi(a) * 0.08838834764831845f);
      const u32 c2 = cvtpk(bflo(c) * 0.08838834764831845f, bfhi(c) * 0.08838834764831845f);
      sc[i] = bflo(a2); sc[i + 1] = bfhi(a2); sc[4 + i] = bflo(c2); sc[5 + i] = bfhi(c2);
    }
  }
  {
    float m = xrow_max(max3(max3(sc[0], sc[1], sc[2]), max3(sc[3], sc[4], sc[5]), max3(sc[6], sc[7], sc[7])));
    if (lane == 0) *lp<float>(M_WM + 4 * wave) = m;
    lds_arrive(C_M);
    lds_wait(C_M, 8);
    const f32x4 a = *lp<f32x4>(M_WM), c = *lp<f32x4>(M_WM + 16);
    m = max3(max3(a[0], a[1], a[2]), max3(a[3], c[0], c[1]), max3(c[2], c[3], c[3]));
    float p[8], l = 0.f;
#pragma unroll
    for (int i = 0; i < 8; ++i) { p[i] = __builtin_amdgcn_exp2f((sc[i] - m) * LOG2E); l += p[i]; }
    l = xrow_sum(l);
    const u32x4 pf = {cvtpk(p[0], p[1]), cvtpk(p[2], p[3]), cvtpk(p[4], p[5]), cvtpk(p[6], p[7])};
    if (n16 == 0) *lp<u32x4>(M_P + 64 * wave + 16 * g) = pf;
    if (lane == 0) *lp<float>(M_WL + 4 * wave) = l;
  }
  lds_arrive(C_P);
  lds_wait(C_P, 8);
  f32x4 oacc = {0.f, 0.f, 0.f, 0.f};
  {
    const u32 va = LB_V + 256 * (4 * g + q4) + 16 * ((2 * wave + (p4 >> 1)) ^ ((4 * (g & 1) + q4) << 1)) + 8 * (p4 & 1);
    bf16x8 pfr[8];
#pragma unroll
    for (int t = 0; t < 8; ++t) pfr[t] = ld_frag(M_P + 64 * t + 16 * g);
    lds_wait(C_LANDED, 92 + fo + 8 * 5, landed);
#pragma unroll
    for (int t = 0; t < 6; ++t) oacc = mma(pfr[t], ld_tr(va + 8192 * t, va + 8192 * t + 4096), oacc);
    lds_wait(C_LANDED, 92 + fo + 8 * 6, landed);
    oacc = mma(pfr[6], ld_tr(va + 8192 * 6, va + 8192 * 6 + 4096), oacc);
    lds_wait_hot(C_LANDED, 92 + fo + 8 * 7, landed);
    oacc = mma(pfr[7], ld_tr(va + 8192 * 7, va + 8192 * 7 + 4096), oacc);
  }
  {
    const float on = dpp<XOR1>(oacc[0]);
    if (lane < 16 && !(lane & 1)) stg(ws + W_O2 + ((h * 2 + b) * 4 + j) * 64 + 8 * wave, lane >> 1, gpack(oacc[0], on, tag));
  }
  const u64* vs = ws + W_O2 + h * 512 + (wave & 1) * 256 + 16 * (wave >> 1);
  const u32 vi = 64 * p4 + (lane >> 2);
  lds_wait(C_ISS, 1);
  u64 gg = ldg(vs, vi);
  bf16x8 wof[4];
  float yb0 = 0.f, yb1 = 0.f;
  f32x4 gates;
  if (wave < 4) {
    lds_wait(C_H, 1);
    gates = *lp<f32x4>(M_G);
    lds_wait(C_LANDED, 154 + fo, landed);
    f32x4 acc = {0.f, 0.f, 0.f, 0.f};
#pragma unroll
    for (int ks = 0; ks < 2; ++ks) acc = mma(ld_frag(M_H + 128 * bm + 64 * ks + 16 * g), ld_tr(LB_D + tile_a + 4096 * ks, LB_D + tile_a + 4096 * ks + 512), acc);
    yb0 = gates[1] * acc[0]; yb1 = gates[3] * acc[1];
    lds_wait_hot(C_LANDED, 170 + fo, landed);
#pragma unroll
    for (int ks = 0; ks < 4; ++ks) wof[ks] = ld_tr(LB_W + tile_a + 4096 * ks, LB_W + tile_a + 4096 * ks + 512);
  }
  lds_wait(C_F, 1);
  const float fq = *lp<float>(M_F + 16 * (wave & 1) + 4 * p4);
  if (__builtin_expect(__any(gbad(gg, tag) != 0), 0)) {
#pragma unroll 1
    for (int spin = 0; spin < SPIN_MAX; ++spin) {
      gg = ldg(vs, vi);
      if (!__any(gbad(gg, tag) != 0)) break;
      __builtin_amdgcn_s_sleep(1);
    }
  }
  {
    float t0 = fq * gval0(gg), t1 = fq * gval1(gg);
    t0 += dpp<XOR1>(t0); t1 += dpp<XOR1>(t1); t0 += dpp<XOR2>(t0); t1 += dpp<XOR2>(t1);
    const u32 ob = cvtpk(t0, t1);
    if (p4 == 0) *lp<u32>(M_O + 256 * (wave & 1) + 64 * (wave >> 1) + lane) = ob;
  }
  WG_BARRIER("B1");
  if (wave < 4) {
    f32x4 acc = {0.f, 0.f, 0.f, 0.f};
#pragma unroll
    for (int ks = 0; ks < 4; ++ks) acc = mma(ld_frag(M_O + 256 * bm + 64 * ks + 16 * g), wof[ks], acc);
    if (lane < 16) stg(ws + W_Y3 + ((s * 4 + ct) * 32 + h) * 16, lane, gpack(fmaf(gates[0], acc[0], yb0), fmaf(gates[2], acc[1], yb1), tag));
  }
}
}

extern "C" void kernel_launch(void* const* d_in, const int* in_sizes, int n_in, void* d_out, int out_size, void* d_ws, size_t ws_size,
                              hipStream_t stream) {
  fused_decode<<<NWG, NTHR, 0, stream>>>((const u16*)d_in[0], (const u16*)d_in[1], (const u16*)d_in[2], (const u16*)d_in[3],
                                         (const u16*)d_in[4], (const u16*)d_in[6], (const u16*)d_in[7], (const u16*)d_in[8],
                                         (const u16*)d_in[9], (u16*)d_out, (u64*)d_ws);
}
```

```cpp
#include <hip/hip_runtime.h>
#include <stdint.h>

typedef unsigned short u16;
typedef unsigned int u32;
typedef unsigned long long u64;
typedef u32 u32x4 __attribute__((ext_vector_type(4)));
typedef short s16x4 __attribute__((ext_vector_type(4)));
typedef short s16x8 __attribute__((ext_vector_type(8)));
typedef float f32x2 __attribute__((ext_vector_type(2)));
typedef float f32x4 __attribute__((ext_vector_type(4)));
typedef __bf16 bf16x2 __attribute__((ext_vector_type(2)));
typedef __bf16 bf16x8 __attribute__((ext_vector_type(8)));
#define GAS __attribute__((address_space(1)))
#define LAS __attribute__((address_space(3)))
typedef GAS u64 gu64;

namespace {
constexpr int NWG = 256, NTHR = 640;
constexpr int FF = 1376;
constexpr u32 W_EPOCH = 0;
constexpr u32 W_Q1 = 1024;
constexpr u32 F1_ROW = 1408;
constexpr u32 W_F1 = W_Q1 + 32 * 512;
constexpr u32 W_O2 = W_F1 + 8 * F1_ROW;
constexpr u32 W_ST = W_O2 + 32 * 512;
constexpr u32 W_Y3 = W_ST + 32 * 128;
constexpr u32 W_END = W_Y3 + 32 * 512;
constexpr u32 LB_M = 0;
constexpr u32 M_CNT = LB_M;
constexpr u32 M_TAG = M_CNT + 48;
constexpr u32 M_G = M_TAG + 16;
constexpr u32 M_WM = M_G + 16;
constexpr u32 M_WL = M_WM + 32;
constexpr u32 M_F = M_WL + 32;
constexpr u32 M_Q = 256;
constexpr u32 M_P = M_Q + 256;
constexpr u32 M_O = M_P + 512;
constexpr u32 M_H = M_O + 512;
constexpr u32 M_X = 2048;
constexpr u32 M_GW = M_X + 2048;
constexpr u32 LB_W = 8192;
constexpr u32 LB_D = LB_W + 16384;
constexpr u32 LB_K = LB_D + 8192;
constexpr u32 LB_V = LB_K + 65536;
constexpr u32 LB_F = LB_V + 49152;
constexpr u32 C_LANDED = M_CNT, C_F = M_CNT + 4, C_QC = M_CNT + 8, C_FC = M_CNT + 12, C_Q = M_CNT + 16, C_M = M_CNT + 20, C_P = M_CNT + 24, C_H = M_CNT + 28,
              C_ISS = M_CNT + 32;
constexpr u32 LDS_BYTES = 163840;
constexpr int SPIN_MAX = 1 << 15, LSPIN_MAX = 1 << 21;
constexpr int CODE_BYTES = 15872;
constexpr float LOG2E = 1.4426950408889634f;
#define WIN 28
constexpr u32 PACE = 220;
constexpr int QSLEEP = 36;
constexpr int FBLK = 6, STBLK = 4;

__device__ __forceinline__ u32 cvtpk(float lo, float hi) {
  const f32x2 v = {lo, hi};
  u32 r = __builtin_bit_cast(u32, __builtin_convertvector(v, bf16x2));
  asm("" : "+v"(r));
  return r;
}
__device__ __forceinline__ float max2(float a, float b) { return __builtin_amdgcn_fmed3f(a, b, __builtin_inff()); }
__device__ __forceinline__ float max3(float a, float b, float c) { return max2(max2(a, b), c); }
__device__ __forceinline__ float dot2(u32 a, u32 b, float c) {
  return __builtin_amdgcn_fdot2_f32_bf16(__builtin_bit_cast(bf16x2, a), __builtin_bit_cast(bf16x2, b), c, false);
}
__device__ __forceinline__ float bflo(u32 p) { return __uint_as_float(p << 16); }
__device__ __forceinline__ float bfhi(u32 p) { return __uint_as_float(p & 0xffff0000u); }
__device__ __forceinline__ float rbf(float x) { return bflo(cvtpk(x, x)); }
template <int CTRL> __device__ __forceinline__ float dpp(float x) {
  return __builtin_bit_cast(float, __builtin_amdgcn_mov_dpp(__builtin_bit_cast(int, x), CTRL, 0xf, 0xf, true));
}
constexpr int XOR1 = 0xB1, XOR2 = 0x4E, ROR8 = 0x128, ROR4 = 0x124, ROR2 = 0x122, ROR1 = 0x121;
__device__ __forceinline__ float swap32_add(float a, float b) {
  auto t = __builtin_amdgcn_permlane32_swap(__float_as_uint(a), __float_as_uint(b), false, false);
  return __uint_as_float(t[0]) + __uint_as_float(t[1]);
}
__device__ __forceinline__ float swap16_add(float a, float b) {
  auto t = __builtin_amdgcn_permlane16_swap(__float_as_uint(a), __float_as_uint(b), false, false);
  return __uint_as_float(t[0]) + __uint_as_float(t[1]);
}
__device__ __forceinline__ float xrow_max(float x) {
  auto s = __builtin_amdgcn_permlane16_swap(__float_as_uint(x), __float_as_uint(x), false, false);
  x = max2(__uint_as_float(s[0]), __uint_as_float(s[1]));
  auto t = __builtin_amdgcn_permlane32_swap(__float_as_uint(x), __float_as_uint(x), false, false);
  return max2(__uint_as_float(t[0]), __uint_as_float(t[1]));
}
__device__ __forceinline__ float xrow_sum(float x) { x = swap16_add(x, x); return swap32_add(x, x); }
__device__ __forceinline__ u64 ldg(const u64* base, u32 i) { return __hip_atomic_load((const gu64*)((const GAS char*)base + 8u * i), __ATOMIC_RELAXED, __HIP_MEMORY_SCOPE_AGENT); }
__device__ __forceinline__ void stg(u64* base, u32 i, u64 v) { __hip_atomic_store((gu64*)((GAS char*)base + 8u * i), v, __ATOMIC_RELAXED, __HIP_MEMORY_SCOPE_AGENT); }
__device__ __forceinline__ u64 gaddr(const u64* base, u32 i) { u64 a = (u64)(base + i); asm volatile("" : "+v"(a)); return a; }
__device__ __forceinline__ u64 ldga(u64 a) { return __hip_atomic_load((const gu64*)a, __ATOMIC_RELAXED, __HIP_MEMORY_SCOPE_AGENT); }
__device__ __forceinline__ void stga(u64 a, u64 v) { __hip_atomic_store((gu64*)a, v, __ATOMIC_RELAXED, __HIP_MEMORY_SCOPE_AGENT); }
struct Tag { u32 a, b; };
__device__ __forceinline__ u64 gpack(float v0, float v1, Tag t) {
  const u32 lo = ((__float_as_uint(v0) + 0x200u) & 0xfffffc00u) | t.a, hi = ((__float_as_uint(v1) + 0x200u) & 0xfffffc00u) | t.b;
  return ((u64)hi << 32) | lo;
}
__device__ __forceinline__ float gval0(u64 g) { return __uint_as_float((u32)g & 0xfffffc00u); }
__device__ __forceinline__ float gval1(u64 g) { return __uint_as_float((u32)(g >> 32) & 0xfffffc00u); }
__device__ __forceinline__ u32 gbad(u64 g, Tag t) { return (((u32)g ^ t.a) | ((u32)(g >> 32) ^ t.b)) & 0x3ffu; }

template <typename T> __device__ __forceinline__ LAS T* lp(u32 a) { return (LAS T*)(size_t)a; }
__device__ __forceinline__ bf16x8 ld_frag(u32 a) { return __builtin_bit_cast(bf16x8, *lp<u32x4>(a)); }
__device__ __forceinline__ bf16x8 ld_tr(u32 a0, u32 a1) {
  const s16x4 lo = __builtin_amdgcn_ds_read_tr16_b64_v4i16(lp<s16x4>(a0)), hi = __builtin_amdgcn_ds_read_tr16_b64_v4i16(lp<s16x4>(a1));
  return __builtin_bit_cast(bf16x8, __builtin_shufflevector(lo, hi, 0, 1, 2, 3, 4, 5, 6, 7));
}
__device__ __forceinline__ f32x4 mma(bf16x8 a, bf16x8 b, f32x4 c) { return __builtin_amdgcn_mfma_f32_16x16x32_bf16(a, b, c, 0, 0, 0); }
__device__ __forceinline__ void lds_arrive(u32 c) {
  asm volatile("s_waitcnt lgkmcnt(0)\n\ts_mov_b64 exec, 1\n\tds_add_u32 %0, %1\n\ts_mov_b64 exec, -1" :: "v"(c), "v"(1u) : "memory");
}
__device__ __forceinline__ void lds_wait(u32 c, u32 n, u32& seen) {
  u32 v, cnt = LSPIN_MAX;
  asm volatile("s_cmp_ge_u32 %1, %4\n\ts_cbranch_scc1 2f\n"
               "1:\n\tds_read_b32 %0, %3\n\ts_waitcnt lgkmcnt(0)\n\tv_readfirstlane_b32 %1, %0\n\ts_cmp_ge_u32 %1, %4\n\ts_cbranch_scc1 2f\n\t"
               "s_sleep 1\n\ts_sub_u32 %2, %2, 1\n\ts_cbranch_scc0 1b\n2:"
               : "=&v"(v), "+&s"(seen), "+&s"(cnt) : "v"(c), "s"(n) : "memory", "scc");
}
__device__ __forceinline__ void lds_wait(u32 c, u32 n) { u32 seen = 0; lds_wait(c, n, seen); }
__device__ __forceinline__ void lds_wait_hot(u32 c, u32 n, u32& seen) {
  u32 v, cnt = LSPIN_MAX;
  asm volatile("s_cmp_ge_u32 %1, %4\n\ts_cbranch_scc1 2f\n"
               "1:\n\tds_read_b32 %0, %3\n\ts_waitcnt lgkmcnt(0)\n\tv_readfirstlane_b32 %1, %0\n\ts_cmp_ge_u32 %1, %4\n\ts_cbranch_scc1 2f\n\t"
               "s_sub_u32 %2, %2, 1\n\ts_cbranch_scc0 1b\n2:"
               : "=&v"(v), "+&s"(seen), "+&s"(cnt) : "v"(c), "s"(n) : "memory", "scc");
}
#define WG_BARRIER(id) asm volatile("s_waitcnt lgkmcnt(0)\n\ts_barrier ; " id ::: "memory")
#define STR2(x) #x
#define STR(x) STR2(x)
#define DMA_PACE "2:\n\ts_memrealtime s[72:73]\n\ts_waitcnt lgkmcnt(0)\n\ts_lshl_b32 s72, s72, 4\n\ts_sub_u32 s72, s72, %[tn]\n\ts_cmp_lt_i32 s72, 0\n\ts_cbranch_scc1 2b\n\ts_add_u32 %[tn], %[tn], %[slot]\n\t"
#define DMA_THROTTLE0 "s_waitcnt vmcnt(%[win])\n\tds_write_b32 %[vz], %[prog]\n\t"
#define DMA_THROTTLE DMA_THROTTLE0 DMA_PACE
#define DMA_TILE_BODY(THR) \
  "s_mov_b32 m0, %[dst]\n\ts_nop 0\n1:\n\t" THR \
  "global_load_lds_dwordx4 %[v0], %[base] nt\n\ts_add_u32 m0, m0, 0x400\n\tv_add_u32 %[v0], %[step], %[v0]\n\t" \
  "global_load_lds_dwordx4 %[v1], %[base] nt\n\ts_add_u32 m0, m0, 0x400\n\tv_add_u32 %[v1], %[step], %[v1]\n\t" \
  "global_load_lds_dwordx4 %[v0], %[base] nt\n\ts_add_u32 m0, m0, 0x400\n\tv_add_u32 %[v0], %[step], %[v0]\n\t" \
  "global_load_lds_dwordx4 %[v1], %[base] nt\n\ts_add_u32 m0, m0, 0x400\n\tv_add_u32 %[v1], %[step], %[v1]\n\t" \
  "v_add_u32 %[prog], 4, %[prog]\n\ts_sub_u32 %[cnt], %[cnt], 1\n\ts_cbranch_scc0 1b"
template <int W>
__device__ __forceinline__ void dma_tile(u32 dst, const void* base, u32 v0, u32 v1, u32 step, u32 groups, u32& prog, u32& tn, u32 slot) {
  u32 cnt = groups - 1;
  if constexpr (W > 0)
    asm volatile(DMA_TILE_BODY(DMA_THROTTLE) : [v0] "+&v"(v0), [v1] "+&v"(v1), [prog] "+&v"(prog), [cnt] "+&s"(cnt), [tn] "+&s"(tn)
                 : [dst] "s"(dst), [base] "s"(base), [step] "s"(step), [vz] "v"(0u), [win] "n"(W), [slot] "s"(slot) : "memory", "scc", "s72", "s73");
  else
    asm volatile(DMA_TILE_BODY("") : [v0] "+&v"(v0), [v1] "+&v"(v1), [prog] "+&v"(prog), [cnt] "+&s"(cnt)
                 : [dst] "s"(dst), [base] "s"(base), [step] "s"(step) : "memory", "scc");
}
template <int W>
__device__ __forceinline__ void dma_two(u32 dst, const void* base, u32 v0, u32 v1, u32& prog) {
  asm volatile("s_mov_b32 m0, %[dst]\n\t" DMA_THROTTLE0 "global_load_lds_dwordx4 %[v0], %[base] nt\n\ts_add_u32 m0, m0, 0x400\n\tv_add_u32 %[prog], 2, %[prog]\n\t"
               "global_load_lds_dwordx4 %[v1], %[base] nt"
               : [prog] "+&v"(prog) : [dst] "s"(dst), [base] "s"(base), [v0] "v"(v0), [v1] "v"(v1), [vz] "v"(0u), [win] "n"(W) : "memory", "scc");
}
#define DMA_ROWS_BODY(THR) \
  "s_mov_b32 m0, %[dst]\n\ts_nop 0\n1:\n\t" THR \
  "global_load_lds_dwordx4 %[v0], %[base] nt\n\tglobal_load_lds_dwordx4 %[v1], %[base] offset:1024 nt\n\t" \
  "global_load_lds_dwordx4 %[v2], %[base] offset:2048 nt\n\tglobal_load_lds_dwordx4 %[v3], %[base] offset:3072 nt\n\t" \
  "s_add_u32 m0, m0, 0x1000\n\tv_add_u32 %[v0], 0x1000, %[v0]\n\tv_add_u32 %[v1], 0x1000, %[v1]\n\tv_add_u32 %[v2], 0x1000, %[v2]\n\tv_add_u32 %[v3], 0x1000, %[v3]\n\t" \
  "v_add_u32 %[prog], 4, %[prog]\n\ts_sub_u32 %[cnt], %[cnt], 1\n\ts_cbranch_scc0 1b"
template <int W>
__device__ __forceinline__ void dma_rows(u32 dst, const void* base, u32& v0, u32& v1, u32& v2, u32& v3, u32 groups, u32& prog, u32& tn, u32 slot) {
  u32 cnt = groups - 1;
  if constexpr (W > 0)
    asm volatile(DMA_ROWS_BODY(DMA_THROTTLE) : [v0] "+&v"(v0), [v1] "+&v"(v1), [v2] "+&v"(v2), [v3] "+&v"(v3), [prog] "+&v"(prog), [cnt] "+&s"(cnt), [tn] "+&s"(tn)
                 : [dst] "s"(dst), [base] "s"(base), [vz] "v"(0u), [win] "n"(W), [slot] "s"(slot) : "memory", "scc", "s72", "s73");
  else
    asm volatile(DMA_ROWS_BODY("") : [v0] "+&v"(v0), [v1] "+&v"(v1), [v2] "+&v"(v2), [v3] "+&v"(v3), [prog] "+&v"(prog), [cnt] "+&s"(cnt)
                 : [dst] "s"(dst), [base] "s"(base) : "memory", "scc");
}

__device__ __forceinline__ void final_reduce(const u64* ws, u16* out, int c, int qd, int lane, Tag tag) {
  const u64* src = ws + W_Y3 + (c * 4 + qd) * 512;
  u64 gg[8];
  __builtin_amdgcn_s_sleep(4);
#pragma unroll
  for (int i = 0; i < 8; ++i) gg[i] = ldg(src, 64 * i + lane);
#pragma unroll 1
  for (int spin = 0; spin < SPIN_MAX; ++spin) {
    u32 bad = 0;
#pragma unroll
    for (int i = 0; i < 8; ++i) bad |= gbad(gg[i], tag);
    if (!__any(bad != 0)) break;
#pragma unroll
    for (int i = 0; i < 8; ++i) gg[i] = ldg(src, 64 * i + lane);
  }
  const float v0 = ((gval0(gg[0]) + gval0(gg[1])) + (gval0(gg[2]) + gval0(gg[3]))) + ((gval0(gg[4]) + gval0(gg[5])) + (gval0(gg[6]) + gval0(gg[7])));
  const float v1 = ((gval1(gg[0]) + gval1(gg[1])) + (gval1(gg[2]) + gval1(gg[3]))) + ((gval1(gg[4]) + gval1(gg[5])) + (gval1(gg[6]) + gval1(gg[7])));
  float v = swap16_add(v0, v1);
  v = swap32_add(v, v);
  const u32 o2 = cvtpk(v, dpp<XOR1>(v));
  if (lane < 32 && !(lane & 1)) *(u32*)(out + (lane >> 4) * 512 + 64 * c + 16 * qd + (lane & 15)) = o2;
}

__global__ void __launch_bounds__(NTHR, 3)
fused_decode(const u16* __restrict__ x, const u16* __restrict__ Kc, const u16* __restrict__ Vc, const u16* __restrict__ wq,
             const u16* __restrict__ wo, const u16* __restrict__ wg, const u16* __restrict__ wu, const u16* __restrict__ wd,
             const u16* __restrict__ gw, u16* __restrict__ out, u64* __restrict__ ws) {
  __shared__ __attribute__((aligned(1024))) unsigned char lds_all[LDS_BYTES];
  asm volatile("" :: "v"((LAS unsigned char*)lds_all));
  asm volatile("" :: "s"(x), "s"(Kc), "s"(Vc), "s"(wq), "s"(wo), "s"(wg), "s"(wu), "s"(wd), "s"(gw), "s"(out), "s"(ws));
  const int tid = threadIdx.x, lane = tid & 63;
  const int wid = __builtin_amdgcn_readfirstlane(tid >> 6);
  const int wgid = blockIdx.x, xcd = wgid & 7, idx = wgid >> 3;
  const int hh = idx >> 3, s = idx & 7, h = xcd * 4 + hh;
  const int rq = s >> 1, par = s & 1;
  const int b = s & 1, j = s >> 1;
  const u32 r8 = lane >> 3, r4 = lane >> 4;
  const u32 sw0 = 16u * ((lane & 7) ^ (r8 & 2));
  const size_t kvoff = ((size_t)(b * 32 + h) * 4096 + 3072 + j * 256) * 256;
  u32 k0 = r4 * 256 + 16 * ((lane & 15) ^ r4), k1 = k0 ^ 64, k2 = k0 ^ 128, k3 = k0 ^ 192;
  u32 prog = 28 - WIN;
  if (wid == 0) {
    __builtin_amdgcn_s_setprio(2);
    if (lane < 12) *lp<u32>(M_CNT + 4 * lane) = 0;
    asm volatile("s_mov_b64 exec, 1\n\ts_mov_b32 m0, %0\n\ts_nop 0\n\tglobal_load_lds_dwordx4 %1, %2 sc1\n\ts_mov_b64 exec, -1\n\t"
                 "s_mov_b32 m0, %3\n\ts_nop 0\n\tglobal_load_lds_dwordx4 %4, %5\n\tglobal_load_lds_dwordx4 %4, %5 offset:1024\n\t"
                 "s_mov_b32 m0, %6\n\ts_nop 0\n\tglobal_load_lds_dwordx4 %4, %7"
                 :: "s"(M_TAG), "v"(0u), "s"(ws + W_EPOCH), "s"(M_X), "v"(16u * lane), "s"(x), "s"(M_GW), "s"(gw) : "memory");
    u32 dummy = 0, dummy2 = 0;
    dma_tile<0>(LB_W, (const char*)wq + ((size_t)rq << 20) + 256 * h + 128 * par, r8 * 8192 + sw0, r8 * 8192 + (sw0 ^ 64) + 65536, 131072, 4, dummy, dummy2, 0);
    dma_rows<0>(LB_K, (const char*)Kc + kvoff, k0, k1, k2, k3, 2, dummy, dummy2, 0);
  }
  u32 pf0;
  {
    u64 pc;
    asm volatile("s_getpc_b64 %0" : "=s"(pc));
    const u32 off = min((idx * 4 + (lane & 3)) * 128u, (u32)CODE_BYTES - 128u);
    pc &= ~127ull;
    if (tid >= 64 && tid < 68) asm volatile("global_load_dword %0, %1, %2" : "=v"(pf0) : "v"(off), "s"(pc) : "memory");
  }
  WG_BARRIER("B0");
  const int fgr = (int)((0x46573210u >> (4 * xcd)) & 15u) * 4 + hh;
  const bool ffn = fgr < 22;
  const int fg = ffn ? fgr : 0;
  const u32 fo = ffn ? 16u : 0u;
  int role = wid;
  asm volatile("" : "+s"(role));
  if (role == 0) {
    const u32 sw1 = sw0 ^ 64;
    u32 tn = 0;
    const u32 slot = PACE;
    { u64 now; asm volatile("s_memrealtime %0\n\ts_waitcnt lgkmcnt(0)" : "=s"(now)); tn = (u32)now << 4; }
    if (ffn)
      dma_tile<WIN>(LB_F, (const char*)((s & 1) ? wu : wg) + (size_t)(256 * (s >> 2) + ((s >> 1) & 1)) * (2 * FF) + 128 * fg - 64 * ((s >> 1) & 1),
                     r8 * (4 * FF) + sw0, r8 * (4 * FF) + sw1 + 8 * 4 * FF, 16 * 4 * FF, 4, prog, tn, slot);
    dma_rows<WIN>(LB_K + 8192, (const char*)Kc + kvoff, k0, k1, k2, k3, 14, prog, tn, slot);
    {
      u32 v0 = r4 * 256 + 16 * ((lane & 15) ^ (r4 << 1)), v1 = v0 ^ 128, v2 = v0, v3 = v1;
      dma_rows<WIN>(LB_V, (const char*)Vc + kvoff, v0, v1, v2, v3, 12, prog, tn, slot);
      if (ffn) lds_wait(C_FC, 4);
      dma_rows<WIN>(LB_V + 49152, (const char*)Vc + kvoff, v0, v1, v2, v3, 4, prog, tn, slot);
    }
    {
      const char* base = (const char*)(wd + (size_t)43 * h * 512 + 64 * s);
      dma_tile<WIN>(LB_D, base, r8 * 1024 + sw0, r8 * 1024 + sw1 + 8192, 16384, 1, prog, tn, slot);
      const u32 rmax = FF - 1 - 43 * h;
      dma_two<WIN>(LB_D + 4096, base, min(32 + r8, rmax) * 1024 + sw0, min(40 + r8, rmax) * 1024 + sw1, prog);
    }
    lds_wait(C_QC, 4);
    dma_tile<WIN>(LB_W, (const char*)(wo + (size_t)128 * h * 512 + 64 * s), r8 * 1024 + sw0, r8 * 1024 + sw1 + 8192, 16384, 4, prog, tn, slot);
    *(volatile LAS u32*)(size_t)C_ISS = 1;
#define DRAIN(k) "s_waitcnt vmcnt(" #k ")\n\tv_add_u32 %1, " STR(WIN) " - " #k ", %0\n\tds_write_b32 %2, %1\n\t"
    u32 t;
    asm volatile(DRAIN(28) DRAIN(24) DRAIN(22) DRAIN(20) DRAIN(16) DRAIN(12) DRAIN(8) DRAIN(4) DRAIN(0) "s_waitcnt lgkmcnt(0)" : "+v"(prog), "=&v"(t) : "v"(0u) : "memory");
    return;
  }
  const int g = lane >> 4, n16 = lane & 15, q4 = n16 >> 2, p4 = lane & 3;
  u32 landed = 0;
  if (role == 9) {
    const u32x4 z4 = {0u, 0u, 0u, 0u};
    *lp<u32x4>(LB_D + 6144 + 16 * lane) = z4; *lp<u32x4>(LB_D + 7168 + 16 * lane) = z4;
    if (lane < 16) *lp<u32x4>(M_H + 16 * lane) = z4;
  }
  lds_wait(C_LANDED, 4, landed);
  const u64 ep = *lp<u64>(M_TAG) + 1;
  const Tag tag = {(u32)ep & 0x3ffu, (u32)(ep >> 10) & 0x3ffu};

  if (role == 9) {
    {
      const u32x4 gv = *lp<u32x4>(M_GW + 16 * lane), x0 = *lp<u32x4>(M_X + 16 * lane), x1 = *lp<u32x4>(M_X + 1024 + 16 * lane);
      float z0 = 0.f, z1 = 0.f;
#pragma unroll
      for (int t = 0; t < 4; ++t) {
        const float gl = bflo(gv[t]), gh = bfhi(gv[t]);
        z0 = dot2(cvtpk(bflo(x0[t]) * gl, bfhi(x0[t]) * gh), 0x3f803f80u, z0);
        z1 = dot2(cvtpk(bflo(x1[t]) * gl, bfhi(x1[t]) * gh), 0x3f803f80u, z1);
      }
      float z = swap32_add(z0, z1);
      z = swap16_add(z, z);
      z += dpp<ROR8>(z); z += dpp<ROR4>(z); z += dpp<ROR2>(z); z += dpp<ROR1>(z);
      const float gt = rbf(__builtin_amdgcn_rcpf(1.f + __expf(-rbf(z))));
      if ((lane & 31) == 0) { *lp<float>(M_G + (lane >> 5) * 8) = gt; *lp<float>(M_G + (lane >> 5) * 8 + 4) = rbf(1.f - gt); }
    }
    lds_wait(C_LANDED, 28 + fo + 8 * FBLK, landed);
    const u32 fb = (43 * h) & ~15u, fe = fb + lane, fo2 = min(fb + 32 + lane, F1_ROW - 1);
    u64 fp[8];
#pragma unroll
    for (int i = 0; i < 8; ++i) fp[i] = ldg(ws + W_F1 + F1_ROW * i, (i & 2) ? fo2 : fe);
    lds_wait(C_P, 8);
    {
      const f32x4 a = *lp<f32x4>(M_WM), c = *lp<f32x4>(M_WM + 16), la = *lp<f32x4>(M_WL), lc = *lp<f32x4>(M_WL + 16);
      const float m = max3(max3(a[0], a[1], a[2]), max3(a[3], c[0], c[1]), max3(c[2], c[3], c[3]));
      const float L = ((la[0] + la[1]) + (la[2] + la[3])) + ((lc[0] + lc[1]) + (lc[2] + lc[3]));
      stg(ws + W_ST + ((h * 2 + b) * 4 + j) * 16, n16, gpack(m, L, tag));
    }
    {
      u32 bad = 0;
#pragma unroll
      for (int i = 0; i < 8; ++i) bad |= gbad(fp[i], tag);
      if (__builtin_expect(__any(bad != 0), 0)) {
#pragma unroll 1
        for (int spin = 0; spin < SPIN_MAX; ++spin) {
          bad = 0;
#pragma unroll
          for (int i = 0; i < 8; ++i) fp[i] = ldg(ws + W_F1 + F1_ROW * i, (i & 2) ? fo2 : fe);
#pragma unroll
          for (int i = 0; i < 8; ++i) bad |= gbad(fp[i], tag);
          if (!__any(bad != 0)) break;
        }
      }
      const u32 gp = cvtpk((gval0(fp[0]) + gval0(fp[1])) + (gval0(fp[2]) + gval0(fp[3])), (gval1(fp[0]) + gval1(fp[1])) + (gval1(fp[2]) + gval1(fp[3])));
      const u32 up = cvtpk((gval0(fp[4]) + gval0(fp[5])) + (gval0(fp[6]) + gval0(fp[7])), (gval1(fp[4]) + gval1(fp[5])) + (gval1(fp[6]) + gval1(fp[7])));
      const u32 hp = cvtpk(bflo(gp) * bflo(up), bfhi(gp) * bfhi(up));
      const u32 i = fe - 43 * h;
      if (i < 43) { *lp<u16>(M_H + 2 * i) = (u16)hp; *lp<u16>(M_H + 128 + 2 * i) = (u16)(hp >> 16); }
      lds_arrive(C_H);
    }
    lds_wait(C_LANDED, 92 + fo + 8 * STBLK, landed);
    {
      const u64* src = ws + W_ST + h * 128;
      u64 s0, s1;
#pragma unroll 1
      for (int spin = 0; spin < SPIN_MAX; ++spin) {
        s0 = ldg(src, lane); s1 = ldg(src, 64 + lane);
        if (!__any((gbad(s0, tag) | gbad(s1, tag)) != 0)) break;
      }
      const u64 sb = (lane & 8) ? s1 : s0;
      const float mj = gval0(sb);
      const float w = __builtin_amdgcn_exp2f((mj - xrow_max(mj)) * LOG2E);
      const float f = w * __builtin_amdgcn_rcpf(xrow_sum(w * gval1(sb)));
      if ((lane & 7) == 0) *lp<float>(M_F + 2 * (lane & 8) + 4 * g) = f;
      lds_arrive(C_F);
    }
    WG_BARRIER("B1 helper");
    if (wgid == 68) {
      const u32 i0 = (lane >> 3) * 512 + ((lane >> 1) & 3) * 64 + (lane & 1) * 32;
#pragma unroll 1
      for (int spin = 0; spin < SPIN_MAX; ++spin) {
        u32 bad = 0;
#pragma unroll
        for (int i = 0; i < 4; ++i) bad |= gbad(ldg(ws + W_Q1 + 4096 * i, i0), tag);
        if (!__any(bad != 0)) { if (lane == 0) stg(ws + W_EPOCH, 0, ep); break; }
        __builtin_amdgcn_s_sleep(8);
      }
    }
    if (hh == 0 && s < 4) final_reduce(ws, out, xcd, s, lane, tag);
    return;
  }

  const int wave = role - 1, ct = wave & 3;
  const int bm = n16 & 1;
  const u32 tile_a = 128 * (8 * g + q4) + 16 * ((2 * ct + (p4 >> 1)) ^ (((q4 >> 1) | ((g & 1) << 1)) << 1)) + 8 * (p4 & 1);
  if (wave < 4) {
    const u32 xa = M_X + 1024 * bm + 256 * rq + 16 * g;
    bf16x8 xf[4];
#pragma unroll
    for (int ks = 0; ks < 4; ++ks) xf[ks] = ld_frag(xa + 64 * ks);
    lds_wait(C_LANDED, 20, landed);
    f32x4 acc = {0.f, 0.f, 0.f, 0.f};
#pragma unroll
    for (int ks = 0; ks < 4; ++ks) acc = mma(xf[ks], ld_tr(LB_W + tile_a + 4096 * ks, LB_W + tile_a + 4096 * ks + 512), acc);
    {
      const float n0 = dpp<XOR1>(acc[0]), n1 = dpp<XOR1>(acc[1]);
      if (lane < 16) stg(ws + W_Q1 + h * 512 + rq * 64 + 32 * par + 8 * ct, 256 * bm + (n16 >> 1), gpack(bm ? n1 : acc[0], bm ? acc[1] : n0, tag));
    }
    lds_arrive(C_QC);
    __builtin_amdgcn_s_sleep(QSLEEP);
    const u64* src = ws + W_Q1 + h * 512 + b * 256 + 16 * wave;
    u64 g0;
#pragma unroll 1
    for (int spin = 0; spin < SPIN_MAX; ++spin) {
      g0 = ldg(src, 64 * g + n16);
      if (!__any(gbad(g0, tag) != 0)) break;
    }
    const u32 qq = cvtpk(xrow_sum(gval0(g0)), xrow_sum(gval1(g0)));
    if (lane < 16) *lp<u32>(M_Q + 4 * (16 * wave + lane)) = qq;
    lds_arrive(C_Q);
  } else if (ffn) {
    const u32 xa = M_X + 1024 * bm + 512 * (s >> 2) + 32 * g, sel = (s & 2) ? 0x07060302u : 0x05040100u;
    u32x4 xf[4];
#pragma unroll
    for (int ks = 0; ks < 4; ++ks) {
      const u32x4 lo = *lp<u32x4>(xa + 128 * ks), hi = *lp<u32x4>(xa + 128 * ks + 16);
      xf[ks] = u32x4{__builtin_amdgcn_perm(lo[1], lo[0], sel), __builtin_amdgcn_perm(lo[3], lo[2], sel),
                     __builtin_amdgcn_perm(hi[1], hi[0], sel), __builtin_amdgcn_perm(hi[3], hi[2], sel)};
    }
    lds_wait(C_LANDED, 44, landed);
    f32x4 acc = {0.f, 0.f, 0.f, 0.f};
#pragma unroll
    for (int ks = 0; ks < 4; ++ks) acc = mma(__builtin_bit_cast(bf16x8, xf[ks]), ld_tr(LB_F + tile_a + 4096 * ks, LB_F + tile_a + 4096 * ks + 512), acc);
    if (lane < 16) stg(ws + W_F1 + ((s & 1) * 4 + (s & 2) + (s >> 2)) * F1_ROW + 64 * fg + 16 * ct, lane, gpack(acc[0], acc[1], tag));
    lds_arrive(C_FC);
  }
  if (wave == 0) asm volatile("" : "+v"(pf0));
  lds_wait(C_LANDED, 28 + 8 * wave + (wave >= 1 ? fo : 0u), landed);
  lds_wait(C_Q, 4);
  float sc[8];
  {
    const u32 ka = LB_K + 256 * (32 * wave + n16) + 16 * (g ^ n16);
    f32x4 d0 = {0.f, 0.f, 0.f, 0.f}, d1 = {0.f, 0.f, 0.f, 0.f};
#pragma unroll
    for (int ks = 0; ks < 4; ++ks) {
      const bf16x8 qf = ld_frag(M_Q + 64 * ks + 16 * g);
      d0 = mma(ld_frag(ka ^ (64 * ks)), qf, d0);
      d1 = mma(ld_frag((ka ^ (64 * ks)) + 4096), qf, d1);
    }
#pragma unroll
    for (int i = 0; i < 4; i += 2) {
      const u32 a = cvtpk(d0[i], d0[i + 1]), c = cvtpk(d1[i], d1[i + 1]);
      const u32 a2 = cvtpk(bflo(a) * 0.08838834764831845f, bfhi(a) * 0.08838834764831845f);
      const u32 c2 = cvtpk(bflo(c) * 0.08838834764831845f, bfhi(c) * 0.08838834764831845f);
      sc[i] = bflo(a2); sc[i + 1] = bfhi(a2); sc[4 + i] = bflo(c2); sc[5 + i] = bfhi(c2);
    }
  }
  {
    float m = xrow_max(max3(max3(sc[0], sc[1], sc[2]), max3(sc[3], sc[4], sc[5]), max3(sc[6], sc[7], sc[7])));
    if (lane == 0) *lp<float>(M_WM + 4 * wave) = m;
    lds_arrive(C_M);
    lds_wait(C_M, 8);
    const f32x4 a = *lp<f32x4>(M_WM), c = *lp<f32x4>(M_WM + 16);
    m = max3(max3(a[0], a[1], a[2]), max3(a[3], c[0], c[1]), max3(c[2], c[3], c[3]));
    float p[8], l = 0.f;
#pragma unroll
    for (int i = 0; i < 8; ++i) { p[i] = __builtin_amdgcn_exp2f((sc[i] - m) * LOG2E); l += p[i]; }
    l = xrow_sum(l);
    const u32x4 pf = {cvtpk(p[0], p[1]), cvtpk(p[2], p[3]), cvtpk(p[4], p[5]), cvtpk(p[6], p[7])};
    if (n16 == 0) *lp<u32x4>(M_P + 64 * wave + 16 * g) = pf;
    if (lane == 0) *lp<float>(M_WL + 4 * wave) = l;
  }
  lds_arrive(C_P);
  lds_wait(C_P, 8);
  const u64 a_o2 = gaddr(ws + W_O2 + ((h * 2 + b) * 4 + j) * 64 + 8 * wave, n16 >> 1);
  const u64 a_g2 = gaddr(ws + W_O2 + h * 512 + (wave & 1) * 256 + 16 * (wave >> 1), 64 * p4 + (lane >> 2));
  const u64 a_y3 = gaddr(ws + W_Y3 + ((s * 4 + ct) * 32 + h) * 16, n16);
  f32x4 oacc = {0.f, 0.f, 0.f, 0.f};
  {
    const u32 va = LB_V + 256 * (4 * g + q4) + 16 * ((2 * wave + (p4 >> 1)) ^ ((4 * (g & 1) + q4) << 1)) + 8 * (p4 & 1);
    bf16x8 pfr[8];
#pragma unroll
    for (int t = 0; t < 8; ++t) pfr[t] = ld_frag(M_P + 64 * t + 16 * g);
    lds_wait(C_LANDED, 92 + fo + 8 * 5, landed);
#pragma unroll
    for (int t = 0; t < 6; ++t) oacc = mma(pfr[t], ld_tr(va + 8192 * t, va + 8192 * t + 4096), oacc);
    lds_wait(C_LANDED, 92 + fo + 8 * 6, landed);
    oacc = mma(pfr[6], ld_tr(va + 8192 * 6, va + 8192 * 6 + 4096), oacc);
    lds_wait_hot(C_LANDED, 92 + fo + 8 * 7, landed);
    oacc = mma(pfr[7], ld_tr(va + 8192 * 7, va + 8192 * 7 + 4096), oacc);
  }
  {
    const float on = dpp<XOR1>(oacc[0]);
    if (lane < 16 && !(lane & 1)) stga(a_o2, gpack(oacc[0], on, tag));
  }
  lds_wait(C_ISS, 1);
  u64 gg = ldga(a_g2);
  bf16x8 wof[4];
  float yb0 = 0.f, yb1 = 0.f;
  f32x4 gates;
  if (wave < 4) {
    lds_wait(C_H, 1);
    gates = *lp<f32x4>(M_G);
    lds_wait(C_LANDED, 154 + fo, landed);
    f32x4 acc = {0.f, 0.f, 0.f, 0.f};
#pragma unroll
    for (int ks = 0; ks < 2; ++ks) acc = mma(ld_frag(M_H + 128 * bm + 64 * ks + 16 * g), ld_tr(LB_D + tile_a + 4096 * ks, LB_D + tile_a + 4096 * ks + 512), acc);
    yb0 = gates[1] * acc[0]; yb1 = gates[3] * acc[1];
    lds_wait_hot(C_LANDED, 170 + fo, landed);
#pragma unroll
    for (int ks = 0; ks < 4; ++ks) wof[ks] = ld_tr(LB_W + tile_a + 4096 * ks, LB_W + tile_a + 4096 * ks + 512);
  }
  lds_wait(C_F, 1);
  const float fq = *lp<float>(M_F + 16 * (wave & 1) + 4 * p4);
  if (__builtin_expect(__any(gbad(gg, tag) != 0), 0)) {
#pragma unroll 1
    for (int spin = 0; spin < SPIN_MAX; ++spin) {
      gg = ldga(a_g2);
      if (!__any(gbad(gg, tag) != 0)) break;
      __builtin_amdgcn_s_sleep(1);
    }
  }
  {
    float t0 = fq * gval0(gg), t1 = fq * gval1(gg);
    t0 += dpp<XOR1>(t0); t1 += dpp<XOR1>(t1); t0 += dpp<XOR2>(t0); t1 += dpp<XOR2>(t1);
    const u32 ob = cvtpk(t0, t1);
    if (p4 == 0) *lp<u32>(M_O + 256 * (wave & 1) + 64 * (wave >> 1) + lane) = ob;
  }
  WG_BARRIER("B1");
  if (wave < 4) {
    bf16x8 of[4];
#pragma unroll
    for (int ks = 0; ks < 4; ++ks) of[ks] = ld_frag(M_O + 256 * bm + 64 * ks + 16 * g);
    __builtin_amdgcn_sched_barrier(0);
    f32x4 acc = {0.f, 0.f, 0.f, 0.f};
#pragma unroll
    for (int ks = 0; ks < 4; ++ks) acc = mma(of[ks], wof[ks], acc);
    if (lane < 16) stga(a_y3, gpack(fmaf(gates[0], acc[0], yb0), fmaf(gates[2], acc[1], yb1), tag));
  }
}
}

extern "C" void kernel_launch(void* const* d_in, const int* in_sizes, int n_in, void* d_out, int out_size, void* d_ws, size_t ws_size,
                              hipStream_t stream) {
  fused_decode<<<NWG, NTHR, 0, stream>>>((const u16*)d_in[0], (const u16*)d_in[1], (const u16*)d_in[2], (const u16*)d_in[3],
                                         (const u16*)d_in[4], (const u16*)d_in[6], (const u16*)d_in[7], (const u16*)d_in[8],
                                         (const u16*)d_in[9], (u16*)d_out, (u64*)d_ws);
}
```

```cpp
#include <hip/hip_runtime.h>
#include <stdint.h>

typedef unsigned short u16;
typedef unsigned int u32;
typedef unsigned long long u64;
typedef u32 u32x4 __attribute__((ext_vector_type(4)));
typedef short s16x4 __attribute__((ext_vector_type(4)));
typedef short s16x8 __attribute__((ext_vector_type(8)));
typedef float f32x2 __attribute__((ext_vector_type(2)));
typedef float f32x4 __attribute__((ext_vector_type(4)));
typedef __bf16 bf16x2 __attribute__((ext_vector_type(2)));
typedef __bf16 bf16x8 __attribute__((ext_vector_type(8)));
#define GAS __attribute__((address_space(1)))
#define LAS __attribute__((address_space(3)))
typedef GAS u64 gu64;

namespace {
constexpr int NWG = 256, NTHR = 640;
constexpr int FF = 1376;
constexpr u32 W_EPOCH = 0;
constexpr u32 W_Q1 = 1024;
constexpr u32 F1_ROW = 1408;
constexpr u32 W_F1 = W_Q1 + 32 * 512;
constexpr u32 W_O2 = W_F1 + 8 * F1_ROW;
constexpr u32 W_ST = W_O2 + 32 * 512;
constexpr u32 W_Y3 = W_ST + 32 * 128;
constexpr u32 W_END = W_Y3 + 32 * 512;
constexpr u32 LB_M = 0;
constexpr u32 M_CNT = LB_M;
constexpr u32 M_TAG = M_CNT + 48;
constexpr u32 M_G = M_TAG + 16;
constexpr u32 M_WM = M_G + 16;
constexpr u32 M_WL = M_WM + 32;
constexpr u32 M_F = M_WL + 32;
constexpr u32 M_Q = 256;
constexpr u32 M_P = M_Q + 256;
constexpr u32 M_O = M_P + 512;
constexpr u32 M_H = M_O + 512;
constexpr u32 M_X = 2048;
constexpr u32 M_GW = M_X + 2048;
constexpr u32 LB_W = 8192;
constexpr u32 LB_D = LB_W + 16384;
constexpr u32 LB_K = LB_D + 8192;
constexpr u32 LB_V = LB_K + 65536;
constexpr u32 LB_F = LB_V + 49152;
constexpr u32 C_LANDED = M_CNT, C_F = M_CNT + 4, C_QC = M_CNT + 8, C_FC = M_CNT + 12, C_Q = M_CNT + 16, C_M = M_CNT + 20, C_P = M_CNT + 24, C_H = M_CNT + 28,
              C_ISS = M_CNT + 32;
constexpr u32 LDS_BYTES = 163840;
constexpr int SPIN_MAX = 1 << 15, LSPIN_MAX = 1 << 21;
constexpr int CODE_BYTES = 16256;
constexpr float LOG2E = 1.4426950408889634f;
#define WIN 28
constexpr u32 PACE = 220;
constexpr int QSLEEP = 36;
constexpr int FBLK = 6, STBLK = 4;

__device__ __forceinline__ u32 cvtpk(float lo, float hi) {
  const f32x2 v = {lo, hi};
  u32 r = __builtin_bit_cast(u32, __builtin_convertvector(v, bf16x2));
  asm("" : "+v"(r));
  return r;
}
__device__ __forceinline__ float max2(float a, float b) { return __builtin_amdgcn_fmed3f(a, b, __builtin_inff()); }
__device__ __forceinline__ float max3(float a, float b, float c) { return max2(max2(a, b), c); }
__device__ __forceinline__ float dot2(u32 a, u32 b, float c) {
  return __builtin_amdgcn_fdot2_f32_bf16(__builtin_bit_cast(bf16x2, a), __builtin_bit_cast(bf16x2, b), c, false);
}
__device__ __forceinline__ float bflo(u32 p) { return __uint_as_float(p << 16); }
__device__ __forceinline__ float bfhi(u32 p) { return __uint_as_float(p & 0xffff0000u); }
__device__ __forceinline__ float rbf(float x) { return bflo(cvtpk(x, x)); }
template <int CTRL> __device__ __forceinline__ float dpp(float x) {
  return __builtin_bit_cast(float, __builtin_amdgcn_mov_dpp(__builtin_bit_cast(int, x), CTRL, 0xf, 0xf, true));
}
constexpr int XOR1 = 0xB1, XOR2 = 0x4E, ROR8 = 0x128, ROR4 = 0x124, ROR2 = 0x122, ROR1 = 0x121;
__device__ __forceinline__ float swap32_add(float a, float b) {
  auto t = __builtin_amdgcn_permlane32_swap(__float_as_uint(a), __float_as_uint(b), false, false);
  return __uint_as_float(t[0]) + __uint_as_float(t[1]);
}
__device__ __forceinline__ float swap16_add(float a, float b) {
  auto t = __builtin_amdgcn_permlane16_swap(__float_as_uint(a), __float_as_uint(b), false, false);
  return __uint_as_float(t[0]) + __uint_as_float(t[1]);
}
__device__ __forceinline__ float xrow_max(float x) {
  auto s = __builtin_amdgcn_permlane16_swap(__float_as_uint(x), __float_as_uint(x), false, false);
  x = max2(__uint_as_float(s[0]), __uint_as_float(s[1]));
  auto t = __builtin_amdgcn_permlane32_swap(__float_as_uint(x), __float_as_uint(x), false, false);
  return max2(__uint_as_float(t[0]), __uint_as_float(t[1]));
}
__device__ __forceinline__ float xrow_sum(float x) { x = swap16_add(x, x); return swap32_add(x, x); }
__device__ __forceinline__ u64 ldg(const u64* base, u32 i) { return __hip_atomic_load((const gu64*)((const GAS char*)base + 8u * i), __ATOMIC_RELAXED, __HIP_MEMORY_SCOPE_AGENT); }
__device__ __forceinline__ void stg(u64* base, u32 i, u64 v) { __hip_atomic_store((gu64*)((GAS char*)base + 8u * i), v, __ATOMIC_RELAXED, __HIP_MEMORY_SCOPE_AGENT); }
__device__ __forceinline__ u64 gaddr(const u64* base, u32 i) { u64 a = (u64)(base + i); asm volatile("" : "+v"(a)); return a; }
__device__ __forceinline__ u64 ldga(u64 a) { return __hip_atomic_load((const gu64*)a, __ATOMIC_RELAXED, __HIP_MEMORY_SCOPE_AGENT); }
__device__ __forceinline__ void stga(u64 a, u64 v) { __hip_atomic_store((gu64*)a, v, __ATOMIC_RELAXED, __HIP_MEMORY_SCOPE_AGENT); }
struct Tag { u32 a, b; };
__device__ __forceinline__ u64 gpack(float v0, float v1, Tag t) {
  const u32 lo = ((__float_as_uint(v0) + 0x200u) & 0xfffffc00u) | t.a, hi = ((__float_as_uint(v1) + 0x200u) & 0xfffffc00u) | t.b;
  return ((u64)hi << 32) | lo;
}
__device__ __forceinline__ float gval0(u64 g) { return __uint_as_float((u32)g & 0xfffffc00u); }
__device__ __forceinline__ float gval1(u64 g) { return __uint_as_float((u32)(g >> 32) & 0xfffffc00u); }
__device__ __forceinline__ u32 gbad(u64 g, Tag t) { return (((u32)g ^ t.a) | ((u32)(g >> 32) ^ t.b)) & 0x3ffu; }

template <typename T> __device__ __forceinline__ LAS T* lp(u32 a) { return (LAS T*)(size_t)a; }
__device__ __forceinline__ bf16x8 ld_frag(u32 a) { return __builtin_bit_cast(bf16x8, *lp<u32x4>(a)); }
__device__ __forceinline__ bf16x8 ld_tr(u32 a0, u32 a1) {
  const s16x4 lo = __builtin_amdgcn_ds_read_tr16_b64_v4i16(lp<s16x4>(a0)), hi = __builtin_amdgcn_ds_read_tr16_b64_v4i16(lp<s16x4>(a1));
  return __builtin_bit_cast(bf16x8, __builtin_shufflevector(lo, hi, 0, 1, 2, 3, 4, 5, 6, 7));
}
__device__ __forceinline__ f32x4 mma(bf16x8 a, bf16x8 b, f32x4 c) { return __builtin_amdgcn_mfma_f32_16x16x32_bf16(a, b, c, 0, 0, 0); }
__device__ __forceinline__ void lds_arrive(u32 c) {
  asm volatile("s_waitcnt lgkmcnt(0)\n\ts_mov_b64 exec, 1\n\tds_add_u32 %0, %1\n\ts_mov_b64 exec, -1" :: "v"(c), "v"(1u) : "memory");
}
__device__ __forceinline__ void lds_wait(u32 c, u32 n, u32& seen) {
  u32 v, cnt = LSPIN_MAX;
  asm volatile("s_cmp_ge_u32 %1, %4\n\ts_cbranch_scc1 2f\n"
               "1:\n\tds_read_b32 %0, %3\n\ts_waitcnt lgkmcnt(0)\n\tv_readfirstlane_b32 %1, %0\n\ts_cmp_ge_u32 %1, %4\n\ts_cbranch_scc1 2f\n\t"
               "s_sleep 1\n\ts_sub_u32 %2, %2, 1\n\ts_cbranch_scc0 1b\n2:"
               : "=&v"(v), "+&s"(seen), "+&s"(cnt) : "v"(c), "s"(n) : "memory", "scc");
}
__device__ __forceinline__ void lds_wait(u32 c, u32 n) { u32 seen = 0; lds_wait(c, n, seen); }
__device__ __forceinline__ void lds_wait_hot(u32 c, u32 n, u32& seen) {
  u32 v, cnt = LSPIN_MAX;
  asm volatile("s_cmp_ge_u32 %1, %4\n\ts_cbranch_scc1 2f\n"
               "1:\n\tds_read_b32 %0, %3\n\ts_waitcnt lgkmcnt(0)\n\tv_readfirstlane_b32 %1, %0\n\ts_cmp_ge_u32 %1, %4\n\ts_cbranch_scc1 2f\n\t"
               "s_sub_u32 %2, %2, 1\n\ts_cbranch_scc0 1b\n2:"
               : "=&v"(v), "+&s"(seen), "+&s"(cnt) : "v"(c), "s"(n) : "memory", "scc");
}
#define WG_BARRIER(id) asm volatile("s_waitcnt lgkmcnt(0)\n\ts_barrier ; " id ::: "memory")
#define STR2(x) #x
#define STR(x) STR2(x)
#define DMA_PACE "2:\n\ts_memrealtime s[72:73]\n\ts_waitcnt lgkmcnt(0)\n\ts_lshl_b32 s72, s72, 4\n\ts_sub_u32 s72, s72, %[tn]\n\ts_cmp_lt_i32 s72, 0\n\ts_cbranch_scc1 2b\n\ts_add_u32 %[tn], %[tn], %[slot]\n\t"
#define DMA_THROTTLE0 "s_waitcnt vmcnt(%[win])\n\tds_write_b32 %[vz], %[prog]\n\t"
#define DMA_THROTTLE DMA_THROTTLE0 DMA_PACE
#define DMA_TILE_BODY(THR) \
  "s_mov_b32 m0, %[dst]\n\ts_nop 0\n1:\n\t" THR \
  "global_load_lds_dwordx4 %[v0], %[base] nt\n\ts_add_u32 m0, m0, 0x400\n\tv_add_u32 %[v0], %[step], %[v0]\n\t" \
  "global_load_lds_dwordx4 %[v1], %[base] nt\n\ts_add_u32 m0, m0, 0x400\n\tv_add_u32 %[v1], %[step], %[v1]\n\t" \
  "global_load_lds_dwordx4 %[v0], %[base] nt\n\ts_add_u32 m0, m0, 0x400\n\tv_add_u32 %[v0], %[step], %[v0]\n\t" \
  "global_load_lds_dwordx4 %[v1], %[base] nt\n\ts_add_u32 m0, m0, 0x400\n\tv_add_u32 %[v1], %[step], %[v1]\n\t" \
  "v_add_u32 %[prog], 4, %[prog]\n\ts_sub_u32 %[cnt], %[cnt], 1\n\ts_cbranch_scc0 1b"
template <int W>
__device__ __forceinline__ void dma_tile(u32 dst, const void* base, u32 v0, u32 v1, u32 step, u32 groups, u32& prog, u32& tn, u32 slot) {
  u32 cnt = groups - 1;
  if constexpr (W > 0)
    asm volatile(DMA_TILE_BODY(DMA_THROTTLE) : [v0] "+&v"(v0), [v1] "+&v"(v1), [prog] "+&v"(prog), [cnt] "+&s"(cnt), [tn] "+&s"(tn)
                 : [dst] "s"(dst), [base] "s"(base), [step] "s"(step), [vz] "v"(0u), [win] "n"(W), [slot] "s"(slot) : "memory", "scc", "s72", "s73");
  else
    asm volatile(DMA_TILE_BODY("") : [v0] "+&v"(v0), [v1] "+&v"(v1), [prog] "+&v"(prog), [cnt] "+&s"(cnt)
                 : [dst] "s"(dst), [base] "s"(base), [step] "s"(step) : "memory", "scc");
}
#define DMA_ROWS_BODY(THR) \
  "s_mov_b32 m0, %[dst]\n\ts_nop 0\n1:\n\t" THR \
  "global_load_lds_dwordx4 %[v0], %[base] nt\n\tglobal_load_lds_dwordx4 %[v1], %[base] offset:1024 nt\n\t" \
  "global_load_lds_dwordx4 %[v2], %[base] offset:2048 nt\n\tglobal_load_lds_dwordx4 %[v3], %[base] offset:3072 nt\n\t" \
  "s_add_u32 m0, m0, 0x1000\n\tv_add_u32 %[v0], 0x1000, %[v0]\n\tv_add_u32 %[v1], 0x1000, %[v1]\n\tv_add_u32 %[v2], 0x1000, %[v2]\n\tv_add_u32 %[v3], 0x1000, %[v3]\n\t" \
  "v_add_u32 %[prog], 4, %[prog]\n\ts_sub_u32 %[cnt], %[cnt], 1\n\ts_cbranch_scc0 1b"
template <int W>
__device__ __forceinline__ void dma_rows(u32 dst, const void* base, u32& v0, u32& v1, u32& v2, u32& v3, u32 groups, u32& prog, u32& tn, u32 slot) {
  u32 cnt = groups - 1;
  if constexpr (W > 0)
    asm volatile(DMA_ROWS_BODY(DMA_THROTTLE) : [v0] "+&v"(v0), [v1] "+&v"(v1), [v2] "+&v"(v2), [v3] "+&v"(v3), [prog] "+&v"(prog), [cnt] "+&s"(cnt), [tn] "+&s"(tn)
                 : [dst] "s"(dst), [base] "s"(base), [vz] "v"(0u), [win] "n"(W), [slot] "s"(slot) : "memory", "scc", "s72", "s73");
  else
    asm volatile(DMA_ROWS_BODY("") : [v0] "+&v"(v0), [v1] "+&v"(v1), [v2] "+&v"(v2), [v3] "+&v"(v3), [prog] "+&v"(prog), [cnt] "+&s"(cnt)
                 : [dst] "s"(dst), [base] "s"(base) : "memory", "scc");
}

__device__ __forceinline__ void final_reduce(const u64* ws, u16* out, int c, int qd, int lane, Tag tag) {
  const u64* src = ws + W_Y3 + (c * 4 + qd) * 512;
  u64 gg[8];
  __builtin_amdgcn_s_sleep(4);
#pragma unroll
  for (int i = 0; i < 8; ++i) gg[i] = ldg(src, 64 * i + lane);
#pragma unroll 1
  for (int spin = 0; spin < SPIN_MAX; ++spin) {
    u32 bad = 0;
#pragma unroll
    for (int i = 0; i < 8; ++i) bad |= gbad(gg[i], tag);
    if (!__any(bad != 0)) break;
#pragma unroll
    for (int i = 0; i < 8; ++i) gg[i] = ldg(src, 64 * i + lane);
  }
  const float v0 = ((gval0(gg[0]) + gval0(gg[1])) + (gval0(gg[2]) + gval0(gg[3]))) + ((gval0(gg[4]) + gval0(gg[5])) + (gval0(gg[6]) + gval0(gg[7])));
  const float v1 = ((gval1(gg[0]) + gval1(gg[1])) + (gval1(gg[2]) + gval1(gg[3]))) + ((gval1(gg[4]) + gval1(gg[5])) + (gval1(gg[6]) + gval1(gg[7])));
  float v = swap16_add(v0, v1);
  v = swap32_add(v, v);
  const u32 o2 = cvtpk(v, dpp<XOR1>(v));
  if (lane < 32 && !(lane & 1)) *(u32*)(out + (lane >> 4) * 512 + 64 * c + 16 * qd + (lane & 15)) = o2;
}

__global__ void __launch_bounds__(NTHR, 3)
fused_decode(const u16* __restrict__ x, const u16* __restrict__ Kc, const u16* __restrict__ Vc, const u16* __restrict__ wq,
             const u16* __restrict__ wo, const u16* __restrict__ wg, const u16* __restrict__ wu, const u16* __restrict__ wd,
             const u16* __restrict__ gw, u16* __restrict__ out, u64* __restrict__ ws) {
  __shared__ __attribute__((aligned(1024))) unsigned char lds_all[LDS_BYTES];
  asm volatile("" :: "v"((LAS unsigned char*)lds_all));
  asm volatile("" :: "s"(x), "s"(Kc), "s"(Vc), "s"(wq), "s"(wo), "s"(wg), "s"(wu), "s"(wd), "s"(gw), "s"(out), "s"(ws));
  const int tid = threadIdx.x, lane = tid & 63;
  const int wid = __builtin_amdgcn_readfirstlane(tid >> 6);
  const int wgid = blockIdx.x, xcd = wgid & 7, idx = wgid >> 3;
  const int hh = idx >> 3, s = idx & 7, h = xcd * 4 + hh;
  const int rq = s >> 1, par = s & 1;
  const int b = s & 1, j = s >> 1;
  const u32 r8 = lane >> 3, r4 = lane >> 4;
  const u32 sw0 = 16u * ((lane & 7) ^ (r8 & 2));
  const size_t kvoff = ((size_t)(b * 32 + h) * 4096 + 3072 + j * 256) * 256;
  u32 k0 = r4 * 256 + 16 * ((lane & 15) ^ r4), k1 = k0 ^ 64, k2 = k0 ^ 128, k3 = k0 ^ 192;
  u32 prog = 28 - WIN;
  if (wid == 0) {
    __builtin_amdgcn_s_setprio(2);
    if (lane < 12) *lp<u32>(M_CNT + 4 * lane) = 0;
    asm volatile("s_mov_b64 exec, 1\n\ts_mov_b32 m0, %0\n\ts_nop 0\n\tglobal_load_lds_dwordx4 %1, %2 sc1\n\ts_mov_b64 exec, -1\n\t"
                 "s_mov_b32 m0, %3\n\ts_nop 0\n\tglobal_load_lds_dwordx4 %4, %5\n\tglobal_load_lds_dwordx4 %4, %5 offset:1024\n\t"
                 "s_mov_b32 m0, %6\n\ts_nop 0\n\tglobal_load_lds_dwordx4 %4, %7"
                 :: "s"(M_TAG), "v"(0u), "s"(ws + W_EPOCH), "s"(M_X), "v"(16u * lane), "s"(x), "s"(M_GW), "s"(gw) : "memory");
    u32 dummy = 0, dummy2 = 0;
    dma_tile<0>(LB_W, (const char*)wq + ((size_t)rq << 20) + 256 * h + 128 * par, r8 * 8192 + sw0, r8 * 8192 + (sw0 ^ 64) + 65536, 131072, 4, dummy, dummy2, 0);
    dma_rows<0>(LB_K, (const char*)Kc + kvoff, k0, k1, k2, k3, 2, dummy, dummy2, 0);
  }
  u32 pf0;
  {
    u64 pc;
    asm volatile("s_getpc_b64 %0" : "=s"(pc));
    const u32 off = min((idx * 4 + (lane & 3)) * 128u, (u32)CODE_BYTES - 128u);
    pc &= ~127ull;
    if (tid >= 64 && tid < 68) asm volatile("global_load_dword %0, %1, %2" : "=v"(pf0) : "v"(off), "s"(pc) : "memory");
  }
  WG_BARRIER("B0");
  const int fgr = (int)((0x46573210u >> (4 * xcd)) & 15u) * 4 + hh;
  const bool ffn = fgr < 22;
  const int fg = ffn ? fgr : 0;
  const u32 fo = ffn ? 16u : 0u;
  int role = wid;
  asm volatile("" : "+s"(role));
  if (role == 0) {
    const u32 sw1 = sw0 ^ 64;
    u32 tn = 0;
    const u32 slot = PACE;
    { u64 now; asm volatile("s_memrealtime %0\n\ts_waitcnt lgkmcnt(0)" : "=s"(now)); tn = (u32)now << 4; }
    if (ffn)
      dma_tile<WIN>(LB_F, (const char*)((s & 1) ? wu : wg) + (size_t)(256 * (s >> 2) + ((s >> 1) & 1)) * (2 * FF) + 128 * fg - 64 * ((s >> 1) & 1),
                     r8 * (4 * FF) + sw0, r8 * (4 * FF) + sw1 + 8 * 4 * FF, 16 * 4 * FF, 4, prog, tn, slot);
    dma_rows<WIN>(LB_K + 8192, (const char*)Kc + kvoff, k0, k1, k2, k3, 14, prog, tn, slot);
    {
      u32 v0 = r4 * 256 + 16 * ((lane & 15) ^ (r4 << 1)), v1 = v0 ^ 128, v2 = v0, v3 = v1;
      dma_rows<WIN>(LB_V, (const char*)Vc + kvoff, v0, v1, v2, v3, 12, prog, tn, slot);
      if (ffn) lds_wait(C_FC, 4);
      dma_rows<WIN>(LB_V + 49152, (const char*)Vc + kvoff, v0, v1, v2, v3, 4, prog, tn, slot);
    }
    if (ffn) {
      const char* base = (const char*)(wd + (size_t)64 * fg * 512 + 64 * s);
      dma_tile<WIN>(LB_D, base, r8 * 1024 + sw0, r8 * 1024 + sw1 + 8192, 16384, 1, prog, tn, slot);
      dma_tile<WIN>(LB_D + 4096, base + (64 * fg + 32 < FF ? 32768 : 0), r8 * 1024 + sw0, r8 * 1024 + sw1 + 8192, 16384, 1, prog, tn, slot);
    }
    lds_wait(C_QC, 4);
    dma_tile<WIN>(LB_W, (const char*)(wo + (size_t)128 * h * 512 + 64 * s), r8 * 1024 + sw0, r8 * 1024 + sw1 + 8192, 16384, 4, prog, tn, slot);
    *(volatile LAS u32*)(size_t)C_ISS = 1;
#define DRAIN(k) "s_waitcnt vmcnt(" #k ")\n\tv_add_u32 %1, " STR(WIN) " - " #k ", %0\n\tds_write_b32 %2, %1\n\t"
    u32 t;
    asm volatile(DRAIN(28) DRAIN(24) DRAIN(22) DRAIN(20) DRAIN(16) DRAIN(12) DRAIN(8) DRAIN(4) DRAIN(0) "s_waitcnt lgkmcnt(0)" : "+v"(prog), "=&v"(t) : "v"(0u) : "memory");
    return;
  }
  const int g = lane >> 4, n16 = lane & 15, q4 = n16 >> 2, p4 = lane & 3;
  u32 landed = 0;
  lds_wait(C_LANDED, 4, landed);
  const u64 ep = *lp<u64>(M_TAG) + 1;
  const Tag tag = {(u32)ep & 0x3ffu, (u32)(ep >> 10) & 0x3ffu};

  if (role == 9) {
    {
      const u32x4 gv = *lp<u32x4>(M_GW + 16 * lane), x0 = *lp<u32x4>(M_X + 16 * lane), x1 = *lp<u32x4>(M_X + 1024 + 16 * lane);
      float z0 = 0.f, z1 = 0.f;
#pragma unroll
      for (int t = 0; t < 4; ++t) {
        const float gl = bflo(gv[t]), gh = bfhi(gv[t]);
        z0 = dot2(cvtpk(bflo(x0[t]) * gl, bfhi(x0[t]) * gh), 0x3f803f80u, z0);
        z1 = dot2(cvtpk(bflo(x1[t]) * gl, bfhi(x1[t]) * gh), 0x3f803f80u, z1);
      }
      float z = swap32_add(z0, z1);
      z = swap16_add(z, z);
      z += dpp<ROR8>(z); z += dpp<ROR4>(z); z += dpp<ROR2>(z); z += dpp<ROR1>(z);
      const float gt = rbf(__builtin_amdgcn_rcpf(1.f + __expf(-rbf(z))));
      if ((lane & 31) == 0) { *lp<float>(M_G + (lane >> 5) * 8) = gt; *lp<float>(M_G + (lane >> 5) * 8 + 4) = rbf(1.f - gt); }
    }
    const u32 fe = 64 * fg + lane, fo2 = min(fe + 32, F1_ROW - 1);
    u64 fp[8];
    if (ffn) {
      lds_wait(C_LANDED, 28 + fo + 8 * FBLK, landed);
#pragma unroll
      for (int i = 0; i < 8; ++i) fp[i] = ldg(ws + W_F1 + F1_ROW * i, (i & 2) ? fo2 : fe);
    }
    lds_wait(C_P, 8);
    {
      const f32x4 a = *lp<f32x4>(M_WM), c = *lp<f32x4>(M_WM + 16), la = *lp<f32x4>(M_WL), lc = *lp<f32x4>(M_WL + 16);
      const float m = max3(max3(a[0], a[1], a[2]), max3(a[3], c[0], c[1]), max3(c[2], c[3], c[3]));
      const float L = ((la[0] + la[1]) + (la[2] + la[3])) + ((lc[0] + lc[1]) + (lc[2] + lc[3]));
      stg(ws + W_ST + ((h * 2 + b) * 4 + j) * 16, n16, gpack(m, L, tag));
    }
    if (ffn) {
      u32 bad = 0;
#pragma unroll
      for (int i = 0; i < 8; ++i) bad |= gbad(fp[i], tag);
      if (__builtin_expect(__any(bad != 0), 0)) {
#pragma unroll 1
        for (int spin = 0; spin < SPIN_MAX; ++spin) {
          bad = 0;
#pragma unroll
          for (int i = 0; i < 8; ++i) fp[i] = ldg(ws + W_F1 + F1_ROW * i, (i & 2) ? fo2 : fe);
#pragma unroll
          for (int i = 0; i < 8; ++i) bad |= gbad(fp[i], tag);
          if (!__any(bad != 0)) break;
        }
      }
      const u32 gp = cvtpk((gval0(fp[0]) + gval0(fp[1])) + (gval0(fp[2]) + gval0(fp[3])), (gval1(fp[0]) + gval1(fp[1])) + (gval1(fp[2]) + gval1(fp[3])));
      const u32 up = cvtpk((gval0(fp[4]) + gval0(fp[5])) + (gval0(fp[6]) + gval0(fp[7])), (gval1(fp[4]) + gval1(fp[5])) + (gval1(fp[6]) + gval1(fp[7])));
      const u32 hp = fe < FF ? cvtpk(bflo(gp) * bflo(up), bfhi(gp) * bfhi(up)) : 0u;
      *lp<u16>(M_H + 2 * lane) = (u16)hp; *lp<u16>(M_H + 128 + 2 * lane) = (u16)(hp >> 16);
    }
    lds_arrive(C_H);
    lds_wait(C_LANDED, 92 + fo + 8 * STBLK, landed);
    {
      const u64* src = ws + W_ST + h * 128;
      u64 s0, s1;
#pragma unroll 1
      for (int spin = 0; spin < SPIN_MAX; ++spin) {
        s0 = ldg(src, lane); s1 = ldg(src, 64 + lane);
        if (!__any((gbad(s0, tag) | gbad(s1, tag)) != 0)) break;
      }
      const u64 sb = (lane & 8) ? s1 : s0;
      const float mj = gval0(sb);
      const float w = __builtin_amdgcn_exp2f((mj - xrow_max(mj)) * LOG2E);
      const float f = w * __builtin_amdgcn_rcpf(xrow_sum(w * gval1(sb)));
      if ((lane & 7) == 0) *lp<float>(M_F + 2 * (lane & 8) + 4 * g) = f;
      lds_arrive(C_F);
    }
    WG_BARRIER("B1 helper");
    if (wgid == 68) {
      const u32 i0 = (lane >> 3) * 512 + ((lane >> 1) & 3) * 64 + (lane & 1) * 32;
#pragma unroll 1
      for (int spin = 0; spin < SPIN_MAX; ++spin) {
        u32 bad = 0;
#pragma unroll
        for (int i = 0; i < 4; ++i) bad |= gbad(ldg(ws + W_Q1 + 4096 * i, i0), tag);
        if (!__any(bad != 0)) { if (lane == 0) stg(ws + W_EPOCH, 0, ep); break; }
        __builtin_amdgcn_s_sleep(8);
      }
    }
    if (hh == 0 && s < 4) final_reduce(ws, out, xcd, s, lane, tag);
    return;
  }

  const int wave = role - 1, ct = wave & 3;
  const int bm = n16 & 1;
  const u32 tile_a = 128 * (8 * g + q4) + 16 * ((2 * ct + (p4 >> 1)) ^ (((q4 >> 1) | ((g & 1) << 1)) << 1)) + 8 * (p4 & 1);
  if (wave < 4) {
    const u32 xa = M_X + 1024 * bm + 256 * rq + 16 * g;
    bf16x8 xf[4];
#pragma unroll
    for (int ks = 0; ks < 4; ++ks) xf[ks] = ld_frag(xa + 64 * ks);
    lds_wait(C_LANDED, 20, landed);
    f32x4 acc = {0.f, 0.f, 0.f, 0.f};
#pragma unroll
    for (int ks = 0; ks < 4; ++ks) acc = mma(xf[ks], ld_tr(LB_W + tile_a + 4096 * ks, LB_W + tile_a + 4096 * ks + 512), acc);
    {
      const float n0 = dpp<XOR1>(acc[0]), n1 = dpp<XOR1>(acc[1]);
      if (lane < 16) stg(ws + W_Q1 + h * 512 + rq * 64 + 32 * par + 8 * ct, 256 * bm + (n16 >> 1), gpack(bm ? n1 : acc[0], bm ? acc[1] : n0, tag));
    }
    lds_arrive(C_QC);
    __builtin_amdgcn_s_sleep(QSLEEP);
    const u64* src = ws + W_Q1 + h * 512 + b * 256 + 16 * wave;
    u64 g0;
#pragma unroll 1
    for (int spin = 0; spin < SPIN_MAX; ++spin) {
      g0 = ldg(src, 64 * g + n16);
      if (!__any(gbad(g0, tag) != 0)) break;
    }
    const u32 qq = cvtpk(xrow_sum(gval0(g0)), xrow_sum(gval1(g0)));
    if (lane < 16) *lp<u32>(M_Q + 4 * (16 * wave + lane)) = qq;
    lds_arrive(C_Q);
  } else if (ffn) {
    const u32 xa = M_X + 1024 * bm + 512 * (s >> 2) + 32 * g, sel = (s & 2) ? 0x07060302u : 0x05040100u;
    u32x4 xf[4];
#pragma unroll
    for (int ks = 0; ks < 4; ++ks) {
      const u32x4 lo = *lp<u32x4>(xa + 128 * ks), hi = *lp<u32x4>(xa + 128 * ks + 16);
      xf[ks] = u32x4{__builtin_amdgcn_perm(lo[1], lo[0], sel), __builtin_amdgcn_perm(lo[3], lo[2], sel),
                     __builtin_amdgcn_perm(hi[1], hi[0], sel), __builtin_amdgcn_perm(hi[3], hi[2], sel)};
    }
    lds_wait(C_LANDED, 44, landed);
    f32x4 acc = {0.f, 0.f, 0.f, 0.f};
#pragma unroll
    for (int ks = 0; ks < 4; ++ks) acc = mma(__builtin_bit_cast(bf16x8, xf[ks]), ld_tr(LB_F + tile_a + 4096 * ks, LB_F + tile_a + 4096 * ks + 512), acc);
    if (lane < 16) stg(ws + W_F1 + ((s & 1) * 4 + (s & 2) + (s >> 2)) * F1_ROW + 64 * fg + 16 * ct, lane, gpack(acc[0], acc[1], tag));
    lds_arrive(C_FC);
  }
  if (wave == 0) asm volatile("" : "+v"(pf0));
  lds_wait(C_LANDED, 28 + 8 * wave + (wave >= 1 ? fo : 0u), landed);
  lds_wait(C_Q, 4);
  float sc[8];
  {
    const u32 ka = LB_K + 256 * (32 * wave + n16) + 16 * (g ^ n16);
    f32x4 d0 = {0.f, 0.f, 0.f, 0.f}, d1 = {0.f, 0.f, 0.f, 0.f};
#pragma unroll
    for (int ks = 0; ks < 4; ++ks) {
      const bf16x8 qf = ld_frag(M_Q + 64 * ks + 16 * g);
      d0 = mma(ld_frag(ka ^ (64 * ks)), qf, d0);
      d1 = mma(ld_frag((ka ^ (64 * ks)) + 4096), qf, d1);
    }
#pragma unroll
    for (int i = 0; i < 4; i += 2) {
      const u32 a = cvtpk(d0[i], d0[i + 1]), c = cvtpk(d1[i], d1[i + 1]);
      const u32 a2 = cvtpk(bflo(a) * 0.08838834764831845f, bfhi(a) * 0.08838834764831845f);
      const u32 c2 = cvtpk(bflo(c) * 0.08838834764831845f, bfhi(c) * 0.08838834764831845f);
      sc[i] = bflo(a2); sc[i + 1] = bfhi(a2); sc[4 + i] = bflo(c2); sc[5 + i] = bfhi(c2);
    }
  }
  {
    float m = xrow_max(max3(max3(sc[0], sc[1], sc[2]), max3(sc[3], sc[4], sc[5]), max3(sc[6], sc[7], sc[7])));
    if (lane == 0) *lp<float>(M_WM + 4 * wave) = m;
    lds_arrive(C_M);
    lds_wait(C_M, 8);
    const f32x4 a = *lp<f32x4>(M_WM), c = *lp<f32x4>(M_WM + 16);
    m = max3(max3(a[0], a[1], a[2]), max3(a[3], c[0], c[1]), max3(c[2], c[3], c[3]));
    float p[8], l = 0.f;
#pragma unroll
    for (int i = 0; i < 8; ++i) { p[i] = __builtin_amdgcn_exp2f((sc[i] - m) * LOG2E); l += p[i]; }
    l = xrow_sum(l);
    const u32x4 pf = {cvtpk(p[0], p[1]), cvtpk(p[2], p[3]), cvtpk(p[4], p[5]), cvtpk(p[6], p[7])};
    if (n16 == 0) *lp<u32x4>(M_P + 64 * wave + 16 * g) = pf;
    if (lane == 0) *lp<float>(M_WL + 4 * wave) = l;
  }
  lds_arrive(C_P);
  lds_wait(C_P, 8);
  const u64 a_o2 = gaddr(ws + W_O2 + ((h * 2 + b) * 4 + j) * 64 + 8 * wave, n16 >> 1);
  const u64 a_g2 = gaddr(ws + W_O2 + h * 512 + (wave & 1) * 256 + 16 * (wave >> 1), 64 * p4 + (lane >> 2));
  const u64 a_y3 = gaddr(ws + W_Y3 + ((s * 4 + ct) * 32 + h) * 16, n16);
  f32x4 oacc = {0.f, 0.f, 0.f, 0.f};
  {
    const u32 va = LB_V + 256 * (4 * g + q4) + 16 * ((2 * wave + (p4 >> 1)) ^ ((4 * (g & 1) + q4) << 1)) + 8 * (p4 & 1);
    bf16x8 pfr[8];
#pragma unroll
    for (int t = 0; t < 8; ++t) pfr[t] = ld_frag(M_P + 64 * t + 16 * g);
    lds_wait(C_LANDED, 92 + fo + 8 * 5, landed);
#pragma unroll
    for (int t = 0; t < 6; ++t) oacc = mma(pfr[t], ld_tr(va + 8192 * t, va + 8192 * t + 4096), oacc);
    lds_wait(C_LANDED, 92 + fo + 8 * 6, landed);
    oacc = mma(pfr[6], ld_tr(va + 8192 * 6, va + 8192 * 6 + 4096), oacc);
    lds_wait_hot(C_LANDED, 92 + fo + 8 * 7, landed);
    oacc = mma(pfr[7], ld_tr(va + 8192 * 7, va + 8192 * 7 + 4096), oacc);
  }
  {
    const float on = dpp<XOR1>(oacc[0]);
    if (lane < 16 && !(lane & 1)) stga(a_o2, gpack(oacc[0], on, tag));
  }
  lds_wait(C_ISS, 1);
  u64 gg = ldga(a_g2);
  bf16x8 wof[4];
  float yb0 = 0.f, yb1 = 0.f;
  f32x4 gates;
  if (wave < 4) {
    lds_wait(C_H, 1);
    gates = *lp<f32x4>(M_G);
    if (ffn) {
      lds_wait(C_LANDED, 172, landed);
      f32x4 acc = {0.f, 0.f, 0.f, 0.f};
#pragma unroll
      for (int ks = 0; ks < 2; ++ks) acc = mma(ld_frag(M_H + 128 * bm + 64 * ks + 16 * g), ld_tr(LB_D + tile_a + 4096 * ks, LB_D + tile_a + 4096 * ks + 512), acc);
      yb0 = gates[1] * acc[0]; yb1 = gates[3] * acc[1];
    }
    lds_wait_hot(C_LANDED, ffn ? 188 : 164, landed);
#pragma unroll
    for (int ks = 0; ks < 4; ++ks) wof[ks] = ld_tr(LB_W + tile_a + 4096 * ks, LB_W + tile_a + 4096 * ks + 512);
  }
  lds_wait(C_F, 1);
  const float fq = *lp<float>(M_F + 16 * (wave & 1) + 4 * p4);
  if (__builtin_expect(__any(gbad(gg, tag) != 0), 0)) {
#pragma unroll 1
    for (int spin = 0; spin < SPIN_MAX; ++spin) {
      gg = ldga(a_g2);
      if (!__any(gbad(gg, tag) != 0)) break;
      __builtin_amdgcn_s_sleep(1);
    }
  }
  {
    float t0 = fq * gval0(gg), t1 = fq * gval1(gg);
    t0 += dpp<XOR1>(t0); t1 += dpp<XOR1>(t1); t0 += dpp<XOR2>(t0); t1 += dpp<XOR2>(t1);
    const u32 ob = cvtpk(t0, t1);
    if (p4 == 0) *lp<u32>(M_O + 256 * (wave & 1) + 64 * (wave >> 1) + lane) = ob;
  }
  WG_BARRIER("B1");
  if (wave < 4) {
    bf16x8 of[4];
#pragma unroll
    for (int ks = 0; ks < 4; ++ks) of[ks] = ld_frag(M_O + 256 * bm + 64 * ks + 16 * g);
    __builtin_amdgcn_sched_barrier(0);
    f32x4 acc = {0.f, 0.f, 0.f, 0.f};
#pragma unroll
    for (int ks = 0; ks < 4; ++ks) acc = mma(of[ks], wof[ks], acc);
    if (lane < 16) stga(a_y3, gpack(fmaf(gates[0], acc[0], yb0), fmaf(gates[2], acc[1], yb1), tag));
  }
}
}

extern "C" void kernel_launch(void* const* d_in, const int* in_sizes, int n_in, void* d_out, int out_size, void* d_ws, size_t ws_size,
                              hipStream_t stream) {
  fused_decode<<<NWG, NTHR, 0, stream>>>((const u16*)d_in[0], (const u16*)d_in[1], (const u16*)d_in[2], (const u16*)d_in[3],
                                         (const u16*)d_in[4], (const u16*)d_in[6], (const u16*)d_in[7], (const u16*)d_in[8],
                                         (const u16*)d_in[9], (u16*)d_out, (u64*)d_ws);
}
```

```cpp
#include <hip/hip_runtime.h>
#include <stdint.h>

typedef unsigned short u16;
typedef unsigned int u32;
typedef unsigned long long u64;
typedef u32 u32x4 __attribute__((ext_vector_type(4)));
typedef short s16x4 __attribute__((ext_vector_type(4)));
typedef short s16x8 __attribute__((ext_vector_type(8)));
typedef float f32x2 __attribute__((ext_vector_type(2)));
typedef float f32x4 __attribute__((ext_vector_type(4)));
typedef __bf16 bf16x2 __attribute__((ext_vector_type(2)));
typedef __bf16 bf16x8 __attribute__((ext_vector_type(8)));
#define GAS __attribute__((address_space(1)))
#define LAS __attribute__((address_space(3)))
typedef GAS u64 gu64;

namespace {
constexpr int NWG = 256, NTHR = 640;
constexpr int FF = 1376;
constexpr u32 W_EPOCH = 0;
constexpr u32 W_Q1 = 1024;
constexpr u32 F1_ROW = 1408;
constexpr u32 W_F1 = W_Q1 + 32 * 512;
constexpr u32 W_O2 = W_F1 + 8 * F1_ROW;
constexpr u32 W_ST = W_O2 + 32 * 512;
constexpr u32 W_Y3 = W_ST + 32 * 128;
constexpr u32 LB_M = 0;
constexpr u32 M_CNT = LB_M;
constexpr u32 M_TAG = M_CNT + 48;
constexpr u32 M_G = M_TAG + 16;
constexpr u32 M_WM = M_G + 16;
constexpr u32 M_WL = M_WM + 32;
constexpr u32 M_F = M_WL + 32;
constexpr u32 M_Q = 256;
constexpr u32 M_P = M_Q + 256;
constexpr u32 M_O = M_P + 512;
constexpr u32 M_H = M_O + 512;
constexpr u32 M_X = 2048;
constexpr u32 M_GW = M_X + 2048;
constexpr u32 LB_W = 8192;
constexpr u32 LB_D = LB_W + 16384;
constexpr u32 LB_K = LB_D + 8192;
constexpr u32 LB_V = LB_K + 65536;
constexpr u32 LB_F = LB_V + 49152;
constexpr u32 C_LANDED = M_CNT, C_F = M_CNT + 4, C_QC = M_CNT + 8, C_FC = M_CNT + 12, C_Q = M_CNT + 16, C_M = M_CNT + 20, C_P = M_CNT + 24, C_H = M_CNT + 28,
              C_ISS = M_CNT + 32;
constexpr u32 LDS_BYTES = 163840;
constexpr int SPIN_MAX = 1 << 15, LSPIN_MAX = 1 << 21;
constexpr int CODE_BYTES = 16256;
constexpr float LOG2E = 1.4426950408889634f;
#define WIN 28
constexpr u32 PACE = 225;
constexpr int QSLEEP = 36;
constexpr int FBLK = 6, STBLK = 4;

__device__ __forceinline__ u32 cvtpk(float lo, float hi) {
  const f32x2 v = {lo, hi};
  u32 r = __builtin_bit_cast(u32, __builtin_convertvector(v, bf16x2));
  asm("" : "+v"(r));
  return r;
}
__device__ __forceinline__ float max2(float a, float b) { return __builtin_amdgcn_fmed3f(a, b, __builtin_inff()); }
__device__ __forceinline__ float max3(float a, float b, float c) { return max2(max2(a, b), c); }
__device__ __forceinline__ float dot2(u32 a, u32 b, float c) {
  return __builtin_amdgcn_fdot2_f32_bf16(__builtin_bit_cast(bf16x2, a), __builtin_bit_cast(bf16x2, b), c, false);
}
__device__ __forceinline__ float bflo(u32 p) { return __uint_as_float(p << 16); }
__device__ __forceinline__ float bfhi(u32 p) { return __uint_as_float(p & 0xffff0000u); }
__device__ __forceinline__ float rbf(float x) { return bflo(cvtpk(x, x)); }
template <int CTRL> __device__ __forceinline__ float dpp(float x) {
  return __builtin_bit_cast(float, __builtin_amdgcn_mov_dpp(__builtin_bit_cast(int, x), CTRL, 0xf, 0xf, true));
}
constexpr int XOR1 = 0xB1, XOR2 = 0x4E, ROR8 = 0x128, ROR4 = 0x124, ROR2 = 0x122, ROR1 = 0x121;
__device__ __forceinline__ float swap32_add(float a, float b) {
  auto t = __builtin_amdgcn_permlane32_swap(__float_as_uint(a), __float_as_uint(b), false, false);
  return __uint_as_float(t[0]) + __uint_as_float(t[1]);
}
__device__ __forceinline__ float swap16_add(float a, float b) {
  auto t = __builtin_amdgcn_permlane16_swap(__float_as_uint(a), __float_as_uint(b), false, false);
  return __uint_as_float(t[0]) + __uint_as_float(t[1]);
}
__device__ __forceinline__ float xrow_max(float x) {
  auto s = __builtin_amdgcn_permlane16_swap(__float_as_uint(x), __float_as_uint(x), false, false);
  x = max2(__uint_as_float(s[0]), __uint_as_float(s[1]));
  auto t = __builtin_amdgcn_permlane32_swap(__float_as_uint(x), __float_as_uint(x), false, false);
  return max2(__uint_as_float(t[0]), __uint_as_float(t[1]));
}
__device__ __forceinline__ float xrow_sum(float x) { x = swap16_add(x, x); return swap32_add(x, x); }
__device__ __forceinline__ u64 ldg(const u64* base, u32 i) { return __hip_atomic_load((const gu64*)((const GAS char*)base + 8u * i), __ATOMIC_RELAXED, __HIP_MEMORY_SCOPE_AGENT); }
__device__ __forceinline__ void stg(u64* base, u32 i, u64 v) { __hip_atomic_store((gu64*)((GAS char*)base + 8u * i), v, __ATOMIC_RELAXED, __HIP_MEMORY_SCOPE_AGENT); }
__device__ __forceinline__ u64 gaddr(const u64* base, u32 i) { u64 a = (u64)(base + i); asm volatile("" : "+v"(a)); return a; }
__device__ __forceinline__ u64 ldga(u64 a) { return __hip_atomic_load((const gu64*)a, __ATOMIC_RELAXED, __HIP_MEMORY_SCOPE_AGENT); }
__device__ __forceinline__ void stga(u64 a, u64 v) { __hip_atomic_store((gu64*)a, v, __ATOMIC_RELAXED, __HIP_MEMORY_SCOPE_AGENT); }
struct Tag { u32 a, b; };
__device__ __forceinline__ u64 gpack(float v0, float v1, Tag t) {
  const u32 lo = ((__float_as_uint(v0) + 0x200u) & 0xfffffc00u) | t.a, hi = ((__float_as_uint(v1) + 0x200u) & 0xfffffc00u) | t.b;
  return ((u64)hi << 32) | lo;
}
__device__ __forceinline__ float gval0(u64 g) { return __uint_as_float((u32)g & 0xfffffc00u); }
__device__ __forceinline__ float gval1(u64 g) { return __uint_as_float((u32)(g >> 32) & 0xfffffc00u); }
__device__ __forceinline__ u32 gbad(u64 g, Tag t) { return (((u32)g ^ t.a) | ((u32)(g >> 32) ^ t.b)) & 0x3ffu; }

template <typename T> __device__ __forceinline__ LAS T* lp(u32 a) { return (LAS T*)(size_t)a; }
__device__ __forceinline__ bf16x8 ld_frag(u32 a) { return __builtin_bit_cast(bf16x8, *lp<u32x4>(a)); }
__device__ __forceinline__ bf16x8 ld_tr(u32 a0, u32 a1) {
  const s16x4 lo = __builtin_amdgcn_ds_read_tr16_b64_v4i16(lp<s16x4>(a0)), hi = __builtin_amdgcn_ds_read_tr16_b64_v4i16(lp<s16x4>(a1));
  return __builtin_bit_cast(bf16x8, __builtin_shufflevector(lo, hi, 0, 1, 2, 3, 4, 5, 6, 7));
}
__device__ __forceinline__ f32x4 mma(bf16x8 a, bf16x8 b, f32x4 c) { return __builtin_amdgcn_mfma_f32_16x16x32_bf16(a, b, c, 0, 0, 0); }
__device__ __forceinline__ void lds_arrive(u32 c) {
  asm volatile("s_waitcnt lgkmcnt(0)\n\ts_mov_b64 exec, 1\n\tds_add_u32 %0, %1\n\ts_mov_b64 exec, -1" :: "v"(c), "v"(1u) : "memory");
}
__device__ __forceinline__ void lds_wait(u32 c, u32 n, u32& seen) {
  u32 v, cnt = LSPIN_MAX;
  asm volatile("s_cmp_ge_u32 %1, %4\n\ts_cbranch_scc1 2f\n"
               "1:\n\tds_read_b32 %0, %3\n\ts_waitcnt lgkmcnt(0)\n\tv_readfirstlane_b32 %1, %0\n\ts_cmp_ge_u32 %1, %4\n\ts_cbranch_scc1 2f\n\t"
               "s_sleep 1\n\ts_sub_u32 %2, %2, 1\n\ts_cbranch_scc0 1b\n2:"
               : "=&v"(v), "+&s"(seen), "+&s"(cnt) : "v"(c), "s"(n) : "memory", "scc");
}
__device__ __forceinline__ void lds_wait(u32 c, u32 n) { u32 seen = 0; lds_wait(c, n, seen); }
__device__ __forceinline__ void lds_wait_hot(u32 c, u32 n, u32& seen) {
  u32 v, cnt = LSPIN_MAX;
  asm volatile("s_cmp_ge_u32 %1, %4\n\ts_cbranch_scc1 2f\n"
               "1:\n\tds_read_b32 %0, %3\n\ts_waitcnt lgkmcnt(0)\n\tv_readfirstlane_b32 %1, %0\n\ts_cmp_ge_u32 %1, %4\n\ts_cbranch_scc1 2f\n\t"
               "s_sub_u32 %2, %2, 1\n\ts_cbranch_scc0 1b\n2:"
               : "=&v"(v), "+&s"(seen), "+&s"(cnt) : "v"(c), "s"(n) : "memory", "scc");
}
#define WG_BARRIER(id) asm volatile("s_waitcnt lgkmcnt(0)\n\ts_barrier ; " id ::: "memory")
#define STR2(x) #x
#define STR(x) STR2(x)
#define DMA_PACE "2:\n\ts_memrealtime s[72:73]\n\ts_waitcnt lgkmcnt(0)\n\ts_lshl_b32 s72, s72, 4\n\ts_sub_u32 s72, s72, %[tn]\n\ts_cmp_lt_i32 s72, 0\n\ts_cbranch_scc1 2b\n\ts_add_u32 %[tn], %[tn], %[slot]\n\t"
#define DMA_THROTTLE0 "s_waitcnt vmcnt(%[win])\n\tds_write_b32 %[vz], %[prog]\n\t"
#define DMA_THROTTLE DMA_THROTTLE0 DMA_PACE
#define DMA_TILE_BODY(THR) \
  "s_mov_b32 m0, %[dst]\n\ts_nop 0\n1:\n\t" THR \
  "global_load_lds_dwordx4 %[v0], %[base] nt\n\ts_add_u32 m0, m0, 0x400\n\tv_add_u32 %[v0], %[step], %[v0]\n\t" \
  "global_load_lds_dwordx4 %[v1], %[base] nt\n\ts_add_u32 m0, m0, 0x400\n\tv_add_u32 %[v1], %[step], %[v1]\n\t" \
  "global_load_lds_dwordx4 %[v0], %[base] nt\n\ts_add_u32 m0, m0, 0x400\n\tv_add_u32 %[v0], %[step], %[v0]\n\t" \
  "global_load_lds_dwordx4 %[v1], %[base] nt\n\ts_add_u32 m0, m0, 0x400\n\tv_add_u32 %[v1], %[step], %[v1]\n\t" \
  "v_add_u32 %[prog], 4, %[prog]\n\ts_sub_u32 %[cnt], %[cnt], 1\n\ts_cbranch_scc0 1b"
template <int W>
__device__ __forceinline__ void dma_tile(u32 dst, const void* base, u32 v0, u32 v1, u32 step, u32 groups, u32& prog, u32& tn, u32 slot) {
  u32 cnt = groups - 1;
  if constexpr (W > 0)
    asm volatile(DMA_TILE_BODY(DMA_THROTTLE) : [v0] "+&v"(v0), [v1] "+&v"(v1), [prog] "+&v"(prog), [cnt] "+&s"(cnt), [tn] "+&s"(tn)
                 : [dst] "s"(dst), [base] "s"(base), [step] "s"(step), [vz] "v"(0u), [win] "n"(W), [slot] "s"(slot) : "memory", "scc", "s72", "s73");
  else
    asm volatile(DMA_TILE_BODY("") : [v0] "+&v"(v0), [v1] "+&v"(v1), [prog] "+&v"(prog), [cnt] "+&s"(cnt)
                 : [dst] "s"(dst), [base] "s"(base), [step] "s"(step) : "memory", "scc");
}
template <int W>
__device__ __forceinline__ void dma_tile128(u32 dst, const void* base, u32 lane_off, u32 pitch, u32 g0, u32& prog, u32& tn, u32 slot) {
  dma_tile<W>(dst + 4096 * g0, base, lane_off + 32 * pitch * g0, (lane_off ^ 64) + 8 * pitch + 32 * pitch * g0, 16 * pitch, 4 - g0, prog, tn, slot);
  if (g0) dma_tile<W>(dst, base, lane_off, (lane_off ^ 64) + 8 * pitch, 16 * pitch, g0, prog, tn, slot);
}
#define DMA_ROWS_BODY(THR) \
  "s_mov_b32 m0, %[dst]\n\ts_nop 0\n1:\n\t" THR \
  "global_load_lds_dwordx4 %[v0], %[base] nt\n\tglobal_load_lds_dwordx4 %[v1], %[base] offset:1024 nt\n\t" \
  "global_load_lds_dwordx4 %[v2], %[base] offset:2048 nt\n\tglobal_load_lds_dwordx4 %[v3], %[base] offset:3072 nt\n\t" \
  "s_add_u32 m0, m0, 0x1000\n\tv_add_u32 %[v0], 0x1000, %[v0]\n\tv_add_u32 %[v1], 0x1000, %[v1]\n\tv_add_u32 %[v2], 0x1000, %[v2]\n\tv_add_u32 %[v3], 0x1000, %[v3]\n\t" \
  "v_add_u32 %[prog], 4, %[prog]\n\ts_sub_u32 %[cnt], %[cnt], 1\n\ts_cbranch_scc0 1b"
template <int W>
__device__ __forceinline__ void dma_rows(u32 dst, const void* base, u32& v0, u32& v1, u32& v2, u32& v3, u32 groups, u32& prog, u32& tn, u32 slot) {
  u32 cnt = groups - 1;
  if constexpr (W > 0)
    asm volatile(DMA_ROWS_BODY(DMA_THROTTLE) : [v0] "+&v"(v0), [v1] "+&v"(v1), [v2] "+&v"(v2), [v3] "+&v"(v3), [prog] "+&v"(prog), [cnt] "+&s"(cnt), [tn] "+&s"(tn)
                 : [dst] "s"(dst), [base] "s"(base), [vz] "v"(0u), [win] "n"(W), [slot] "s"(slot) : "memory", "scc", "s72", "s73");
  else
    asm volatile(DMA_ROWS_BODY("") : [v0] "+&v"(v0), [v1] "+&v"(v1), [v2] "+&v"(v2), [v3] "+&v"(v3), [prog] "+&v"(prog), [cnt] "+&s"(cnt)
                 : [dst] "s"(dst), [base] "s"(base) : "memory", "scc");
}

__device__ __forceinline__ void final_reduce(const u64* ws, u16* out, int c, int qd, int lane, Tag tag) {
  const u64* src = ws + W_Y3 + (c * 4 + qd) * 512;
  u64 gg[8];
  __builtin_amdgcn_s_sleep(4);
#pragma unroll
  for (int i = 0; i < 8; ++i) gg[i] = ldg(src, 64 * i + lane);
#pragma unroll 1
  for (int spin = 0; spin < SPIN_MAX; ++spin) {
    u32 bad = 0;
#pragma unroll
    for (int i = 0; i < 8; ++i) bad |= gbad(gg[i], tag);
    if (!__any(bad != 0)) break;
#pragma unroll
    for (int i = 0; i < 8; ++i) gg[i] = ldg(src, 64 * i + lane);
  }
  const float v0 = ((gval0(gg[0]) + gval0(gg[1])) + (gval0(gg[2]) + gval0(gg[3]))) + ((gval0(gg[4]) + gval0(gg[5])) + (gval0(gg[6]) + gval0(gg[7])));
  const float v1 = ((gval1(gg[0]) + gval1(gg[1])) + (gval1(gg[2]) + gval1(gg[3]))) + ((gval1(gg[4]) + gval1(gg[5])) + (gval1(gg[6]) + gval1(gg[7])));
  float v = swap16_add(v0, v1);
  v = swap32_add(v, v);
  const u32 o2 = cvtpk(v, dpp<XOR1>(v));
  if (lane < 32 && !(lane & 1)) *(u32*)(out + (lane >> 4) * 512 + 64 * c + 16 * qd + (lane & 15)) = o2;
}

__global__ void __launch_bounds__(NTHR, 3)
fused_decode(const u16* __restrict__ x, const u16* __restrict__ Kc, const u16* __restrict__ Vc, const u16* __restrict__ wq,
             const u16* __restrict__ wo, const u16* __restrict__ wg, const u16* __restrict__ wu, const u16* __restrict__ wd,
             const u16* __restrict__ gw, u16* __restrict__ out, u64* __restrict__ ws) {
  __shared__ __attribute__((aligned(1024))) unsigned char lds_all[LDS_BYTES];
  asm volatile("" :: "v"((LAS unsigned char*)lds_all));
  asm volatile("" :: "s"(x), "s"(Kc), "s"(Vc), "s"(wq), "s"(wo), "s"(wg), "s"(wu), "s"(wd), "s"(gw), "s"(out), "s"(ws));
  const int tid = threadIdx.x, lane = tid & 63;
  const int wid = __builtin_amdgcn_readfirstlane(tid >> 6);
  const int wgid = blockIdx.x, xcd = wgid & 7, idx = wgid >> 3;
  const int hh = idx >> 3, s = idx & 7, h = xcd * 4 + hh;
  const int rq = s >> 1, par = s & 1;
  const int b = s & 1, j = s >> 1;
  const u32 r8 = lane >> 3, r4 = lane >> 4;
  const u32 sw0 = 16u * ((lane & 7) ^ (r8 & 2));
  const size_t kvoff = ((size_t)(b * 32 + h) * 4096 + 3072 + j * 256) * 256;
  const int rot = s;
  u32 k0 = r4 * 256 + 16 * ((lane & 15) ^ r4) + 8192 * rot, k1 = k0 ^ 64, k2 = k0 ^ 128, k3 = k0 ^ 192;
  u32 prog = 28 - WIN;
  if (wid == 0) {
    __builtin_amdgcn_s_setprio(2);
    if (lane < 12) *lp<u32>(M_CNT + 4 * lane) = 0;
    asm volatile("s_mov_b64 exec, 1\n\ts_mov_b32 m0, %0\n\ts_nop 0\n\tglobal_load_lds_dwordx4 %1, %2 sc1\n\ts_mov_b64 exec, -1\n\t"
                 "s_mov_b32 m0, %3\n\ts_nop 0\n\tglobal_load_lds_dwordx4 %4, %5\n\tglobal_load_lds_dwordx4 %4, %5 offset:1024\n\t"
                 "s_mov_b32 m0, %6\n\ts_nop 0\n\tglobal_load_lds_dwordx4 %4, %7"
                 :: "s"(M_TAG), "v"(0u), "s"(ws + W_EPOCH), "s"(M_X), "v"(16u * lane), "s"(x), "s"(M_GW), "s"(gw) : "memory");
    u32 dummy = 0, dummy2 = 0;
    dma_tile128<0>(LB_W, (const char*)wq + ((size_t)rq << 20) + 256 * h + 128 * par, r8 * 8192 + sw0, 8192, 0, dummy, dummy2, 0);
    dma_rows<0>(LB_K + 8192 * rot, (const char*)Kc + kvoff, k0, k1, k2, k3, 2, dummy, dummy2, 0);
  }
  u32 pf0;
  {
    u64 pc;
    asm volatile("s_getpc_b64 %0" : "=s"(pc));
    const u32 off = min((idx * 4 + (lane & 3)) * 128u, (u32)CODE_BYTES - 128u);
    pc &= ~127ull;
    if (tid >= 64 && tid < 68) asm volatile("global_load_dword %0, %1, %2" : "=v"(pf0) : "v"(off), "s"(pc) : "memory");
  }
  WG_BARRIER("B0");
  const int fgr = (int)((0x46573210u >> (4 * xcd)) & 15u) * 4 + hh;
  const bool ffn = fgr < 22;
  const int fg = ffn ? fgr : 0;
  const u32 fo = ffn ? 16u : 0u;
  int role = wid;
  asm volatile("" : "+s"(role));
  if (role == 0) {
    const u32 sw1 = sw0 ^ 64;
    u32 tn = 0;
    const u32 slot = PACE;
    { u64 now; asm volatile("s_memrealtime %0\n\ts_waitcnt lgkmcnt(0)" : "=s"(now)); tn = (u32)now << 4; }
    if (ffn)
      dma_tile128<WIN>(LB_F, (const char*)((s & 1) ? wu : wg) + (size_t)(256 * (s >> 2) + ((s >> 1) & 1)) * (2 * FF) + 128 * fg - 64 * ((s >> 1) & 1),
                       r8 * (4 * FF) + sw0, 4 * FF, h & 3, prog, tn, slot);
    if (rot < 7) dma_rows<WIN>(LB_K + 8192 * (rot + 1), (const char*)Kc + kvoff, k0, k1, k2, k3, 2 * (7 - rot), prog, tn, slot);
    if (rot > 0) {
      k0 -= 65536; k1 -= 65536; k2 -= 65536; k3 -= 65536;
      dma_rows<WIN>(LB_K, (const char*)Kc + kvoff, k0, k1, k2, k3, 2 * rot, prog, tn, slot);
    }
    {
      u32 v0 = r4 * 256 + 16 * ((lane & 15) ^ (r4 << 1)) + 8192 * rot, v1 = v0 ^ 128, v2 = v0, v3 = v1;
      if (ffn) lds_wait(C_FC, 4);
      dma_rows<WIN>(LB_V + 8192 * rot, (const char*)Vc + kvoff, v0, v1, v2, v3, 2 * (8 - rot), prog, tn, slot);
      if (rot > 0) {
        v0 -= 65536; v1 -= 65536; v2 -= 65536; v3 -= 65536;
        dma_rows<WIN>(LB_V, (const char*)Vc + kvoff, v0, v1, v2, v3, 2 * rot, prog, tn, slot);
      }
    }
    if (ffn) {
      const char* base = (const char*)(wd + (size_t)64 * fg * 512 + 64 * s);
      dma_tile<WIN>(LB_D, base, r8 * 1024 + sw0, r8 * 1024 + sw1 + 8192, 16384, 1, prog, tn, slot);
      dma_tile<WIN>(LB_D + 4096, base + (64 * fg + 32 < FF ? 32768 : 0), r8 * 1024 + sw0, r8 * 1024 + sw1 + 8192, 16384, 1, prog, tn, slot);
    }
    lds_wait(C_QC, 4);
    dma_tile128<WIN>(LB_W, (const char*)(wo + (size_t)128 * h * 512 + 64 * s), r8 * 1024 + sw0, 1024, (h + s) & 3, prog, tn, slot);
    *(volatile LAS u32*)(size_t)C_ISS = 1;
#define DRAIN(k) "s_waitcnt vmcnt(" #k ")\n\tv_add_u32 %1, " STR(WIN) " - " #k ", %0\n\tds_write_b32 %2, %1\n\t"
    u32 t;
    asm volatile(DRAIN(28) DRAIN(24) DRAIN(22) DRAIN(20) DRAIN(16) DRAIN(12) DRAIN(8) DRAIN(4) DRAIN(0) "s_waitcnt lgkmcnt(0)" : "+v"(prog), "=&v"(t) : "v"(0u) : "memory");
    return;
  }
  const int g = lane >> 4, n16 = lane & 15, q4 = n16 >> 2, p4 = lane & 3;
  u32 landed = 0;
  lds_wait(C_LANDED, 4, landed);
  const u64 ep = *lp<u64>(M_TAG) + 1;
  const Tag tag = {(u32)ep & 0x3ffu, (u32)(ep >> 10) & 0x3ffu};

  if (role == 9) {
    {
      const u32x4 gv = *lp<u32x4>(M_GW + 16 * lane), x0 = *lp<u32x4>(M_X + 16 * lane), x1 = *lp<u32x4>(M_X + 1024 + 16 * lane);
      float z0 = 0.f, z1 = 0.f;
#pragma unroll
      for (int t = 0; t < 4; ++t) {
        const float gl = bflo(gv[t]), gh = bfhi(gv[t]);
        z0 = dot2(cvtpk(bflo(x0[t]) * gl, bfhi(x0[t]) * gh), 0x3f803f80u, z0);
        z1 = dot2(cvtpk(bflo(x1[t]) * gl, bfhi(x1[t]) * gh), 0x3f803f80u, z1);
      }
      float z = swap32_add(z0, z1);
      z = swap16_add(z, z);
      z += dpp<ROR8>(z); z += dpp<ROR4>(z); z += dpp<ROR2>(z); z += dpp<ROR1>(z);
      const float gt = rbf(__builtin_amdgcn_rcpf(1.f + __expf(-rbf(z))));
      if ((lane & 31) == 0) { *lp<float>(M_G + (lane >> 5) * 8) = gt; *lp<float>(M_G + (lane >> 5) * 8 + 4) = rbf(1.f - gt); }
    }
    const u32 fe = 64 * fg + lane, fo2 = min(fe + 32, F1_ROW - 1);
    u64 fp[8];
    if (ffn) {
      lds_wait(C_LANDED, 28 + fo + 8 * FBLK, landed);
#pragma unroll
      for (int i = 0; i < 8; ++i) fp[i] = ldg(ws + W_F1 + F1_ROW * i, (i & 2) ? fo2 : fe);
    }
    lds_wait(C_P, 8);
    {
      const f32x4 a = *lp<f32x4>(M_WM), c = *lp<f32x4>(M_WM + 16), la = *lp<f32x4>(M_WL), lc = *lp<f32x4>(M_WL + 16);
      const float m = max3(max3(a[0], a[1], a[2]), max3(a[3], c[0], c[1]), max3(c[2], c[3], c[3]));
      const float L = ((la[0] + la[1]) + (la[2] + la[3])) + ((lc[0] + lc[1]) + (lc[2] + lc[3]));
      stg(ws + W_ST + ((h * 2 + b) * 4 + j) * 16, n16, gpack(m, L, tag));
    }
    if (ffn) {
      u32 bad = 0;
#pragma unroll
      for (int i = 0; i < 8; ++i) bad |= gbad(fp[i], tag);
      if (__builtin_expect(__any(bad != 0), 0)) {
#pragma unroll 1
        for (int spin = 0; spin < SPIN_MAX; ++spin) {
          bad = 0;
#pragma unroll
          for (int i = 0; i < 8; ++i) fp[i] = ldg(ws + W_F1 + F1_ROW * i, (i & 2) ? fo2 : fe);
#pragma unroll
          for (int i = 0; i < 8; ++i) bad |= gbad(fp[i], tag);
          if (!__any(bad != 0)) break;
        }
      }
      const u32 gp = cvtpk((gval0(fp[0]) + gval0(fp[1])) + (gval0(fp[2]) + gval0(fp[3])), (gval1(fp[0]) + gval1(fp[1])) + (gval1(fp[2]) + gval1(fp[3])));
      const u32 up = cvtpk((gval0(fp[4]) + gval0(fp[5])) + (gval0(fp[6]) + gval0(fp[7])), (gval1(fp[4]) + gval1(fp[5])) + (gval1(fp[6]) + gval1(fp[7])));
      const u32 hp = fe < FF ? cvtpk(bflo(gp) * bflo(up), bfhi(gp) * bfhi(up)) : 0u;
      *lp<u16>(M_H + 2 * lane) = (u16)hp; *lp<u16>(M_H + 128 + 2 * lane) = (u16)(hp >> 16);
    }
    lds_arrive(C_H);
    lds_wait(C_LANDED, 92 + fo + 8 * STBLK, landed);
    {
      const u64* src = ws + W_ST + h * 128;
      u64 s0, s1;
#pragma unroll 1
      for (int spin = 0; spin < SPIN_MAX; ++spin) {
        s0 = ldg(src, lane); s1 = ldg(src, 64 + lane);
        if (!__any((gbad(s0, tag) | gbad(s1, tag)) != 0)) break;
      }
      const u64 sb = (lane & 8) ? s1 : s0;
      const float mj = gval0(sb);
      const float w = __builtin_amdgcn_exp2f((mj - xrow_max(mj)) * LOG2E);
      const float f = w * __builtin_amdgcn_rcpf(xrow_sum(w * gval1(sb)));
      if ((lane & 7) == 0) *lp<float>(M_F + 2 * (lane & 8) + 4 * g) = f;
      lds_arrive(C_F);
    }
    WG_BARRIER("B1 helper");
    if (wgid == 68) {
      const u32 i0 = (lane >> 3) * 512 + ((lane >> 1) & 3) * 64 + (lane & 1) * 32;
#pragma unroll 1
      for (int spin = 0; spin < SPIN_MAX; ++spin) {
        u32 bad = 0;
#pragma unroll
        for (int i = 0; i < 4; ++i) bad |= gbad(ldg(ws + W_Q1 + 4096 * i, i0), tag);
        if (!__any(bad != 0)) { if (lane == 0) stg(ws + W_EPOCH, 0, ep); break; }
        __builtin_amdgcn_s_sleep(8);
      }
    }
    if (hh == 0 && s < 4) final_reduce(ws, out, xcd, s, lane, tag);
    return;
  }

  const int wave = role - 1, ct = wave & 3;
  const int bm = n16 & 1;
  const u32 tile_a = 128 * (8 * g + q4) + 16 * ((2 * ct + (p4 >> 1)) ^ (((q4 >> 1) | ((g & 1) << 1)) << 1)) + 8 * (p4 & 1);
  if (wave < 4) {
    const u32 xa = M_X + 1024 * bm + 256 * rq + 16 * g;
    bf16x8 xf[4];
#pragma unroll
    for (int ks = 0; ks < 4; ++ks) xf[ks] = ld_frag(xa + 64 * ks);
    lds_wait(C_LANDED, 20, landed);
    f32x4 acc = {0.f, 0.f, 0.f, 0.f};
#pragma unroll
    for (int ks = 0; ks < 4; ++ks) acc = mma(xf[ks], ld_tr(LB_W + tile_a + 4096 * ks, LB_W + tile_a + 4096 * ks + 512), acc);
    {
      const float n0 = dpp<XOR1>(acc[0]), n1 = dpp<XOR1>(acc[1]);
      if (lane < 16) stg(ws + W_Q1 + h * 512 + rq * 64 + 32 * par + 8 * ct, 256 * bm + (n16 >> 1), gpack(bm ? n1 : acc[0], bm ? acc[1] : n0, tag));
    }
    lds_arrive(C_QC);
    __builtin_amdgcn_s_sleep(QSLEEP);
    const u64* src = ws + W_Q1 + h * 512 + b * 256 + 16 * wave;
    u64 g0;
#pragma unroll 1
    for (int spin = 0; spin < SPIN_MAX; ++spin) {
      g0 = ldg(src, 64 * g + n16);
      if (!__any(gbad(g0, tag) != 0)) break;
    }
    const u32 qq = cvtpk(xrow_sum(gval0(g0)), xrow_sum(gval1(g0)));
    if (lane < 16) *lp<u32>(M_Q + 4 * (16 * wave + lane)) = qq;
    lds_arrive(C_Q);
  } else if (ffn) {
    const u32 xa = M_X + 1024 * bm + 512 * (s >> 2) + 32 * g, sel = (s & 2) ? 0x07060302u : 0x05040100u;
    u32x4 xf[4];
#pragma unroll
    for (int ks = 0; ks < 4; ++ks) {
      const u32x4 lo = *lp<u32x4>(xa + 128 * ks), hi = *lp<u32x4>(xa + 128 * ks + 16);
      xf[ks] = u32x4{__builtin_amdgcn_perm(lo[1], lo[0], sel), __builtin_amdgcn_perm(lo[3], lo[2], sel),
                     __builtin_amdgcn_perm(hi[1], hi[0], sel), __builtin_amdgcn_perm(hi[3], hi[2], sel)};
    }
    lds_wait(C_LANDED, 44, landed);
    f32x4 acc = {0.f, 0.f, 0.f, 0.f};
#pragma unroll
    for (int ks = 0; ks < 4; ++ks) acc = mma(__builtin_bit_cast(bf16x8, xf[ks]), ld_tr(LB_F + tile_a + 4096 * ks, LB_F + tile_a + 4096 * ks + 512), acc);
    if (lane < 16) stg(ws + W_F1 + ((s & 1) * 4 + (s & 2) + (s >> 2)) * F1_ROW + 64 * fg + 16 * ct, lane, gpack(acc[0], acc[1], tag));
    lds_arrive(C_FC);
  }
  if (wave == 0) asm volatile("" : "+v"(pf0));
  { const u32 pos = (wave - rot) & 7; lds_wait(C_LANDED, 28 + 8 * pos + (pos >= 1 ? fo : 0u), landed); }
  lds_wait(C_Q, 4);
  float sc[8];
  {
    const u32 ka = LB_K + 256 * (32 * wave + n16) + 16 * (g ^ n16);
    f32x4 d0 = {0.f, 0.f, 0.f, 0.f}, d1 = {0.f, 0.f, 0.f, 0.f};
#pragma unroll
    for (int ks = 0; ks < 4; ++ks) {
      const bf16x8 qf = ld_frag(M_Q + 64 * ks + 16 * g);
      d0 = mma(ld_frag(ka ^ (64 * ks)), qf, d0);
      d1 = mma(ld_frag((ka ^ (64 * ks)) + 4096), qf, d1);
    }
#pragma unroll
    for (int i = 0; i < 4; i += 2) {
      const u32 a = cvtpk(d0[i], d0[i + 1]), c = cvtpk(d1[i], d1[i + 1]);
      const u32 a2 = cvtpk(bflo(a) * 0.08838834764831845f, bfhi(a) * 0.08838834764831845f);
      const u32 c2 = cvtpk(bflo(c) * 0.08838834764831845f, bfhi(c) * 0.08838834764831845f);
      sc[i] = bflo(a2); sc[i + 1] = bfhi(a2); sc[4 + i] = bflo(c2); sc[5 + i] = bfhi(c2);
    }
  }
  {
    float m = xrow_max(max3(max3(sc[0], sc[1], sc[2]), max3(sc[3], sc[4], sc[5]), max3(sc[6], sc[7], sc[7])));
    if (lane == 0) *lp<float>(M_WM + 4 * wave) = m;
    lds_arrive(C_M);
    lds_wait(C_M, 8);
    const f32x4 a = *lp<f32x4>(M_WM), c = *lp<f32x4>(M_WM + 16);
    m = max3(max3(a[0], a[1], a[2]), max3(a[3], c[0], c[1]), max3(c[2], c[3], c[3]));
    float p[8], l = 0.f;
#pragma unroll
    for (int i = 0; i < 8; ++i) { p[i] = __builtin_amdgcn_exp2f((sc[i] - m) * LOG2E); l += p[i]; }
    l = xrow_sum(l);
    const u32x4 pf = {cvtpk(p[0], p[1]), cvtpk(p[2], p[3]), cvtpk(p[4], p[5]), cvtpk(p[6], p[7])};
    if (n16 == 0) *lp<u32x4>(M_P + 64 * wave + 16 * g) = pf;
    if (lane == 0) *lp<float>(M_WL + 4 * wave) = l;
  }
  lds_arrive(C_P);
  lds_wait(C_P, 8);
  const u64 a_o2 = gaddr(ws + W_O2 + ((h * 2 + b) * 4 + j) * 64 + 8 * wave, n16 >> 1);
  const u64 a_g2 = gaddr(ws + W_O2 + h * 512 + (wave & 1) * 256 + 16 * (wave >> 1), 64 * p4 + (lane >> 2));
  const u64 a_y3 = gaddr(ws + W_Y3 + ((s * 4 + ct) * 32 + h) * 16, n16);
  f32x4 oacc = {0.f, 0.f, 0.f, 0.f};
  {
    const u32 va = LB_V + 256 * (4 * g + q4) + 16 * ((2 * wave + (p4 >> 1)) ^ ((4 * (g & 1) + q4) << 1)) + 8 * (p4 & 1);
    bf16x8 pfr[8];
    u32 vk[8];
#pragma unroll
    for (int k = 0; k < 8; ++k) {
      const u32 t = (rot + k) & 7;
      pfr[k] = ld_frag(M_P + 64 * t + 16 * g);
      vk[k] = va + 8192 * t;
    }
    lds_wait(C_LANDED, 92 + fo + 8 * 5, landed);
#pragma unroll
    for (int k = 0; k < 6; ++k) oacc = mma(pfr[k], ld_tr(vk[k], vk[k] + 4096), oacc);
    lds_wait(C_LANDED, 92 + fo + 8 * 6, landed);
    oacc = mma(pfr[6], ld_tr(vk[6], vk[6] + 4096), oacc);
    lds_wait_hot(C_LANDED, 92 + fo + 8 * 7, landed);
    oacc = mma(pfr[7], ld_tr(vk[7], vk[7] + 4096), oacc);
  }
  {
    const float on = dpp<XOR1>(oacc[0]);
    if (lane < 16 && !(lane & 1)) stga(a_o2, gpack(oacc[0], on, tag));
  }
  lds_wait(C_ISS, 1);
  u64 gg = ldga(a_g2);
  bf16x8 wof[4];
  float yb0 = 0.f, yb1 = 0.f;
  f32x4 gates;
  if (wave < 4) {
    lds_wait(C_H, 1);
    gates = *lp<f32x4>(M_G);
    if (ffn) {
      lds_wait(C_LANDED, 172, landed);
      f32x4 acc = {0.f, 0.f, 0.f, 0.f};
#pragma unroll
      for (int ks = 0; ks < 2; ++ks) acc = mma(ld_frag(M_H + 128 * bm + 64 * ks + 16 * g), ld_tr(LB_D + tile_a + 4096 * ks, LB_D + tile_a + 4096 * ks + 512), acc);
      yb0 = gates[1] * acc[0]; yb1 = gates[3] * acc[1];
    }
    lds_wait_hot(C_LANDED, ffn ? 188 : 164, landed);
#pragma unroll
    for (int ks = 0; ks < 4; ++ks) wof[ks] = ld_tr(LB_W + tile_a + 4096 * ks, LB_W + tile_a + 4096 * ks + 512);
  }
  lds_wait(C_F, 1);
  const float fq = *lp<float>(M_F + 16 * (wave & 1) + 4 * p4);
  if (__builtin_expect(__any(gbad(gg, tag) != 0), 0)) {
#pragma unroll 1
    for (int spin = 0; spin < SPIN_MAX; ++spin) {
      gg = ldga(a_g2);
      if (!__any(gbad(gg, tag) != 0)) break;
      __builtin_amdgcn_s_sleep(1);
    }
  }
  {
    float t0 = fq * gval0(gg), t1 = fq * gval1(gg);
    t0 += dpp<XOR1>(t0); t1 += dpp<XOR1>(t1); t0 += dpp<XOR2>(t0); t1 += dpp<XOR2>(t1);
    const u32 ob = cvtpk(t0, t1);
    if (p4 == 0) *lp<u32>(M_O + 256 * (wave & 1) + 64 * (wave >> 1) + lane) = ob;
  }
  WG_BARRIER("B1");
  if (wave < 4) {
    bf16x8 of[4];
#pragma unroll
    for (int ks = 0; ks < 4; ++ks) of[ks] = ld_frag(M_O + 256 * bm + 64 * ks + 16 * g);
    __builtin_amdgcn_sched_barrier(0);
    f32x4 acc = {0.f, 0.f, 0.f, 0.f};
#pragma unroll
    for (int ks = 0; ks < 4; ++ks) acc = mma(of[ks], wof[ks], acc);
    if (lane < 16) stga(a_y3, gpack(fmaf(gates[0], acc[0], yb0), fmaf(gates[2], acc[1], yb1), tag));
  }
}
}

extern "C" void kernel_launch(void* const* d_in, const int* in_sizes, int n_in, void* d_out, int out_size, void* d_ws, size_t ws_size,
                              hipStream_t stream) {
  fused_decode<<<NWG, NTHR, 0, stream>>>((const u16*)d_in[0], (const u16*)d_in[1], (const u16*)d_in[2], (const u16*)d_in[3],
                                         (const u16*)d_in[4], (const u16*)d_in[6], (const u16*)d_in[7], (const u16*)d_in[8],
                                         (const u16*)d_in[9], (u16*)d_out, (u64*)d_ws);
}
```
